```python
import torch

def _softmax(x, axis=-1):
    return torch.softmax(x, dim=axis)
def _gelu(x):
    return torch.nn.functional.gelu(x, approximate="tanh")
def _silu(x):
    return torch.nn.functional.silu(x)
def _sigmoid(x):
    return torch.sigmoid(x)
def _layernorm(x, axis=-1, eps=1e-5):
    m = x.mean(dim=axis, keepdim=True)
    v = x.var(dim=axis, keepdim=True, unbiased=False)
    return (x - m) / torch.sqrt(v + eps)
def _scatter_add(tgt, idx, src, axis=0):
    return tgt.clone().index_add_(axis, idx, src)
def _segment_sum(src, seg, n):
    out = torch.zeros((n,) + src.shape[1:], dtype=src.dtype)
    return out.index_add_(0, seg, src)
def _pad(x, pw):
    flat = tuple(v for pair in reversed(pw) for v in pair)
    return torch.nn.functional.pad(x, flat)

kernel_name = 'standalone__gqa_softmax__near_ridge'

def setup_inputs():

    _w = (lambda a: (lambda t: t.to(torch.bfloat16) if t.is_floating_point() else t)(torch.as_tensor(a)))
    B, Hq, Hkv, Sq, Sk, D = 8, 32, 8, 112, 1024, 128
    return {
        "q": _w((lambda *s: torch.randn(*s, dtype=torch.bfloat16) * 0.8)(B, Hq, Sq, D)),
        "k": _w((lambda *s: torch.randn(*s, dtype=torch.bfloat16) * 0.8)(B, Hkv, Sk, D)),
        "v": _w((lambda *s: torch.randn(*s, dtype=torch.bfloat16) * 0.05)(B, Hkv, Sk, D)),
    }

def reference(q, k, v):
    B, Hq, Sq, D = q.shape
    Hkv = k.shape[1]
    rep = Hq // Hkv
    k = (lambda x, n, axis: torch.repeat_interleave(x, n, dim=axis))(k, rep, axis=1)
    v = (lambda x, n, axis: torch.repeat_interleave(x, n, dim=axis))(v, rep, axis=1)
    scale = D ** -0.5
    scores = torch.einsum("bhqd,bhkd->bhqk", q, k) * scale
    p = _softmax(scores, axis=-1)
    return torch.einsum("bhqk,bhkd->bhqd", p, v)
```

```cpp
#include <hip/hip_runtime.h>
#include <hip/hip_bf16.h>
typedef __bf16 bf16x8 __attribute__((ext_vector_type(8)));
typedef __bf16 bf16x4 __attribute__((ext_vector_type(4)));
typedef __bf16 bf16x2 __attribute__((ext_vector_type(2)));
typedef short s16x4 __attribute__((ext_vector_type(4)));
typedef float f32x16 __attribute__((ext_vector_type(16)));
typedef float f32x4 __attribute__((ext_vector_type(4)));
typedef float f32x2 __attribute__((ext_vector_type(2)));
typedef unsigned u32x4 __attribute__((ext_vector_type(4)));
typedef unsigned u32x2 __attribute__((ext_vector_type(2)));
constexpr int B = 8, HQ = 32, HKV = 8, SQ = 112, SK = 1024, D = 128;
#define DI __device__ __forceinline__
DI unsigned cvtpk(float lo, float hi) { f32x2 v = {lo, hi}; bf16x2 b = __builtin_convertvector(v, bf16x2); return __builtin_bit_cast(unsigned, b); }
DI unsigned offK(unsigned row, unsigned ch) { return 2048u * (row >> 3) + 512u * (ch >> 2) + 64u * (row & 7) + 16u * ((ch & 3) ^ ((row >> 2) & 3)); }
DI unsigned offV(unsigned row, unsigned ch) { return 2048u * (row >> 3) + 512u * (ch >> 2) + 64u * (row & 7) + 16u * (ch & 3); }

__global__ __launch_bounds__(256) void attn_v0(const unsigned short* __restrict__ Q, const unsigned short* __restrict__ K, const unsigned short* __restrict__ V, unsigned short* __restrict__ O) {
  __shared__ __attribute__((aligned(16))) char lds[32768];
  const int tid = threadIdx.x, lane = tid & 63, w = tid >> 6, r32 = lane & 31, h = lane >> 5;
  const int bid = blockIdx.x, xcd = bid & 7, idx = bid >> 3;
  const int kvg = xcd * 8 + (idx >> 2), g = idx & 3;
  const int b = kvg >> 3, kvh = kvg & 7, hq = kvh * 4 + g;
  const unsigned short* Qh = Q + (size_t)(b * HQ + hq) * SQ * D;
  const unsigned short* Kh = K + (size_t)kvg * SK * D;
  const unsigned short* Vh = V + (size_t)kvg * SK * D;
  unsigned short* Oh = O + (size_t)(b * HQ + hq) * SQ * D;
  const int row = w * 32 + r32, rowc = row < SQ ? row : SQ - 1;
  const float C = 0.08838834764831845f * 1.4426950408889634f;
  bf16x8 qf[8];
  for (int s = 0; s < 8; ++s) qf[s] = *(const bf16x8*)(Qh + rowc * D + 16 * s + 8 * h);
  f32x16 o[4] = {};
  float m = -1e30f, l = 0.f;
  char* Kl = lds; char* Vl = lds + 16384;
  const unsigned kb0 = 2048u * (r32 >> 3) + 64u * (r32 & 7) + 16u * (h ^ ((r32 >> 2) & 3));
  const unsigned vb = 64u * (4 * h + ((lane & 15) >> 2)) + 32u * ((lane >> 4) & 1) + 8u * (lane & 3);
  for (int t = 0; t < 16; ++t) {
    __syncthreads();
    for (int i = 0; i < 4; ++i) {
      const int n = tid + 256 * i, rr = n >> 4, ch = n & 15;
      *(u32x4*)(Kl + offK(rr, ch)) = *(const u32x4*)(Kh + (t * 64 + rr) * D + ch * 8);
      *(u32x4*)(Vl + offV(rr, ch)) = *(const u32x4*)(Vh + (t * 64 + rr) * D + ch * 8);
    }
    __syncthreads();
    f32x16 s0 = {}, s1 = {};
#pragma unroll
    for (int s = 0; s < 8; ++s) {
      const unsigned a = (kb0 ^ (32u * (s & 1))) + 512u * (s >> 1);
      bf16x8 k0 = *(const bf16x8*)(Kl + a), k1 = *(const bf16x8*)(Kl + a + 8192);
      s0 = __builtin_amdgcn_mfma_f32_32x32x16_bf16(k0, qf[s], s0, 0, 0, 0);
      s1 = __builtin_amdgcn_mfma_f32_32x32x16_bf16(k1, qf[s], s1, 0, 0, 0);
    }
    float pm = s0[0];
#pragma unroll
    for (int r = 1; r < 16; ++r) pm = fmaxf(pm, s0[r]);
#pragma unroll
    for (int r = 0; r < 16; ++r) pm = fmaxf(pm, s1[r]);
    { auto rr = __builtin_amdgcn_permlane32_swap(__float_as_uint(pm), __float_as_uint(pm), false, false); pm = fmaxf(__uint_as_float(rr[0]), __uint_as_float(rr[1])); }
    const float mn = fmaxf(m, pm), alpha = __builtin_amdgcn_exp2f((m - mn) * C);
    m = mn;
    float ps = 0.f;
#pragma unroll
    for (int r = 0; r < 16; ++r) { s0[r] = __builtin_amdgcn_exp2f((s0[r] - mn) * C); s1[r] = __builtin_amdgcn_exp2f((s1[r] - mn) * C); ps += s0[r] + s1[r]; }
    { auto rr = __builtin_amdgcn_permlane32_swap(__float_as_uint(ps), __float_as_uint(ps), false, false); ps = __uint_as_float(rr[0]) + __uint_as_float(rr[1]); }
    l = l * alpha + ps;
#pragma unroll
    for (int d = 0; d < 4; ++d)
#pragma unroll
      for (int r = 0; r < 16; ++r) o[d][r] *= alpha;
    bf16x8 p[4];
#pragma unroll
    for (int ks = 0; ks < 4; ++ks) {
      const f32x16& s = ks < 2 ? s0 : s1; const int o8 = 8 * (ks & 1);
      u32x4 wv = {cvtpk(s[o8], s[o8 + 1]), cvtpk(s[o8 + 2], s[o8 + 3]), cvtpk(s[o8 + 4], s[o8 + 5]), cvtpk(s[o8 + 6], s[o8 + 7])};
      p[ks] = __builtin_bit_cast(bf16x8, wv);
    }
#pragma unroll
    for (int ks = 0; ks < 4; ++ks)
#pragma unroll
      for (int d = 0; d < 4; ++d) {
        const unsigned a = vb + 4096u * ks + 512u * d;
        bf16x4 lo = __builtin_amdgcn_ds_read_tr16_b64_v4bf16((__attribute__((address_space(3))) bf16x4*)(Vl + a));
        bf16x4 hi = __builtin_amdgcn_ds_read_tr16_b64_v4bf16((__attribute__((address_space(3))) bf16x4*)(Vl + a + 2048));
        bf16x8 vf = __builtin_shufflevector(lo, hi, 0, 1, 2, 3, 4, 5, 6, 7);
        o[d] = __builtin_amdgcn_mfma_f32_32x32x16_bf16(vf, p[ks], o[d], 0, 0, 0);
      }
  }
  const float il = 1.f / l;
  if (row < SQ) {
#pragma unroll
    for (int d = 0; d < 4; ++d)
#pragma unroll
      for (int gq = 0; gq < 4; ++gq) {
        u32x2 wv = {cvtpk(o[d][4 * gq] * il, o[d][4 * gq + 1] * il), cvtpk(o[d][4 * gq + 2] * il, o[d][4 * gq + 3] * il)};
        *(u32x2*)(Oh + row * D + 32 * d + 8 * gq + 4 * h) = wv;
      }
  }
}

extern "C" void kernel_launch(void* const* d_in, const int* in_sizes, int n_in, void* d_out, int out_size, void* d_ws, size_t ws_size, hipStream_t stream) {
  attn_v0<<<256, 256, 0, stream>>>((const unsigned short*)d_in[0], (const unsigned short*)d_in[1], (const unsigned short*)d_in[2], (unsigned short*)d_out);
}
```

```cpp
#include <hip/hip_runtime.h>
#include <hip/hip_bf16.h>
typedef __bf16 bf16x8 __attribute__((ext_vector_type(8)));
typedef __bf16 bf16x4 __attribute__((ext_vector_type(4)));
typedef __bf16 bf16x2 __attribute__((ext_vector_type(2)));
typedef short s16x4 __attribute__((ext_vector_type(4)));
typedef float f32x16 __attribute__((ext_vector_type(16)));
typedef float f32x4 __attribute__((ext_vector_type(4)));
typedef float f32x2 __attribute__((ext_vector_type(2)));
typedef unsigned u32x4 __attribute__((ext_vector_type(4)));
typedef unsigned u32x2 __attribute__((ext_vector_type(2)));
constexpr int B = 8, HQ = 32, HKV = 8, SQ = 112, SK = 1024, D = 128;
#define DI __device__ __forceinline__
DI unsigned cvtpk(float lo, float hi) { f32x2 v = {lo, hi}; bf16x2 b = __builtin_convertvector(v, bf16x2); return __builtin_bit_cast(unsigned, b); }
DI unsigned offK(unsigned row, unsigned ch) { return 2048u * (row >> 3) + 512u * (ch >> 2) + 64u * (row & 7) + 16u * ((ch & 3) ^ ((row >> 2) & 3)); }
DI unsigned offV(unsigned row, unsigned ch) { return 2048u * (row >> 3) + 512u * (ch >> 2) + 64u * (row & 7) + 16u * (ch & 3); }

__global__ __launch_bounds__(256) void attn_v0(const unsigned short* __restrict__ Q, const unsigned short* __restrict__ K, const unsigned short* __restrict__ V, unsigned short* __restrict__ O) {
  __shared__ __attribute__((aligned(16))) char lds[32768];
  const int tid = threadIdx.x, lane = tid & 63, w = tid >> 6, r32 = lane & 31, h = lane >> 5;
  const int bid = blockIdx.x, xcd = bid & 7, idx = bid >> 3;
  const int kvg = xcd * 8 + (idx >> 2), g = idx & 3;
  const int b = kvg >> 3, kvh = kvg & 7, hq = kvh * 4 + g;
  const unsigned short* Qh = Q + (size_t)(b * HQ + hq) * SQ * D;
  const unsigned short* Kh = K + (size_t)kvg * SK * D;
  const unsigned short* Vh = V + (size_t)kvg * SK * D;
  unsigned short* Oh = O + (size_t)(b * HQ + hq) * SQ * D;
  const int row = w * 32 + r32, rowc = row < SQ ? row : SQ - 1;
  const float C = 0.08838834764831845f * 1.4426950408889634f;
  bf16x8 qf[8];
  for (int s = 0; s < 8; ++s) qf[s] = *(const bf16x8*)(Qh + rowc * D + 16 * s + 8 * h);
  f32x16 o[4] = {};
  float m = -1e30f, l = 0.f;
  char* Kl = lds; char* Vl = lds + 16384;
  const unsigned kb0 = 2048u * (r32 >> 3) + 64u * (r32 & 7) + 16u * (h ^ ((r32 >> 2) & 3));
  const unsigned vb = 64u * (4 * h + ((lane & 15) >> 2)) + 32u * ((lane >> 4) & 1) + 8u * (lane & 3);
  for (int t = 0; t < 16; ++t) {
    __syncthreads();
    for (int i = 0; i < 4; ++i) {
      const int n = tid + 256 * i, rr = n >> 4, ch = n & 15;
      *(u32x4*)(Kl + offK(rr, ch)) = *(const u32x4*)(Kh + (t * 64 + rr) * D + ch * 8);
      *(u32x4*)(Vl + offV(rr, ch)) = *(const u32x4*)(Vh + (t * 64 + rr) * D + ch * 8);
    }
    __syncthreads();
    f32x16 s0 = {}, s1 = {};
#pragma unroll
    for (int s = 0; s < 8; ++s) {
      const unsigned a = (kb0 ^ (32u * (s & 1))) + 512u * (s >> 1);
      bf16x8 k0 = *(const bf16x8*)(Kl + a), k1 = *(const bf16x8*)(Kl + a + 8192);
      s0 = __builtin_amdgcn_mfma_f32_32x32x16_bf16(k0, qf[s], s0, 0, 0, 0);
      s1 = __builtin_amdgcn_mfma_f32_32x32x16_bf16(k1, qf[s], s1, 0, 0, 0);
    }
    float pm = s0[0];
#pragma unroll
    for (int r = 1; r < 16; ++r) pm = fmaxf(pm, s0[r]);
#pragma unroll
    for (int r = 0; r < 16; ++r) pm = fmaxf(pm, s1[r]);
    { auto rr = __builtin_amdgcn_permlane32_swap(__float_as_uint(pm), __float_as_uint(pm), false, false); pm = fmaxf(__uint_as_float(rr[0]), __uint_as_float(rr[1])); }
    const float mn = fmaxf(m, pm), alpha = __builtin_amdgcn_exp2f((m - mn) * C);
    m = mn;
    float ps = 0.f;
#pragma unroll
    for (int r = 0; r < 16; ++r) { s0[r] = __builtin_amdgcn_exp2f((s0[r] - mn) * C); s1[r] = __builtin_amdgcn_exp2f((s1[r] - mn) * C); ps += s0[r] + s1[r]; }
    { auto rr = __builtin_amdgcn_permlane32_swap(__float_as_uint(ps), __float_as_uint(ps), false, false); ps = __uint_as_float(rr[0]) + __uint_as_float(rr[1]); }
    l = l * alpha + ps;
#pragma unroll
    for (int d = 0; d < 4; ++d)
#pragma unroll
      for (int r = 0; r < 16; ++r) o[d][r] *= alpha;
    bf16x8 p[4];
#pragma unroll
    for (int ks = 0; ks < 4; ++ks) {
      const f32x16& s = ks < 2 ? s0 : s1; const int o8 = 8 * (ks & 1);
      u32x4 wv = {cvtpk(s[o8], s[o8 + 1]), cvtpk(s[o8 + 2], s[o8 + 3]), cvtpk(s[o8 + 4], s[o8 + 5]), cvtpk(s[o8 + 6], s[o8 + 7])};
      p[ks] = __builtin_bit_cast(bf16x8, wv);
    }
#pragma unroll
    for (int ks = 0; ks < 4; ++ks)
#pragma unroll
      for (int d = 0; d < 4; ++d) {
        const unsigned a = vb + 4096u * ks + 512u * d;
        bf16x4 lo = __builtin_amdgcn_ds_read_tr16_b64_v4bf16((__attribute__((address_space(3))) bf16x4*)(Vl + a));
        bf16x4 hi = __builtin_amdgcn_ds_read_tr16_b64_v4bf16((__attribute__((address_space(3))) bf16x4*)(Vl + a + 2048));
        bf16x8 vf = __builtin_shufflevector(lo, hi, 0, 1, 2, 3, 4, 5, 6, 7);
        o[d] = __builtin_amdgcn_mfma_f32_32x32x16_bf16(vf, p[ks], o[d], 0, 0, 0);
      }
  }
  const float il = 1.f / l;
  if (row < SQ) {
#pragma unroll
    for (int d = 0; d < 4; ++d)
#pragma unroll
      for (int gq = 0; gq < 4; ++gq) {
        u32x2 wv = {cvtpk(o[d][4 * gq] * il, o[d][4 * gq + 1] * il), cvtpk(o[d][4 * gq + 2] * il, o[d][4 * gq + 3] * il)};
        *(u32x2*)(Oh + row * D + 32 * d + 8 * gq + 4 * h) = wv;
      }
  }
}

__global__ __launch_bounds__(256) void probe(const u32x4* q, const u32x4* k, const u32x4* v, unsigned* ws) {
  const unsigned n = blockIdx.x * 256 + threadIdx.x, T = 2048 * 256;
  u32x4 a = {0, 0, 0, 0};
  u32x4 x[5];
  x[0] = q[n < 458752 ? n : 0]; x[1] = k[n]; x[2] = k[n + T]; x[3] = v[n]; x[4] = v[n + T];
  for (int i = 0; i < 5; ++i) a ^= x[i];
  if ((a[0] ^ a[1] ^ a[2] ^ a[3]) == 0x12345678u) ws[n] = 1;
}
extern "C" void kernel_launch(void* const* d_in, const int* in_sizes, int n_in, void* d_out, int out_size, void* d_ws, size_t ws_size, hipStream_t stream) {
  probe<<<2048, 256, 0, stream>>>((const u32x4*)d_in[0], (const u32x4*)d_in[1], (const u32x4*)d_in[2], (unsigned*)d_ws);
  attn_v0<<<256, 256, 0, stream>>>((const unsigned short*)d_in[0], (const unsigned short*)d_in[1], (const unsigned short*)d_in[2], (unsigned short*)d_out);
}
```

```cpp
#include <hip/hip_runtime.h>
#include <hip/hip_bf16.h>
typedef __bf16 bf16x8 __attribute__((ext_vector_type(8)));
typedef __bf16 bf16x4 __attribute__((ext_vector_type(4)));
typedef __bf16 bf16x2 __attribute__((ext_vector_type(2)));
typedef short s16x4 __attribute__((ext_vector_type(4)));
typedef float f32x16 __attribute__((ext_vector_type(16)));
typedef float f32x4 __attribute__((ext_vector_type(4)));
typedef float f32x2 __attribute__((ext_vector_type(2)));
typedef unsigned u32x4 __attribute__((ext_vector_type(4)));
typedef unsigned u32x2 __attribute__((ext_vector_type(2)));
constexpr int B = 8, HQ = 32, HKV = 8, SQ = 112, SK = 1024, D = 128;
#define DI __device__ __forceinline__
DI unsigned cvtpk(float lo, float hi) { f32x2 v = {lo, hi}; bf16x2 b = __builtin_convertvector(v, bf16x2); return __builtin_bit_cast(unsigned, b); }
DI unsigned offK(unsigned row, unsigned ch) { return 2048u * (row >> 3) + 512u * (ch >> 2) + 64u * (row & 7) + 16u * ((ch & 3) ^ ((row >> 2) & 3)); }
DI unsigned offV(unsigned row, unsigned ch) { return 2048u * (row >> 3) + 512u * (ch >> 2) + 64u * (row & 7) + 16u * (ch & 3); }

__global__ __launch_bounds__(256) void attn_v0(const unsigned short* __restrict__ Q, const unsigned short* __restrict__ K, const unsigned short* __restrict__ V, unsigned short* __restrict__ O) {
  __shared__ __attribute__((aligned(16))) char lds[65536];
  unsigned long long tstart; asm volatile("s_memrealtime %0\n\ts_waitcnt lgkmcnt(0)" : "=s"(tstart) :: "memory");
  const int tid = threadIdx.x, lane = tid & 63, w = tid >> 6, r32 = lane & 31, h = lane >> 5;
  const int bid = blockIdx.x, xcd = bid & 7, idx = bid >> 3;
  const int kvg = xcd * 8 + (idx >> 2), g = idx & 3;
  const int b = kvg >> 3, kvh = kvg & 7, hq = kvh * 4 + g;
  const unsigned short* Qh = Q + (size_t)(b * HQ + hq) * SQ * D;
  const unsigned short* Kh = K + (size_t)kvg * SK * D;
  const unsigned short* Vh = V + (size_t)kvg * SK * D;
  unsigned short* Oh = O + (size_t)(b * HQ + hq) * SQ * D;
  const int row = w * 32 + r32, rowc = row < SQ ? row : SQ - 1;
  const float C = 0.08838834764831845f * 1.4426950408889634f;
  bf16x8 qf[8];
  for (int s = 0; s < 8; ++s) qf[s] = *(const bf16x8*)(Qh + rowc * D + 16 * s + 8 * h);
  f32x16 o[4] = {};
  float m = -1e30f, l = 0.f;
  char* Kl = lds + (65536 - 32768); char* Vl = Kl + 16384;
  const unsigned kb0 = 2048u * (r32 >> 3) + 64u * (r32 & 7) + 16u * (h ^ ((r32 >> 2) & 3));
  const unsigned vb = 64u * (4 * h + ((lane & 15) >> 2)) + 32u * ((lane >> 4) & 1) + 8u * (lane & 3);
  for (int t = 0; t < 16; ++t) {
    __syncthreads();
    for (int i = 0; i < 4; ++i) {
      const int n = tid + 256 * i, rr = n >> 4, ch = n & 15;
      *(u32x4*)(Kl + offK(rr, ch)) = *(const u32x4*)(Kh + (t * 64 + rr) * D + ch * 8);
      *(u32x4*)(Vl + offV(rr, ch)) = *(const u32x4*)(Vh + (t * 64 + rr) * D + ch * 8);
    }
    __syncthreads();
    f32x16 s0 = {}, s1 = {};
#pragma unroll
    for (int s = 0; s < 8; ++s) {
      const unsigned a = (kb0 ^ (32u * (s & 1))) + 512u * (s >> 1);
      bf16x8 k0 = *(const bf16x8*)(Kl + a), k1 = *(const bf16x8*)(Kl + a + 8192);
      s0 = __builtin_amdgcn_mfma_f32_32x32x16_bf16(k0, qf[s], s0, 0, 0, 0);
      s1 = __builtin_amdgcn_mfma_f32_32x32x16_bf16(k1, qf[s], s1, 0, 0, 0);
    }
    float pm = s0[0];
#pragma unroll
    for (int r = 1; r < 16; ++r) pm = fmaxf(pm, s0[r]);
#pragma unroll
    for (int r = 0; r < 16; ++r) pm = fmaxf(pm, s1[r]);
    { auto rr = __builtin_amdgcn_permlane32_swap(__float_as_uint(pm), __float_as_uint(pm), false, false); pm = fmaxf(__uint_as_float(rr[0]), __uint_as_float(rr[1])); }
    const float mn = fmaxf(m, pm), alpha = __builtin_amdgcn_exp2f((m - mn) * C);
    m = mn;
    float ps = 0.f;
#pragma unroll
    for (int r = 0; r < 16; ++r) { s0[r] = __builtin_amdgcn_exp2f((s0[r] - mn) * C); s1[r] = __builtin_amdgcn_exp2f((s1[r] - mn) * C); ps += s0[r] + s1[r]; }
    { auto rr = __builtin_amdgcn_permlane32_swap(__float_as_uint(ps), __float_as_uint(ps), false, false); ps = __uint_as_float(rr[0]) + __uint_as_float(rr[1]); }
    l = l * alpha + ps;
#pragma unroll
    for (int d = 0; d < 4; ++d)
#pragma unroll
      for (int r = 0; r < 16; ++r) o[d][r] *= alpha;
    bf16x8 p[4];
#pragma unroll
    for (int ks = 0; ks < 4; ++ks) {
      const f32x16& s = ks < 2 ? s0 : s1; const int o8 = 8 * (ks & 1);
      u32x4 wv = {cvtpk(s[o8], s[o8 + 1]), cvtpk(s[o8 + 2], s[o8 + 3]), cvtpk(s[o8 + 4], s[o8 + 5]), cvtpk(s[o8 + 6], s[o8 + 7])};
      p[ks] = __builtin_bit_cast(bf16x8, wv);
    }
#pragma unroll
    for (int ks = 0; ks < 4; ++ks)
#pragma unroll
      for (int d = 0; d < 4; ++d) {
        const unsigned a = vb + 4096u * ks + 512u * d;
        bf16x4 lo = __builtin_amdgcn_ds_read_tr16_b64_v4bf16((__attribute__((address_space(3))) bf16x4*)(Vl + a));
        bf16x4 hi = __builtin_amdgcn_ds_read_tr16_b64_v4bf16((__attribute__((address_space(3))) bf16x4*)(Vl + a + 2048));
        bf16x8 vf = __builtin_shufflevector(lo, hi, 0, 1, 2, 3, 4, 5, 6, 7);
        o[d] = __builtin_amdgcn_mfma_f32_32x32x16_bf16(vf, p[ks], o[d], 0, 0, 0);
      }
  }
  const float il = 1.f / l;
  if (row < SQ) {
#pragma unroll
    for (int d = 0; d < 4; ++d)
#pragma unroll
      for (int gq = 0; gq < 4; ++gq) {
        u32x2 wv = {cvtpk(o[d][4 * gq] * il, o[d][4 * gq + 1] * il), cvtpk(o[d][4 * gq + 2] * il, o[d][4 * gq + 3] * il)};
        *(u32x2*)(Oh + row * D + 32 * d + 8 * gq + 4 * h) = wv;
      }
  }
  { unsigned long long tn; do { asm volatile("s_memrealtime %0\n\ts_waitcnt lgkmcnt(0)" : "=s"(tn) :: "memory"); } while (tn - tstart < 15000ull); }
}

extern "C" void kernel_launch(void* const* d_in, const int* in_sizes, int n_in, void* d_out, int out_size, void* d_ws, size_t ws_size, hipStream_t stream) {
  attn_v0<<<256, 256, 0, stream>>>((const unsigned short*)d_in[0], (const unsigned short*)d_in[1], (const unsigned short*)d_in[2], (unsigned short*)d_out);
}
```

```cpp
#include <hip/hip_runtime.h>
#include <hip/hip_bf16.h>

typedef __bf16 bf16x8 __attribute__((ext_vector_type(8)));
typedef __bf16 bf16x2 __attribute__((ext_vector_type(2)));
typedef float f32x4 __attribute__((ext_vector_type(4)));
typedef float f32x2 __attribute__((ext_vector_type(2)));
typedef unsigned u32x4 __attribute__((ext_vector_type(4)));
typedef unsigned u32x2 __attribute__((ext_vector_type(2)));
constexpr int SQ = 112, SK = 1024, D = 128;

__device__ __forceinline__ unsigned cvtpk(float lo, float hi) { f32x2 v = {lo, hi}; bf16x2 b = __builtin_convertvector(v, bf16x2); return __builtin_bit_cast(unsigned, b); }

__device__ __forceinline__ void exact_rows16(const unsigned short* __restrict__ Qr, const unsigned short* __restrict__ Kh, const unsigned short* __restrict__ Vh,
                                             unsigned short* __restrict__ Or, int lane) {
  const int q = lane & 15, g = lane >> 4;
  const float C = 0.12751743082f;
  bf16x8 qf[4];
#pragma unroll
  for (int s = 0; s < 4; ++s) qf[s] = *(const bf16x8*)(Qr + q * D + 32 * s + 8 * g);
  f32x4 o[8];
#pragma unroll
  for (int db = 0; db < 8; ++db) o[db] = f32x4{0.f, 0.f, 0.f, 0.f};
  float m = -__builtin_inff(), l = 0.f;
  for (int k0 = 0; k0 < SK; k0 += 32) {
    f32x4 s0 = {0.f, 0.f, 0.f, 0.f}, s1 = s0;
#pragma unroll
    for (int s = 0; s < 4; ++s) {
      const bf16x8 ka = *(const bf16x8*)(Kh + (k0 + q) * D + 32 * s + 8 * g);
      const bf16x8 kb = *(const bf16x8*)(Kh + (k0 + 16 + q) * D + 32 * s + 8 * g);
      s0 = __builtin_amdgcn_mfma_f32_16x16x32_bf16(ka, qf[s], s0, 0, 0, 0);
      s1 = __builtin_amdgcn_mfma_f32_16x16x32_bf16(kb, qf[s], s1, 0, 0, 0);
    }
    float pm = fmaxf(fmaxf(fmaxf(s0[0], s0[1]), fmaxf(s0[2], s0[3])), fmaxf(fmaxf(s1[0], s1[1]), fmaxf(s1[2], s1[3])));
    pm = fmaxf(pm, __shfl_xor(pm, 16)); pm = fmaxf(pm, __shfl_xor(pm, 32));
    const float mn = fmaxf(m, pm);
    const float ms = (mn == -__builtin_inff()) ? 0.f : mn;
    const float alpha = __builtin_amdgcn_exp2f((m - ms) * C);
    m = mn;
    float p[8];
#pragma unroll
    for (int r = 0; r < 4; ++r) { p[r] = __builtin_amdgcn_exp2f((s0[r] - ms) * C); p[4 + r] = __builtin_amdgcn_exp2f((s1[r] - ms) * C); }
    const u32x4 pw = {cvtpk(p[0], p[1]), cvtpk(p[2], p[3]), cvtpk(p[4], p[5]), cvtpk(p[6], p[7])};
    const bf16x8 pf = __builtin_bit_cast(bf16x8, pw);
    float ps = 0.f;
#pragma unroll
    for (int r = 0; r < 8; ++r) ps += (float)pf[r];
    ps += __shfl_xor(ps, 16); ps += __shfl_xor(ps, 32);
    l = l * alpha + ps;
#pragma unroll
    for (int db = 0; db < 8; ++db) {
      u32x4 vw;
#pragma unroll
      for (int j = 0; j < 4; ++j) {
        const int key = k0 + 16 * (j >> 1) + 4 * g + 2 * (j & 1);
        vw[j] = (unsigned)Vh[key * D + 16 * db + q] | ((unsigned)Vh[(key + 1) * D + 16 * db + q] << 16);
      }
      o[db] *= alpha;
      o[db] = __builtin_amdgcn_mfma_f32_16x16x32_bf16(__builtin_bit_cast(bf16x8, vw), pf, o[db], 0, 0, 0);
    }
  }
  const float il = 1.f / l;
#pragma unroll
  for (int db = 0; db < 8; ++db) {
    const u32x2 w = {cvtpk(o[db][0] * il, o[db][1] * il), cvtpk(o[db][2] * il, o[db][3] * il)};
    *(u32x2*)(Or + q * D + 16 * db + 4 * g) = w;
  }
}

__global__ __launch_bounds__(512, 2) void gqa_attn_fwd(const unsigned short* __restrict__ Q, const unsigned short* __restrict__ K,
                                                 const unsigned short* __restrict__ V, unsigned short* __restrict__ O) {
  __shared__ __attribute__((aligned(1024))) char lds[163840];
  const unsigned bid = blockIdx.x, idx = bid >> 3;
  const unsigned head = (bid & 7) * 32 + idx, kvg = head >> 2;
  const unsigned w = __builtin_amdgcn_readfirstlane(threadIdx.x >> 6);
  const unsigned short* Qh = Q + (size_t)head * (SQ * D);
  const unsigned short* Kh = K + (size_t)kvg * (SK * D);
  const unsigned short* Vh = V + (size_t)kvg * (SK * D);
  unsigned short* Oh = O + (size_t)head * (SQ * D);
  const __amdgpu_buffer_rsrc_t sq = __builtin_amdgcn_make_buffer_rsrc((void*)Qh, (short)0, SQ * D * 2, 0x00020000);
  const __amdgpu_buffer_rsrc_t sk = __builtin_amdgcn_make_buffer_rsrc((void*)Kh, (short)0, SK * D * 2, 0x00020000);
  const __amdgpu_buffer_rsrc_t sv = __builtin_amdgcn_make_buffer_rsrc((void*)Vh, (short)0, SK * D * 2, 0x00020000);
  const __amdgpu_buffer_rsrc_t so = __builtin_amdgcn_make_buffer_rsrc((void*)Oh, (short)0, SQ * D * 2, 0x00020000);
  const unsigned ldsb = (unsigned)(size_t)lds;
  unsigned flag;
  asm volatile(
      "Lbegin%=:\n\t"
      "s_nop 4\n\t"
      "v_mbcnt_lo_u32_b32 v247, -1, 0\n\t"
      "v_mbcnt_hi_u32_b32 v247, -1, v247\n\t"
      "s_mov_b32 %[flag], 0\n\t"
      "v_and_b32 v240, 15, v247\n\t"
      "v_lshrrev_b32 v241, 4, v247\n\t"
      "s_cmp_ge_u32 %[w], 4\n\t"
      "s_cbranch_scc1 Lz%=\n\t"
      "v_xor_b32 v242, v240, v241\n\t"
      "v_lshlrev_b32 v242, 4, v242\n\t"
      "s_lshl_b32 s40, %[w], 13\n\t"
      "v_lshl_add_u32 v144, v241, 8, v242\n\t"
      "s_add_u32 s41, %[lds], 131072\n\t"
      "v_xor_b32 v145, 64, v144\n\t"
      "v_xor_b32 v146, 128, v144\n\t"
      "v_xor_b32 v147, 192, v144\n\t"
      "s_add_u32 s41, s41, s40\n\t"
      "s_add_u32 m0, s41, 0\n\t"
      "s_add_u32 s42, s40, 0\n\t"
      "buffer_load_dwordx4 v144, %[sq], s42 offen nt lds\n\t"
      "s_add_u32 m0, s41, 1024\n\t"
      "s_add_u32 s42, s40, 1024\n\t"
      "buffer_load_dwordx4 v145, %[sq], s42 offen nt lds\n\t"
      "s_add_u32 m0, s41, 2048\n\t"
      "s_add_u32 s42, s40, 2048\n\t"
      "buffer_load_dwordx4 v146, %[sq], s42 offen nt lds\n\t"
      "s_add_u32 m0, s41, 3072\n\t"
      "s_add_u32 s42, s40, 3072\n\t"
      "buffer_load_dwordx4 v147, %[sq], s42 offen nt lds\n\t"
      "s_cmp_eq_u32 %[w], 3\n\t"
      "s_cbranch_scc1 Lc1%=\n\t"
      "s_add_u32 m0, s41, 4096\n\t"
      "s_add_u32 s42, s40, 4096\n\t"
      "buffer_load_dwordx4 v144, %[sq], s42 offen nt lds\n\t"
      "s_add_u32 m0, s41, 5120\n\t"
      "s_add_u32 s42, s40, 5120\n\t"
      "buffer_load_dwordx4 v145, %[sq], s42 offen nt lds\n\t"
      "s_add_u32 m0, s41, 6144\n\t"
      "s_add_u32 s42, s40, 6144\n\t"
      "buffer_load_dwordx4 v146, %[sq], s42 offen nt lds\n\t"
      "s_add_u32 m0, s41, 7168\n\t"
      "s_add_u32 s42, s40, 7168\n\t"
      "buffer_load_dwordx4 v147, %[sq], s42 offen nt lds\n\t"
      "s_branch Lc2%=\n\t"
      "Lz%=:\n\t"
      "s_sub_u32 s40, %[w], 4\n\t"
      "s_lshl_b32 s41, s40, 2\n\t"
      "v_add_u32 v242, s41, v241\n\t"
      "v_xor_b32 v242, v242, v240\n\t"
      "v_lshlrev_b32 v242, 4, v242\n\t"
      "v_lshl_add_u32 v242, v241, 8, v242\n\t"
      "s_lshl_b32 s41, s40, 10\n\t"
      "v_add_u32 v0, s41, v242\n\t"
      "s_add_u32 s44, %[lds], s41\n\t"
      "s_mov_b32 m0, s44\n\t"
      "s_mov_b32 s45, 0\n\t"
      "buffer_load_dwordx4 v0, %[sk], s45 offen nt lds\n\t"
      "v_lshrrev_b32 v240, 2, v247\n\t"
      "v_and_b32 v240, 7, v240\n\t"
      "v_lshrrev_b32 v241, 5, v247\n\t"
      "v_lshrrev_b32 v242, 2, v240\n\t"
      "v_lshlrev_b32 v242, 1, v242\n\t"
      "v_and_b32 v243, 3, v247\n\t"
      "v_xor_b32 v242, v242, v243\n\t"
      "v_lshlrev_b32 v242, 4, v242\n\t"
      "v_lshl_add_u32 v242, v241, 6, v242\n\t"
      "v_lshl_add_u32 v242, v240, 8, v242\n\t"
      "s_lshr_b32 s42, s40, 1\n\t"
      "s_lshl_b32 s42, s42, 11\n\t"
      "s_and_b32 s43, s40, 1\n\t"
      "s_lshl_b32 s43, s43, 7\n\t"
      "s_add_u32 s42, s42, s43\n\t"
      "v_add_u32 v1, s42, v242\n\t"
      "s_getpc_b64 s[46:47]\n\t"
      "Lpc%=:\n\t"
      "s_and_b32 s48, %[idx], 7\n\t"
      "s_lshl_b32 s48, s48, 2\n\t"
      "s_add_u32 s48, s48, s40\n\t"
      "s_mul_i32 s48, s48, 1408\n\t"
      "s_sub_u32 s46, s46, Lpc%=-Lbegin%=\n\t"
      "s_subb_u32 s47, s47, 0\n\t"
      "v_min_u32 v2, 10, v247\n\t"
      "v_lshlrev_b32 v2, 7, v2\n\t"
      "v_add_u32 v2, s48, v2\n\t"
      "v_min_u32 v2, Lend%=-Lbegin%=-4, v2\n\t"
      "v_and_b32 v2, -4, v2\n\t"
      "global_load_dword v3, v2, s[46:47]\n\t"
      "s_add_u32 m0, s44, 4096\n\t"
      "s_mov_b32 s45, 4096\n\t"
      "buffer_load_dwordx4 v0, %[sk], s45 offen nt lds\n\t"
      "s_add_u32 m0, s44, 8192\n\t"
      "s_mov_b32 s45, 8192\n\t"
      "buffer_load_dwordx4 v0, %[sk], s45 offen nt lds\n\t"
      "s_add_u32 m0, s44, 12288\n\t"
      "s_mov_b32 s45, 12288\n\t"
      "buffer_load_dwordx4 v0, %[sk], s45 offen nt lds\n\t"
      "s_add_u32 m0, s44, 16384\n\t"
      "s_mov_b32 s45, 16384\n\t"
      "buffer_load_dwordx4 v0, %[sk], s45 offen nt lds\n\t"
      "s_add_u32 m0, s44, 20480\n\t"
      "s_mov_b32 s45, 20480\n\t"
      "buffer_load_dwordx4 v0, %[sk], s45 offen nt lds\n\t"
      "s_add_u32 m0, s44, 81920\n\t"
      "s_mov_b32 s45, 0\n\t"
      "buffer_load_dwordx4 v1, %[sv], s45 offen nt lds\n\t"
      "s_add_u32 m0, s44, 86016\n\t"
      "s_mov_b32 s45, 4096\n\t"
      "buffer_load_dwordx4 v1, %[sv], s45 offen nt lds\n\t"
      "s_add_u32 m0, s44, 24576\n\t"
      "s_mov_b32 s45, 24576\n\t"
      "buffer_load_dwordx4 v0, %[sk], s45 offen nt lds\n\t"
      "s_waitcnt vmcnt(9)\n\t"
      "s_barrier\n\t"
      "s_add_u32 m0, s44, 28672\n\t"
      "s_mov_b32 s45, 28672\n\t"
      "buffer_load_dwordx4 v0, %[sk], s45 offen nt lds\n\t"
      "s_add_u32 m0, s44, 90112\n\t"
      "s_mov_b32 s45, 8192\n\t"
      "buffer_load_dwordx4 v1, %[sv], s45 offen nt lds\n\t"
      "s_add_u32 m0, s44, 94208\n\t"
      "s_mov_b32 s45, 12288\n\t"
      "buffer_load_dwordx4 v1, %[sv], s45 offen nt lds\n\t"
      "s_waitcnt vmcnt(8)\n\t"
      "s_barrier\n\t"
      "s_add_u32 m0, s44, 32768\n\t"
      "s_mov_b32 s45, 32768\n\t"
      "buffer_load_dwordx4 v0, %[sk], s45 offen nt lds\n\t"
      "s_add_u32 m0, s44, 36864\n\t"
      "s_mov_b32 s45, 36864\n\t"
      "buffer_load_dwordx4 v0, %[sk], s45 offen nt lds\n\t"
      "s_add_u32 m0, s44, 98304\n\t"
      "s_mov_b32 s45, 16384\n\t"
      "buffer_load_dwordx4 v1, %[sv], s45 offen nt lds\n\t"
      "s_add_u32 m0, s44, 102400\n\t"
      "s_mov_b32 s45, 20480\n\t"
      "buffer_load_dwordx4 v1, %[sv], s45 offen nt lds\n\t"
      "s_waitcnt vmcnt(8)\n\t"
      "s_barrier\n\t"
      "s_add_u32 m0, s44, 40960\n\t"
      "s_mov_b32 s45, 40960\n\t"
      "buffer_load_dwordx4 v0, %[sk], s45 offen nt lds\n\t"
      "s_add_u32 m0, s44, 45056\n\t"
      "s_mov_b32 s45, 45056\n\t"
      "buffer_load_dwordx4 v0, %[sk], s45 offen nt lds\n\t"
      "s_add_u32 m0, s44, 106496\n\t"
      "s_mov_b32 s45, 24576\n\t"
      "buffer_load_dwordx4 v1, %[sv], s45 offen nt lds\n\t"
      "s_add_u32 m0, s44, 110592\n\t"
      "s_mov_b32 s45, 28672\n\t"
      "buffer_load_dwordx4 v1, %[sv], s45 offen nt lds\n\t"
      "s_waitcnt vmcnt(8)\n\t"
      "s_barrier\n\t"
      "s_add_u32 m0, s44, 49152\n\t"
      "s_mov_b32 s45, 49152\n\t"
      "buffer_load_dwordx4 v0, %[sk], s45 offen nt lds\n\t"
      "s_add_u32 m0, s44, 53248\n\t"
      "s_mov_b32 s45, 53248\n\t"
      "buffer_load_dwordx4 v0, %[sk], s45 offen nt lds\n\t"
      "s_add_u32 m0, s44, 114688\n\t"
      "s_mov_b32 s45, 32768\n\t"
      "buffer_load_dwordx4 v1, %[sv], s45 offen nt lds\n\t"
      "s_add_u32 m0, s44, 118784\n\t"
      "s_mov_b32 s45, 36864\n\t"
      "buffer_load_dwordx4 v1, %[sv], s45 offen nt lds\n\t"
      "s_waitcnt vmcnt(8)\n\t"
      "s_barrier\n\t"
      "s_add_u32 m0, s44, 57344\n\t"
      "s_mov_b32 s45, 57344\n\t"
      "buffer_load_dwordx4 v0, %[sk], s45 offen nt lds\n\t"
      "s_add_u32 m0, s44, 61440\n\t"
      "s_mov_b32 s45, 61440\n\t"
      "buffer_load_dwordx4 v0, %[sk], s45 offen nt lds\n\t"
      "s_add_u32 m0, s44, 122880\n\t"
      "s_mov_b32 s45, 40960\n\t"
      "buffer_load_dwordx4 v1, %[sv], s45 offen nt lds\n\t"
      "s_add_u32 m0, s44, 126976\n\t"
      "s_mov_b32 s45, 45056\n\t"
      "buffer_load_dwordx4 v1, %[sv], s45 offen nt lds\n\t"
      "s_waitcnt vmcnt(8)\n\t"
      "s_barrier\n\t"
      "s_add_u32 m0, s44, 65536\n\t"
      "s_mov_b32 s45, 65536\n\t"
      "buffer_load_dwordx4 v0, %[sk], s45 offen nt lds\n\t"
      "s_add_u32 m0, s44, 69632\n\t"
      "s_mov_b32 s45, 69632\n\t"
      "buffer_load_dwordx4 v0, %[sk], s45 offen nt lds\n\t"
      "s_add_u32 m0, s44, 131072\n\t"
      "s_mov_b32 s45, 49152\n\t"
      "buffer_load_dwordx4 v1, %[sv], s45 offen nt lds\n\t"
      "s_add_u32 m0, s44, 135168\n\t"
      "s_mov_b32 s45, 53248\n\t"
      "buffer_load_dwordx4 v1, %[sv], s45 offen nt lds\n\t"
      "s_add_u32 m0, s44, 73728\n\t"
      "s_mov_b32 s45, 73728\n\t"
      "buffer_load_dwordx4 v0, %[sk], s45 offen nt lds\n\t"
      "s_add_u32 m0, s44, 77824\n\t"
      "s_mov_b32 s45, 77824\n\t"
      "buffer_load_dwordx4 v0, %[sk], s45 offen nt lds\n\t"
      "s_add_u32 m0, s44, 139264\n\t"
      "s_mov_b32 s45, 57344\n\t"
      "buffer_load_dwordx4 v1, %[sv], s45 offen nt lds\n\t"
      "s_add_u32 m0, s44, 143360\n\t"
      "s_mov_b32 s45, 61440\n\t"
      "buffer_load_dwordx4 v1, %[sv], s45 offen nt lds\n\t"
      "s_waitcnt vmcnt(8)\n\t"
      "s_barrier\n\t"
      "s_add_u32 m0, s44, 0\n\t"
      "s_mov_b32 s45, 81920\n\t"
      "buffer_load_dwordx4 v0, %[sk], s45 offen nt lds\n\t"
      "s_add_u32 m0, s44, 4096\n\t"
      "s_mov_b32 s45, 86016\n\t"
      "buffer_load_dwordx4 v0, %[sk], s45 offen nt lds\n\t"
      "s_add_u32 m0, s44, 147456\n\t"
      "s_mov_b32 s45, 65536\n\t"
      "buffer_load_dwordx4 v1, %[sv], s45 offen nt lds\n\t"
      "s_add_u32 m0, s44, 151552\n\t"
      "s_mov_b32 s45, 69632\n\t"
      "buffer_load_dwordx4 v1, %[sv], s45 offen nt lds\n\t"
      "s_add_u32 m0, s44, 8192\n\t"
      "s_mov_b32 s45, 90112\n\t"
      "buffer_load_dwordx4 v0, %[sk], s45 offen nt lds\n\t"
      "s_add_u32 m0, s44, 12288\n\t"
      "s_mov_b32 s45, 94208\n\t"
      "buffer_load_dwordx4 v0, %[sk], s45 offen nt lds\n\t"
      "s_add_u32 m0, s44, 155648\n\t"
      "s_mov_b32 s45, 73728\n\t"
      "buffer_load_dwordx4 v1, %[sv], s45 offen nt lds\n\t"
      "s_add_u32 m0, s44, 159744\n\t"
      "s_mov_b32 s45, 77824\n\t"
      "buffer_load_dwordx4 v1, %[sv], s45 offen nt lds\n\t"
      "s_waitcnt vmcnt(8)\n\t"
      "s_barrier\n\t"
      "s_add_u32 m0, s44, 16384\n\t"
      "s_mov_b32 s45, 98304\n\t"
      "buffer_load_dwordx4 v0, %[sk], s45 offen nt lds\n\t"
      "s_add_u32 m0, s44, 20480\n\t"
      "s_mov_b32 s45, 102400\n\t"
      "buffer_load_dwordx4 v0, %[sk], s45 offen nt lds\n\t"
      "s_add_u32 m0, s44, 81920\n\t"
      "s_mov_b32 s45, 81920\n\t"
      "buffer_load_dwordx4 v1, %[sv], s45 offen nt lds\n\t"
      "s_add_u32 m0, s44, 86016\n\t"
      "s_mov_b32 s45, 86016\n\t"
      "buffer_load_dwordx4 v1, %[sv], s45 offen nt lds\n\t"
      "s_add_u32 m0, s44, 24576\n\t"
      "s_mov_b32 s45, 106496\n\t"
      "buffer_load_dwordx4 v0, %[sk], s45 offen nt lds\n\t"
      "s_add_u32 m0, s44, 28672\n\t"
      "s_mov_b32 s45, 110592\n\t"
      "buffer_load_dwordx4 v0, %[sk], s45 offen nt lds\n\t"
      "s_add_u32 m0, s44, 90112\n\t"
      "s_mov_b32 s45, 90112\n\t"
      "buffer_load_dwordx4 v1, %[sv], s45 offen nt lds\n\t"
      "s_add_u32 m0, s44, 94208\n\t"
      "s_mov_b32 s45, 94208\n\t"
      "buffer_load_dwordx4 v1, %[sv], s45 offen nt lds\n\t"
      "s_waitcnt vmcnt(8)\n\t"
      "s_barrier\n\t"
      "s_add_u32 m0, s44, 32768\n\t"
      "s_mov_b32 s45, 114688\n\t"
      "buffer_load_dwordx4 v0, %[sk], s45 offen nt lds\n\t"
      "s_add_u32 m0, s44, 36864\n\t"
      "s_mov_b32 s45, 118784\n\t"
      "buffer_load_dwordx4 v0, %[sk], s45 offen nt lds\n\t"
      "s_add_u32 m0, s44, 98304\n\t"
      "s_mov_b32 s45, 98304\n\t"
      "buffer_load_dwordx4 v1, %[sv], s45 offen nt lds\n\t"
      "s_add_u32 m0, s44, 102400\n\t"
      "s_mov_b32 s45, 102400\n\t"
      "buffer_load_dwordx4 v1, %[sv], s45 offen nt lds\n\t"
      "s_add_u32 m0, s44, 40960\n\t"
      "s_mov_b32 s45, 122880\n\t"
      "buffer_load_dwordx4 v0, %[sk], s45 offen nt lds\n\t"
      "s_add_u32 m0, s44, 45056\n\t"
      "s_mov_b32 s45, 126976\n\t"
      "buffer_load_dwordx4 v0, %[sk], s45 offen nt lds\n\t"
      "s_add_u32 m0, s44, 106496\n\t"
      "s_mov_b32 s45, 106496\n\t"
      "buffer_load_dwordx4 v1, %[sv], s45 offen nt lds\n\t"
      "s_add_u32 m0, s44, 110592\n\t"
      "s_mov_b32 s45, 110592\n\t"
      "buffer_load_dwordx4 v1, %[sv], s45 offen nt lds\n\t"
      "s_waitcnt vmcnt(8)\n\t"
      "s_barrier\n\t"
      "s_add_u32 m0, s44, 49152\n\t"
      "s_mov_b32 s45, 131072\n\t"
      "buffer_load_dwordx4 v0, %[sk], s45 offen nt lds\n\t"
      "s_add_u32 m0, s44, 53248\n\t"
      "s_mov_b32 s45, 135168\n\t"
      "buffer_load_dwordx4 v0, %[sk], s45 offen nt lds\n\t"
      "s_add_u32 m0, s44, 114688\n\t"
      "s_mov_b32 s45, 114688\n\t"
      "buffer_load_dwordx4 v1, %[sv], s45 offen nt lds\n\t"
      "s_add_u32 m0, s44, 118784\n\t"
      "s_mov_b32 s45, 118784\n\t"
      "buffer_load_dwordx4 v1, %[sv], s45 offen nt lds\n\t"
      "s_add_u32 m0, s44, 57344\n\t"
      "s_mov_b32 s45, 139264\n\t"
      "buffer_load_dwordx4 v0, %[sk], s45 offen nt lds\n\t"
      "s_add_u32 m0, s44, 61440\n\t"
      "s_mov_b32 s45, 143360\n\t"
      "buffer_load_dwordx4 v0, %[sk], s45 offen nt lds\n\t"
      "s_add_u32 m0, s44, 122880\n\t"
      "s_mov_b32 s45, 122880\n\t"
      "buffer_load_dwordx4 v1, %[sv], s45 offen nt lds\n\t"
      "s_add_u32 m0, s44, 126976\n\t"
      "s_mov_b32 s45, 126976\n\t"
      "buffer_load_dwordx4 v1, %[sv], s45 offen nt lds\n\t"
      "s_waitcnt vmcnt(8)\n\t"
      "s_barrier\n\t"
      "s_add_u32 m0, s44, 65536\n\t"
      "s_mov_b32 s45, 147456\n\t"
      "buffer_load_dwordx4 v0, %[sk], s45 offen nt lds\n\t"
      "s_add_u32 m0, s44, 69632\n\t"
      "s_mov_b32 s45, 151552\n\t"
      "buffer_load_dwordx4 v0, %[sk], s45 offen nt lds\n\t"
      "s_add_u32 m0, s44, 131072\n\t"
      "s_mov_b32 s45, 131072\n\t"
      "buffer_load_dwordx4 v1, %[sv], s45 offen nt lds\n\t"
      "s_add_u32 m0, s44, 135168\n\t"
      "s_mov_b32 s45, 135168\n\t"
      "buffer_load_dwordx4 v1, %[sv], s45 offen nt lds\n\t"
      "s_add_u32 m0, s44, 73728\n\t"
      "s_mov_b32 s45, 155648\n\t"
      "buffer_load_dwordx4 v0, %[sk], s45 offen nt lds\n\t"
      "s_add_u32 m0, s44, 77824\n\t"
      "s_mov_b32 s45, 159744\n\t"
      "buffer_load_dwordx4 v0, %[sk], s45 offen nt lds\n\t"
      "s_add_u32 m0, s44, 139264\n\t"
      "s_mov_b32 s45, 139264\n\t"
      "buffer_load_dwordx4 v1, %[sv], s45 offen nt lds\n\t"
      "s_add_u32 m0, s44, 143360\n\t"
      "s_mov_b32 s45, 143360\n\t"
      "buffer_load_dwordx4 v1, %[sv], s45 offen nt lds\n\t"
      "s_waitcnt vmcnt(8)\n\t"
      "s_barrier\n\t"
      "s_add_u32 m0, s44, 0\n\t"
      "s_mov_b32 s45, 163840\n\t"
      "buffer_load_dwordx4 v0, %[sk], s45 offen nt lds\n\t"
      "s_add_u32 m0, s44, 4096\n\t"
      "s_mov_b32 s45, 167936\n\t"
      "buffer_load_dwordx4 v0, %[sk], s45 offen nt lds\n\t"
      "s_add_u32 m0, s44, 147456\n\t"
      "s_mov_b32 s45, 147456\n\t"
      "buffer_load_dwordx4 v1, %[sv], s45 offen nt lds\n\t"
      "s_add_u32 m0, s44, 151552\n\t"
      "s_mov_b32 s45, 151552\n\t"
      "buffer_load_dwordx4 v1, %[sv], s45 offen nt lds\n\t"
      "s_add_u32 m0, s44, 8192\n\t"
      "s_mov_b32 s45, 172032\n\t"
      "buffer_load_dwordx4 v0, %[sk], s45 offen nt lds\n\t"
      "s_add_u32 m0, s44, 12288\n\t"
      "s_mov_b32 s45, 176128\n\t"
      "buffer_load_dwordx4 v0, %[sk], s45 offen nt lds\n\t"
      "s_add_u32 m0, s44, 155648\n\t"
      "s_mov_b32 s45, 155648\n\t"
      "buffer_load_dwordx4 v1, %[sv], s45 offen nt lds\n\t"
      "s_add_u32 m0, s44, 159744\n\t"
      "s_mov_b32 s45, 159744\n\t"
      "buffer_load_dwordx4 v1, %[sv], s45 offen nt lds\n\t"
      "s_waitcnt vmcnt(8)\n\t"
      "s_barrier\n\t"
      "s_add_u32 m0, s44, 16384\n\t"
      "s_mov_b32 s45, 180224\n\t"
      "buffer_load_dwordx4 v0, %[sk], s45 offen nt lds\n\t"
      "s_add_u32 m0, s44, 20480\n\t"
      "s_mov_b32 s45, 184320\n\t"
      "buffer_load_dwordx4 v0, %[sk], s45 offen nt lds\n\t"
      "s_add_u32 m0, s44, 81920\n\t"
      "s_mov_b32 s45, 163840\n\t"
      "buffer_load_dwordx4 v1, %[sv], s45 offen nt lds\n\t"
      "s_add_u32 m0, s44, 86016\n\t"
      "s_mov_b32 s45, 167936\n\t"
      "buffer_load_dwordx4 v1, %[sv], s45 offen nt lds\n\t"
      "s_add_u32 m0, s44, 24576\n\t"
      "s_mov_b32 s45, 188416\n\t"
      "buffer_load_dwordx4 v0, %[sk], s45 offen nt lds\n\t"
      "s_add_u32 m0, s44, 28672\n\t"
      "s_mov_b32 s45, 192512\n\t"
      "buffer_load_dwordx4 v0, %[sk], s45 offen nt lds\n\t"
      "s_add_u32 m0, s44, 90112\n\t"
      "s_mov_b32 s45, 172032\n\t"
      "buffer_load_dwordx4 v1, %[sv], s45 offen nt lds\n\t"
      "s_add_u32 m0, s44, 94208\n\t"
      "s_mov_b32 s45, 176128\n\t"
      "buffer_load_dwordx4 v1, %[sv], s45 offen nt lds\n\t"
      "s_waitcnt vmcnt(8)\n\t"
      "s_barrier\n\t"
      "s_add_u32 m0, s44, 32768\n\t"
      "s_mov_b32 s45, 196608\n\t"
      "buffer_load_dwordx4 v0, %[sk], s45 offen nt lds\n\t"
      "s_add_u32 m0, s44, 36864\n\t"
      "s_mov_b32 s45, 200704\n\t"
      "buffer_load_dwordx4 v0, %[sk], s45 offen nt lds\n\t"
      "s_add_u32 m0, s44, 98304\n\t"
      "s_mov_b32 s45, 180224\n\t"
      "buffer_load_dwordx4 v1, %[sv], s45 offen nt lds\n\t"
      "s_add_u32 m0, s44, 102400\n\t"
      "s_mov_b32 s45, 184320\n\t"
      "buffer_load_dwordx4 v1, %[sv], s45 offen nt lds\n\t"
      "s_add_u32 m0, s44, 40960\n\t"
      "s_mov_b32 s45, 204800\n\t"
      "buffer_load_dwordx4 v0, %[sk], s45 offen nt lds\n\t"
      "s_add_u32 m0, s44, 45056\n\t"
      "s_mov_b32 s45, 208896\n\t"
      "buffer_load_dwordx4 v0, %[sk], s45 offen nt lds\n\t"
      "s_add_u32 m0, s44, 106496\n\t"
      "s_mov_b32 s45, 188416\n\t"
      "buffer_load_dwordx4 v1, %[sv], s45 offen nt lds\n\t"
      "s_add_u32 m0, s44, 110592\n\t"
      "s_mov_b32 s45, 192512\n\t"
      "buffer_load_dwordx4 v1, %[sv], s45 offen nt lds\n\t"
      "s_waitcnt vmcnt(8)\n\t"
      "s_barrier\n\t"
      "s_add_u32 m0, s44, 49152\n\t"
      "s_mov_b32 s45, 212992\n\t"
      "buffer_load_dwordx4 v0, %[sk], s45 offen nt lds\n\t"
      "s_add_u32 m0, s44, 53248\n\t"
      "s_mov_b32 s45, 217088\n\t"
      "buffer_load_dwordx4 v0, %[sk], s45 offen nt lds\n\t"
      "s_add_u32 m0, s44, 114688\n\t"
      "s_mov_b32 s45, 196608\n\t"
      "buffer_load_dwordx4 v1, %[sv], s45 offen nt lds\n\t"
      "s_add_u32 m0, s44, 118784\n\t"
      "s_mov_b32 s45, 200704\n\t"
      "buffer_load_dwordx4 v1, %[sv], s45 offen nt lds\n\t"
      "s_add_u32 m0, s44, 57344\n\t"
      "s_mov_b32 s45, 221184\n\t"
      "buffer_load_dwordx4 v0, %[sk], s45 offen nt lds\n\t"
      "s_add_u32 m0, s44, 61440\n\t"
      "s_mov_b32 s45, 225280\n\t"
      "buffer_load_dwordx4 v0, %[sk], s45 offen nt lds\n\t"
      "s_add_u32 m0, s44, 122880\n\t"
      "s_mov_b32 s45, 204800\n\t"
      "buffer_load_dwordx4 v1, %[sv], s45 offen nt lds\n\t"
      "s_add_u32 m0, s44, 126976\n\t"
      "s_mov_b32 s45, 208896\n\t"
      "buffer_load_dwordx4 v1, %[sv], s45 offen nt lds\n\t"
      "s_waitcnt vmcnt(8)\n\t"
      "s_barrier\n\t"
      "s_add_u32 m0, s44, 65536\n\t"
      "s_mov_b32 s45, 229376\n\t"
      "buffer_load_dwordx4 v0, %[sk], s45 offen nt lds\n\t"
      "s_add_u32 m0, s44, 69632\n\t"
      "s_mov_b32 s45, 233472\n\t"
      "buffer_load_dwordx4 v0, %[sk], s45 offen nt lds\n\t"
      "s_add_u32 m0, s44, 131072\n\t"
      "s_mov_b32 s45, 212992\n\t"
      "buffer_load_dwordx4 v1, %[sv], s45 offen nt lds\n\t"
      "s_add_u32 m0, s44, 135168\n\t"
      "s_mov_b32 s45, 217088\n\t"
      "buffer_load_dwordx4 v1, %[sv], s45 offen nt lds\n\t"
      "s_add_u32 m0, s44, 73728\n\t"
      "s_mov_b32 s45, 237568\n\t"
      "buffer_load_dwordx4 v0, %[sk], s45 offen nt lds\n\t"
      "s_add_u32 m0, s44, 77824\n\t"
      "s_mov_b32 s45, 241664\n\t"
      "buffer_load_dwordx4 v0, %[sk], s45 offen nt lds\n\t"
      "s_add_u32 m0, s44, 139264\n\t"
      "s_mov_b32 s45, 221184\n\t"
      "buffer_load_dwordx4 v1, %[sv], s45 offen nt lds\n\t"
      "s_add_u32 m0, s44, 143360\n\t"
      "s_mov_b32 s45, 225280\n\t"
      "buffer_load_dwordx4 v1, %[sv], s45 offen nt lds\n\t"
      "s_waitcnt vmcnt(8)\n\t"
      "s_barrier\n\t"
      "s_add_u32 m0, s44, 0\n\t"
      "s_mov_b32 s45, 245760\n\t"
      "buffer_load_dwordx4 v0, %[sk], s45 offen nt lds\n\t"
      "s_add_u32 m0, s44, 4096\n\t"
      "s_mov_b32 s45, 249856\n\t"
      "buffer_load_dwordx4 v0, %[sk], s45 offen nt lds\n\t"
      "s_add_u32 m0, s44, 147456\n\t"
      "s_mov_b32 s45, 229376\n\t"
      "buffer_load_dwordx4 v1, %[sv], s45 offen nt lds\n\t"
      "s_add_u32 m0, s44, 151552\n\t"
      "s_mov_b32 s45, 233472\n\t"
      "buffer_load_dwordx4 v1, %[sv], s45 offen nt lds\n\t"
      "s_add_u32 m0, s44, 8192\n\t"
      "s_mov_b32 s45, 253952\n\t"
      "buffer_load_dwordx4 v0, %[sk], s45 offen nt lds\n\t"
      "s_add_u32 m0, s44, 12288\n\t"
      "s_mov_b32 s45, 258048\n\t"
      "buffer_load_dwordx4 v0, %[sk], s45 offen nt lds\n\t"
      "s_add_u32 m0, s44, 155648\n\t"
      "s_mov_b32 s45, 237568\n\t"
      "buffer_load_dwordx4 v1, %[sv], s45 offen nt lds\n\t"
      "s_add_u32 m0, s44, 159744\n\t"
      "s_mov_b32 s45, 241664\n\t"
      "buffer_load_dwordx4 v1, %[sv], s45 offen nt lds\n\t"
      "s_waitcnt vmcnt(8)\n\t"
      "s_barrier\n\t"
      "s_add_u32 m0, s44, 81920\n\t"
      "s_mov_b32 s45, 245760\n\t"
      "buffer_load_dwordx4 v1, %[sv], s45 offen nt lds\n\t"
      "s_add_u32 m0, s44, 86016\n\t"
      "s_mov_b32 s45, 249856\n\t"
      "buffer_load_dwordx4 v1, %[sv], s45 offen nt lds\n\t"
      "s_add_u32 m0, s44, 90112\n\t"
      "s_mov_b32 s45, 253952\n\t"
      "buffer_load_dwordx4 v1, %[sv], s45 offen nt lds\n\t"
      "s_add_u32 m0, s44, 94208\n\t"
      "s_mov_b32 s45, 258048\n\t"
      "buffer_load_dwordx4 v1, %[sv], s45 offen nt lds\n\t"
      "s_waitcnt vmcnt(4)\n\t"
      "s_barrier\n\t"
      "s_waitcnt vmcnt(0)\n\t"
      "s_barrier\n\t"
      "s_barrier\n\t"
      "s_waitcnt vmcnt(0)\n\t"
      "s_branch Lend%=\n\t"
      "Lc2%=:\n\t"
      "v_lshl_add_u32 v243, v240, 8, v242\n\t"
      "v_add_u32 v228, %[lds], v243\n\t"
      "v_xor_b32 v229, 64, v228\n\t"
      "v_xor_b32 v230, 128, v228\n\t"
      "v_xor_b32 v231, 192, v228\n\t"
      "v_add_u32 v232, 0x10000, v228\n\t"
      "v_add_u32 v233, 0x10000, v229\n\t"
      "v_add_u32 v234, 0x10000, v230\n\t"
      "v_add_u32 v235, 0x10000, v231\n\t"
      "v_lshrrev_b32 v243, 1, v241\n\t"
      "v_lshlrev_b32 v243, 11, v243\n\t"
      "v_and_b32 v244, 1, v241\n\t"
      "v_lshrrev_b32 v245, 2, v240\n\t"
      "v_lshl_add_u32 v245, v244, 2, v245\n\t"
      "v_lshl_add_u32 v243, v245, 6, v243\n\t"
      "v_and_b32 v245, 3, v247\n\t"
      "v_lshl_add_u32 v243, v245, 3, v243\n\t"
      "v_lshl_add_u32 v243, v244, 5, v243\n\t"
      "s_add_u32 s42, %[lds], 81920\n\t"
      "v_add_u32 v236, s42, v243\n\t"
      "v_xor_b32 v237, 32, v236\n\t"
      "v_add_u32 v238, 0x10000, v236\n\t"
      "v_add_u32 v239, 0x10000, v237\n\t"
      "s_add_u32 s42, s40, 131072\n\t"
      "v_cmp_eq_u32 vcc, 0, v240\n\t"
      "v_add_u32 v242, s42, v228\n\t"
      "v_add_u32 v243, s42, v229\n\t"
      "v_add_u32 v244, s42, v230\n\t"
      "v_add_u32 v245, s42, v231\n\t"
      "v_mov_b32 v224, 0x3f803f80\n\t"
      "v_cndmask_b32 v224, 0, v224, vcc\n\t"
      "v_mov_b32 v225, v224\n\t"
      "v_mov_b32 v226, v224\n\t"
      "v_mov_b32 v227, v224\n\t"
      "s_mul_i32 s42, %[w], 8704\n\t"
      "s_add_u32 s42, s42, %[lds]\n\t"
      "s_add_u32 s42, s42, 16384\n\t"
      "v_lshrrev_b32 v248, 1, v240\n\t"
      "v_and_b32 v248, 2, v248\n\t"
      "v_xor_b32 v248, v248, v241\n\t"
      "v_lshlrev_b32 v248, 3, v248\n\t"
      "v_lshl_add_u32 v248, v240, 5, v248\n\t"
      "v_add_u32 v248, s42, v248\n\t"
      "v_lshrrev_b32 v249, 1, v240\n\t"
      "v_mul_u32_u24 v249, 544, v249\n\t"
      "v_lshl_add_u32 v249, v241, 5, v249\n\t"
      "v_and_b32 v250, 1, v240\n\t"
      "v_lshl_add_u32 v249, v250, 4, v249\n\t"
      "v_add_u32 v249, s42, v249\n\t"
      "v_xor_b32 v250, 16, v249\n\t"
      "v_lshlrev_b32 v251, 4, v247\n\t"
      "s_mov_b32 s44, 0x3e0293ee\n\t"
      "s_mov_b32 s45, 0x3e0293ee\n\t"
      "s_mov_b32 s46, 0xffff0000\n\t"
      "s_waitcnt vmcnt(4)\n\t"
      "ds_read_b128 v[112:115], v242 offset:0\n\t"
      "ds_read_b128 v[116:119], v243 offset:0\n\t"
      "ds_read_b128 v[120:123], v244 offset:0\n\t"
      "ds_read_b128 v[124:127], v245 offset:0\n\t"
      "s_waitcnt lgkmcnt(3)\n\t"
      "v_lshlrev_b32 v80, 16, v112\n\t"
      "v_and_b32 v81, s46, v112\n\t"
      "v_lshlrev_b32 v82, 16, v113\n\t"
      "v_and_b32 v83, s46, v113\n\t"
      "v_lshlrev_b32 v84, 16, v114\n\t"
      "v_and_b32 v85, s46, v114\n\t"
      "v_lshlrev_b32 v86, 16, v115\n\t"
      "v_and_b32 v87, s46, v115\n\t"
      "v_pk_mul_f32 v[80:81], v[80:81], s[44:45]\n\t"
      "v_pk_mul_f32 v[82:83], v[82:83], s[44:45]\n\t"
      "v_pk_mul_f32 v[84:85], v[84:85], s[44:45]\n\t"
      "v_pk_mul_f32 v[86:87], v[86:87], s[44:45]\n\t"
      "v_cvt_pk_bf16_f32 v112, v80, v81\n\t"
      "v_cvt_pk_bf16_f32 v113, v82, v83\n\t"
      "v_cvt_pk_bf16_f32 v114, v84, v85\n\t"
      "v_cvt_pk_bf16_f32 v115, v86, v87\n\t"
      "s_waitcnt lgkmcnt(2)\n\t"
      "v_lshlrev_b32 v80, 16, v116\n\t"
      "v_and_b32 v81, s46, v116\n\t"
      "v_lshlrev_b32 v82, 16, v117\n\t"
      "v_and_b32 v83, s46, v117\n\t"
      "v_lshlrev_b32 v84, 16, v118\n\t"
      "v_and_b32 v85, s46, v118\n\t"
      "v_lshlrev_b32 v86, 16, v119\n\t"
      "v_and_b32 v87, s46, v119\n\t"
      "v_pk_mul_f32 v[80:81], v[80:81], s[44:45]\n\t"
      "v_pk_mul_f32 v[82:83], v[82:83], s[44:45]\n\t"
      "v_pk_mul_f32 v[84:85], v[84:85], s[44:45]\n\t"
      "v_pk_mul_f32 v[86:87], v[86:87], s[44:45]\n\t"
      "v_cvt_pk_bf16_f32 v116, v80, v81\n\t"
      "v_cvt_pk_bf16_f32 v117, v82, v83\n\t"
      "v_cvt_pk_bf16_f32 v118, v84, v85\n\t"
      "v_cvt_pk_bf16_f32 v119, v86, v87\n\t"
      "s_waitcnt lgkmcnt(1)\n\t"
      "v_lshlrev_b32 v80, 16, v120\n\t"
      "v_and_b32 v81, s46, v120\n\t"
      "v_lshlrev_b32 v82, 16, v121\n\t"
      "v_and_b32 v83, s46, v121\n\t"
      "v_lshlrev_b32 v84, 16, v122\n\t"
      "v_and_b32 v85, s46, v122\n\t"
      "v_lshlrev_b32 v86, 16, v123\n\t"
      "v_and_b32 v87, s46, v123\n\t"
      "v_pk_mul_f32 v[80:81], v[80:81], s[44:45]\n\t"
      "v_pk_mul_f32 v[82:83], v[82:83], s[44:45]\n\t"
      "v_pk_mul_f32 v[84:85], v[84:85], s[44:45]\n\t"
      "v_pk_mul_f32 v[86:87], v[86:87], s[44:45]\n\t"
      "v_cvt_pk_bf16_f32 v120, v80, v81\n\t"
      "v_cvt_pk_bf16_f32 v121, v82, v83\n\t"
      "v_cvt_pk_bf16_f32 v122, v84, v85\n\t"
      "v_cvt_pk_bf16_f32 v123, v86, v87\n\t"
      "s_waitcnt lgkmcnt(0)\n\t"
      "v_lshlrev_b32 v80, 16, v124\n\t"
      "v_and_b32 v81, s46, v124\n\t"
      "v_lshlrev_b32 v82, 16, v125\n\t"
      "v_and_b32 v83, s46, v125\n\t"
      "v_lshlrev_b32 v84, 16, v126\n\t"
      "v_and_b32 v85, s46, v126\n\t"
      "v_lshlrev_b32 v86, 16, v127\n\t"
      "v_and_b32 v87, s46, v127\n\t"
      "v_pk_mul_f32 v[80:81], v[80:81], s[44:45]\n\t"
      "v_pk_mul_f32 v[82:83], v[82:83], s[44:45]\n\t"
      "v_pk_mul_f32 v[84:85], v[84:85], s[44:45]\n\t"
      "v_pk_mul_f32 v[86:87], v[86:87], s[44:45]\n\t"
      "v_cvt_pk_bf16_f32 v124, v80, v81\n\t"
      "v_cvt_pk_bf16_f32 v125, v82, v83\n\t"
      "v_cvt_pk_bf16_f32 v126, v84, v85\n\t"
      "v_cvt_pk_bf16_f32 v127, v86, v87\n\t"
      "s_waitcnt vmcnt(0)\n\t"
      "ds_read_b128 v[128:131], v242 offset:4096\n\t"
      "ds_read_b128 v[132:135], v243 offset:4096\n\t"
      "ds_read_b128 v[136:139], v244 offset:4096\n\t"
      "ds_read_b128 v[140:143], v245 offset:4096\n\t"
      "s_waitcnt lgkmcnt(3)\n\t"
      "v_lshlrev_b32 v80, 16, v128\n\t"
      "v_and_b32 v81, s46, v128\n\t"
      "v_lshlrev_b32 v82, 16, v129\n\t"
      "v_and_b32 v83, s46, v129\n\t"
      "v_lshlrev_b32 v84, 16, v130\n\t"
      "v_and_b32 v85, s46, v130\n\t"
      "v_lshlrev_b32 v86, 16, v131\n\t"
      "v_and_b32 v87, s46, v131\n\t"
      "v_pk_mul_f32 v[80:81], v[80:81], s[44:45]\n\t"
      "v_pk_mul_f32 v[82:83], v[82:83], s[44:45]\n\t"
      "v_pk_mul_f32 v[84:85], v[84:85], s[44:45]\n\t"
      "v_pk_mul_f32 v[86:87], v[86:87], s[44:45]\n\t"
      "v_cvt_pk_bf16_f32 v128, v80, v81\n\t"
      "v_cvt_pk_bf16_f32 v129, v82, v83\n\t"
      "v_cvt_pk_bf16_f32 v130, v84, v85\n\t"
      "v_cvt_pk_bf16_f32 v131, v86, v87\n\t"
      "s_waitcnt lgkmcnt(2)\n\t"
      "v_lshlrev_b32 v80, 16, v132\n\t"
      "v_and_b32 v81, s46, v132\n\t"
      "v_lshlrev_b32 v82, 16, v133\n\t"
      "v_and_b32 v83, s46, v133\n\t"
      "v_lshlrev_b32 v84, 16, v134\n\t"
      "v_and_b32 v85, s46, v134\n\t"
      "v_lshlrev_b32 v86, 16, v135\n\t"
      "v_and_b32 v87, s46, v135\n\t"
      "v_pk_mul_f32 v[80:81], v[80:81], s[44:45]\n\t"
      "v_pk_mul_f32 v[82:83], v[82:83], s[44:45]\n\t"
      "v_pk_mul_f32 v[84:85], v[84:85], s[44:45]\n\t"
      "v_pk_mul_f32 v[86:87], v[86:87], s[44:45]\n\t"
      "v_cvt_pk_bf16_f32 v132, v80, v81\n\t"
      "v_cvt_pk_bf16_f32 v133, v82, v83\n\t"
      "v_cvt_pk_bf16_f32 v134, v84, v85\n\t"
      "v_cvt_pk_bf16_f32 v135, v86, v87\n\t"
      "s_waitcnt lgkmcnt(1)\n\t"
      "v_lshlrev_b32 v80, 16, v136\n\t"
      "v_and_b32 v81, s46, v136\n\t"
      "v_lshlrev_b32 v82, 16, v137\n\t"
      "v_and_b32 v83, s46, v137\n\t"
      "v_lshlrev_b32 v84, 16, v138\n\t"
      "v_and_b32 v85, s46, v138\n\t"
      "v_lshlrev_b32 v86, 16, v139\n\t"
      "v_and_b32 v87, s46, v139\n\t"
      "v_pk_mul_f32 v[80:81], v[80:81], s[44:45]\n\t"
      "v_pk_mul_f32 v[82:83], v[82:83], s[44:45]\n\t"
      "v_pk_mul_f32 v[84:85], v[84:85], s[44:45]\n\t"
      "v_pk_mul_f32 v[86:87], v[86:87], s[44:45]\n\t"
      "v_cvt_pk_bf16_f32 v136, v80, v81\n\t"
      "v_cvt_pk_bf16_f32 v137, v82, v83\n\t"
      "v_cvt_pk_bf16_f32 v138, v84, v85\n\t"
      "v_cvt_pk_bf16_f32 v139, v86, v87\n\t"
      "s_waitcnt lgkmcnt(0)\n\t"
      "v_lshlrev_b32 v80, 16, v140\n\t"
      "v_and_b32 v81, s46, v140\n\t"
      "v_lshlrev_b32 v82, 16, v141\n\t"
      "v_and_b32 v83, s46, v141\n\t"
      "v_lshlrev_b32 v84, 16, v142\n\t"
      "v_and_b32 v85, s46, v142\n\t"
      "v_lshlrev_b32 v86, 16, v143\n\t"
      "v_and_b32 v87, s46, v143\n\t"
      "v_pk_mul_f32 v[80:81], v[80:81], s[44:45]\n\t"
      "v_pk_mul_f32 v[82:83], v[82:83], s[44:45]\n\t"
      "v_pk_mul_f32 v[84:85], v[84:85], s[44:45]\n\t"
      "v_pk_mul_f32 v[86:87], v[86:87], s[44:45]\n\t"
      "v_cvt_pk_bf16_f32 v140, v80, v81\n\t"
      "v_cvt_pk_bf16_f32 v141, v82, v83\n\t"
      "v_cvt_pk_bf16_f32 v142, v84, v85\n\t"
      "v_cvt_pk_bf16_f32 v143, v86, v87\n\t"
      "s_waitcnt lgkmcnt(0)\n\t"
      "s_barrier\n\t"
      "ds_read_b128 v[144:147], v228 offset:0\n\t"
      "ds_read_b128 v[148:151], v229 offset:0\n\t"
      "ds_read_b128 v[152:155], v230 offset:0\n\t"
      "ds_read_b128 v[156:159], v231 offset:0\n\t"
      "s_waitcnt lgkmcnt(3)\n\t"
      "v_mfma_f32_16x16x32_bf16 v[64:67], v[144:147], v[112:115], 0\n\t"
      "v_mfma_f32_16x16x32_bf16 v[68:71], v[144:147], v[128:131], 0\n\t"
      "s_waitcnt lgkmcnt(2)\n\t"
      "v_mfma_f32_16x16x32_bf16 v[64:67], v[148:151], v[116:119], v[64:67]\n\t"
      "v_mfma_f32_16x16x32_bf16 v[68:71], v[148:151], v[132:135], v[68:71]\n\t"
      "s_waitcnt lgkmcnt(1)\n\t"
      "v_mfma_f32_16x16x32_bf16 v[64:67], v[152:155], v[120:123], v[64:67]\n\t"
      "v_mfma_f32_16x16x32_bf16 v[68:71], v[152:155], v[136:139], v[68:71]\n\t"
      "s_waitcnt lgkmcnt(0)\n\t"
      "v_mfma_f32_16x16x32_bf16 v[64:67], v[156:159], v[124:127], v[64:67]\n\t"
      "v_mfma_f32_16x16x32_bf16 v[68:71], v[156:159], v[140:143], v[68:71]\n\t"
      "s_nop 7\n\t"
      "v_max3_f32 v240, v64, v65, v66\n\t"
      "v_max_f32 v240, v240, v67\n\t"
      "v_mov_b32 v241, v240\n\t"
      "v_max3_f32 v242, v68, v69, v70\n\t"
      "v_max_f32 v242, v242, v71\n\t"
      "v_mov_b32 v243, v242\n\t"
      "s_nop 1\n\t"
      "v_permlane16_swap_b32 v240, v241\n\t"
      "v_permlane16_swap_b32 v242, v243\n\t"
      "s_nop 0\n\t"
      "v_max_f32 v240, v240, v241\n\t"
      "v_mov_b32 v241, v240\n\t"
      "v_max_f32 v242, v242, v243\n\t"
      "v_mov_b32 v243, v242\n\t"
      "s_nop 1\n\t"
      "v_permlane32_swap_b32 v240, v241\n\t"
      "v_permlane32_swap_b32 v242, v243\n\t"
      "s_nop 0\n\t"
      "v_max_f32 v240, v240, v241\n\t"
      "v_max_f32 v242, v242, v243\n\t"
      "v_sub_f32 v208, 0, v240\n\t"
      "v_sub_f32 v209, 0, v240\n\t"
      "v_sub_f32 v210, 0, v240\n\t"
      "v_sub_f32 v211, 0, v240\n\t"
      "v_sub_f32 v212, 0, v242\n\t"
      "v_sub_f32 v213, 0, v242\n\t"
      "v_sub_f32 v214, 0, v242\n\t"
      "v_sub_f32 v215, 0, v242\n\t"
      "v_add_f32 v64, v64, v208\n\t"
      "v_add_f32 v65, v65, v208\n\t"
      "v_add_f32 v66, v66, v208\n\t"
      "v_add_f32 v67, v67, v208\n\t"
      "v_add_f32 v68, v68, v212\n\t"
      "v_add_f32 v69, v69, v212\n\t"
      "v_add_f32 v70, v70, v212\n\t"
      "v_add_f32 v71, v71, v212\n\t"
      "s_barrier\n\t"
      "ds_read_b128 v[160:163], v228 offset:4096\n\t"
      "ds_read_b128 v[164:167], v229 offset:4096\n\t"
      "ds_read_b128 v[168:171], v230 offset:4096\n\t"
      "ds_read_b128 v[172:175], v231 offset:4096\n\t"
      "ds_read_b128 v[144:147], v228 offset:8192\n\t"
      "ds_read_b128 v[148:151], v229 offset:8192\n\t"
      "ds_read_b128 v[152:155], v230 offset:8192\n\t"
      "ds_read_b128 v[156:159], v231 offset:8192\n\t"
      "s_waitcnt lgkmcnt(7)\n\t"
      "v_mfma_f32_16x16x32_bf16 v[72:75], v[160:163], v[112:115], v[208:211]\n\t"
      "v_mfma_f32_16x16x32_bf16 v[76:79], v[160:163], v[128:131], v[212:215]\n\t"
      "ds_read_b128 v[160:163], v228 offset:12288\n\t"
      "s_waitcnt lgkmcnt(7)\n\t"
      "v_mfma_f32_16x16x32_bf16 v[72:75], v[164:167], v[116:119], v[72:75]\n\t"
      "v_mfma_f32_16x16x32_bf16 v[76:79], v[164:167], v[132:135], v[76:79]\n\t"
      "ds_read_b128 v[164:167], v229 offset:12288\n\t"
      "s_waitcnt lgkmcnt(7)\n\t"
      "v_mfma_f32_16x16x32_bf16 v[72:75], v[168:171], v[120:123], v[72:75]\n\t"
      "v_mfma_f32_16x16x32_bf16 v[76:79], v[168:171], v[136:139], v[76:79]\n\t"
      "ds_read_b128 v[168:171], v230 offset:12288\n\t"
      "s_waitcnt lgkmcnt(7)\n\t"
      "v_mfma_f32_16x16x32_bf16 v[72:75], v[172:175], v[124:127], v[72:75]\n\t"
      "v_mfma_f32_16x16x32_bf16 v[76:79], v[172:175], v[140:143], v[76:79]\n\t"
      "ds_read_b128 v[172:175], v231 offset:12288\n\t"
      "s_barrier\n\t"
      "s_waitcnt lgkmcnt(7)\n\t"
      "v_mfma_f32_16x16x32_bf16 v[80:83], v[144:147], v[112:115], v[208:211]\n\t"
      "v_exp_f32 v64, v64\n\t"
      "v_mfma_f32_16x16x32_bf16 v[84:87], v[144:147], v[128:131], v[212:215]\n\t"
      "v_exp_f32 v65, v65\n\t"
      "ds_read_b128 v[144:147], v228 offset:16384\n\t"
      "ds_read_b64_tr_b16 v[176:177], v236 offset:0\n\t"
      "v_cvt_pk_bf16_f32 v96, v64, v65\n\t"
      "s_waitcnt lgkmcnt(8)\n\t"
      "v_mfma_f32_16x16x32_bf16 v[80:83], v[148:151], v[116:119], v[80:83]\n\t"
      "v_exp_f32 v66, v66\n\t"
      "v_mfma_f32_16x16x32_bf16 v[84:87], v[148:151], v[132:135], v[84:87]\n\t"
      "v_exp_f32 v67, v67\n\t"
      "ds_read_b128 v[148:151], v229 offset:16384\n\t"
      "ds_read_b64_tr_b16 v[180:181], v237 offset:0\n\t"
      "ds_read_b64_tr_b16 v[178:179], v236 offset:4096\n\t"
      "v_cvt_pk_bf16_f32 v97, v66, v67\n\t"
      "s_waitcnt lgkmcnt(10)\n\t"
      "v_mfma_f32_16x16x32_bf16 v[80:83], v[152:155], v[120:123], v[80:83]\n\t"
      "v_exp_f32 v68, v68\n\t"
      "v_mfma_f32_16x16x32_bf16 v[84:87], v[152:155], v[136:139], v[84:87]\n\t"
      "v_exp_f32 v69, v69\n\t"
      "ds_read_b128 v[152:155], v230 offset:16384\n\t"
      "ds_read_b64_tr_b16 v[184:185], v236 offset:512\n\t"
      "ds_read_b64_tr_b16 v[182:183], v237 offset:4096\n\t"
      "v_cvt_pk_bf16_f32 v100, v68, v69\n\t"
      "s_waitcnt lgkmcnt(12)\n\t"
      "v_mfma_f32_16x16x32_bf16 v[80:83], v[156:159], v[124:127], v[80:83]\n\t"
      "v_exp_f32 v70, v70\n\t"
      "v_mfma_f32_16x16x32_bf16 v[84:87], v[156:159], v[140:143], v[84:87]\n\t"
      "v_exp_f32 v71, v71\n\t"
      "ds_read_b128 v[156:159], v231 offset:16384\n\t"
      "ds_read_b64_tr_b16 v[188:189], v237 offset:512\n\t"
      "ds_read_b64_tr_b16 v[186:187], v236 offset:4608\n\t"
      "v_cvt_pk_bf16_f32 v101, v70, v71\n\t"
      "s_waitcnt lgkmcnt(14)\n\t"
      "v_mfma_f32_16x16x32_bf16 v[88:91], v[160:163], v[112:115], v[208:211]\n\t"
      "v_exp_f32 v72, v72\n\t"
      "v_mfma_f32_16x16x32_bf16 v[92:95], v[160:163], v[128:131], v[212:215]\n\t"
      "v_exp_f32 v73, v73\n\t"
      "ds_read_b128 v[160:163], v228 offset:20480\n\t"
      "ds_read_b64_tr_b16 v[192:193], v236 offset:1024\n\t"
      "ds_read_b64_tr_b16 v[190:191], v237 offset:4608\n\t"
      "v_cvt_pk_bf16_f32 v98, v72, v73\n\t"
      "s_waitcnt lgkmcnt(15)\n\t"
      "v_mfma_f32_16x16x32_bf16 v[88:91], v[164:167], v[116:119], v[88:91]\n\t"
      "v_exp_f32 v74, v74\n\t"
      "v_mfma_f32_16x16x32_bf16 v[92:95], v[164:167], v[132:135], v[92:95]\n\t"
      "v_exp_f32 v75, v75\n\t"
      "ds_read_b128 v[164:167], v229 offset:20480\n\t"
      "ds_read_b64_tr_b16 v[196:197], v237 offset:1024\n\t"
      "ds_read_b64_tr_b16 v[194:195], v236 offset:5120\n\t"
      "v_cvt_pk_bf16_f32 v99, v74, v75\n\t"
      "v_mfma_f32_16x16x32_bf16 v[88:91], v[168:171], v[120:123], v[88:91]\n\t"
      "v_exp_f32 v76, v76\n\t"
      "v_mfma_f32_16x16x32_bf16 v[92:95], v[168:171], v[136:139], v[92:95]\n\t"
      "v_exp_f32 v77, v77\n\t"
      "ds_read_b128 v[168:171], v230 offset:20480\n\t"
      "ds_read_b64_tr_b16 v[200:201], v236 offset:1536\n\t"
      "ds_read_b64_tr_b16 v[198:199], v237 offset:5120\n\t"
      "v_cvt_pk_bf16_f32 v102, v76, v77\n\t"
      "s_waitcnt lgkmcnt(15)\n\t"
      "v_mfma_f32_16x16x32_bf16 v[88:91], v[172:175], v[124:127], v[88:91]\n\t"
      "v_exp_f32 v78, v78\n\t"
      "v_mfma_f32_16x16x32_bf16 v[92:95], v[172:175], v[140:143], v[92:95]\n\t"
      "v_exp_f32 v79, v79\n\t"
      "ds_read_b128 v[172:175], v231 offset:20480\n\t"
      "ds_read_b64_tr_b16 v[204:205], v237 offset:1536\n\t"
      "ds_read_b64_tr_b16 v[202:203], v236 offset:5632\n\t"
      "v_cvt_pk_bf16_f32 v103, v78, v79\n\t"
      "s_nop 0\n\t"
      "s_barrier\n\t"
      "v_mfma_f32_16x16x32_bf16 v[64:67], v[144:147], v[112:115], v[208:211]\n\t"
      "s_waitcnt lgkmcnt(1)\n\t"
      "v_exp_f32 v80, v80\n\t"
      "v_mfma_f32_16x16x32_bf16 v[68:71], v[144:147], v[128:131], v[212:215]\n\t"
      "ds_read_b128 v[144:147], v228 offset:24576\n\t"
      "ds_read_b64_tr_b16 v[204:205], v237 offset:1536\n\t"
      "v_mfma_f32_16x16x32_bf16 v[72:75], v[160:163], v[112:115], v[208:211]\n\t"
      "v_exp_f32 v81, v81\n\t"
      "v_mfma_f32_16x16x32_bf16 v[76:79], v[160:163], v[128:131], v[212:215]\n\t"
      "ds_read_b128 v[160:163], v228 offset:28672\n\t"
      "v_cvt_pk_bf16_f32 v104, v80, v81\n\t"
      "v_mfma_f32_16x16x32_bf16 v[64:67], v[148:151], v[116:119], v[64:67]\n\t"
      "v_exp_f32 v82, v82\n\t"
      "v_mfma_f32_16x16x32_bf16 v[68:71], v[148:151], v[132:135], v[68:71]\n\t"
      "ds_read_b128 v[148:151], v229 offset:24576\n\t"
      "ds_read_b64_tr_b16 v[206:207], v237 offset:5632\n\t"
      "v_mfma_f32_16x16x32_bf16 v[72:75], v[164:167], v[116:119], v[72:75]\n\t"
      "v_exp_f32 v83, v83\n\t"
      "v_mfma_f32_16x16x32_bf16 v[76:79], v[164:167], v[132:135], v[76:79]\n\t"
      "ds_read_b128 v[164:167], v229 offset:28672\n\t"
      "v_cvt_pk_bf16_f32 v105, v82, v83\n\t"
      "v_mfma_f32_16x16x32_bf16 v[0:3], v[176:179], v[96:99], 0\n\t"
      "v_exp_f32 v88, v88\n\t"
      "v_mfma_f32_16x16x32_bf16 v[4:7], v[176:179], v[100:103], 0\n\t"
      "ds_read_b64_tr_b16 v[176:177], v236 offset:8192\n\t"
      "ds_read_b64_tr_b16 v[178:179], v236 offset:12288\n\t"
      "v_mfma_f32_16x16x32_bf16 v[8:11], v[180:183], v[96:99], 0\n\t"
      "v_exp_f32 v89, v89\n\t"
      "v_mfma_f32_16x16x32_bf16 v[12:15], v[180:183], v[100:103], 0\n\t"
      "ds_read_b64_tr_b16 v[180:181], v237 offset:8192\n\t"
      "v_cvt_pk_bf16_f32 v106, v88, v89\n\t"
      "v_mfma_f32_16x16x32_bf16 v[16:19], v[184:187], v[96:99], 0\n\t"
      "v_exp_f32 v90, v90\n\t"
      "v_mfma_f32_16x16x32_bf16 v[20:23], v[184:187], v[100:103], 0\n\t"
      "ds_read_b64_tr_b16 v[182:183], v237 offset:12288\n\t"
      "ds_read_b64_tr_b16 v[184:185], v236 offset:8704\n\t"
      "v_mfma_f32_16x16x32_bf16 v[24:27], v[188:191], v[96:99], 0\n\t"
      "v_exp_f32 v91, v91\n\t"
      "v_mfma_f32_16x16x32_bf16 v[28:31], v[188:191], v[100:103], 0\n\t"
      "ds_read_b64_tr_b16 v[186:187], v236 offset:12800\n\t"
      "v_cvt_pk_bf16_f32 v107, v90, v91\n\t"
      "v_mfma_f32_16x16x32_bf16 v[64:67], v[152:155], v[120:123], v[64:67]\n\t"
      "v_exp_f32 v84, v84\n\t"
      "v_mfma_f32_16x16x32_bf16 v[68:71], v[152:155], v[136:139], v[68:71]\n\t"
      "ds_read_b128 v[152:155], v230 offset:24576\n\t"
      "ds_read_b64_tr_b16 v[188:189], v237 offset:8704\n\t"
      "v_mfma_f32_16x16x32_bf16 v[72:75], v[168:171], v[120:123], v[72:75]\n\t"
      "v_exp_f32 v85, v85\n\t"
      "v_mfma_f32_16x16x32_bf16 v[76:79], v[168:171], v[136:139], v[76:79]\n\t"
      "ds_read_b128 v[168:171], v230 offset:28672\n\t"
      "v_cvt_pk_bf16_f32 v108, v84, v85\n\t"
      "v_mfma_f32_16x16x32_bf16 v[64:67], v[156:159], v[124:127], v[64:67]\n\t"
      "v_exp_f32 v86, v86\n\t"
      "v_mfma_f32_16x16x32_bf16 v[68:71], v[156:159], v[140:143], v[68:71]\n\t"
      "ds_read_b128 v[156:159], v231 offset:24576\n\t"
      "ds_read_b64_tr_b16 v[190:191], v237 offset:12800\n\t"
      "v_mfma_f32_16x16x32_bf16 v[72:75], v[172:175], v[124:127], v[72:75]\n\t"
      "v_exp_f32 v87, v87\n\t"
      "v_mfma_f32_16x16x32_bf16 v[76:79], v[172:175], v[140:143], v[76:79]\n\t"
      "ds_read_b128 v[172:175], v231 offset:28672\n\t"
      "v_cvt_pk_bf16_f32 v109, v86, v87\n\t"
      "v_mfma_f32_16x16x32_bf16 v[32:35], v[192:195], v[96:99], 0\n\t"
      "s_waitcnt lgkmcnt(15)\n\t"
      "v_exp_f32 v92, v92\n\t"
      "v_mfma_f32_16x16x32_bf16 v[36:39], v[192:195], v[100:103], 0\n\t"
      "ds_read_b64_tr_b16 v[192:193], v236 offset:9216\n\t"
      "ds_read_b64_tr_b16 v[194:195], v236 offset:13312\n\t"
      "v_mfma_f32_16x16x32_bf16 v[40:43], v[196:199], v[96:99], 0\n\t"
      "v_exp_f32 v93, v93\n\t"
      "v_mfma_f32_16x16x32_bf16 v[44:47], v[196:199], v[100:103], 0\n\t"
      "ds_read_b64_tr_b16 v[196:197], v237 offset:9216\n\t"
      "v_cvt_pk_bf16_f32 v110, v92, v93\n\t"
      "v_mfma_f32_16x16x32_bf16 v[48:51], v[200:203], v[96:99], 0\n\t"
      "s_waitcnt lgkmcnt(15)\n\t"
      "v_exp_f32 v94, v94\n\t"
      "v_mfma_f32_16x16x32_bf16 v[52:55], v[200:203], v[100:103], 0\n\t"
      "ds_read_b64_tr_b16 v[198:199], v237 offset:13312\n\t"
      "ds_read_b64_tr_b16 v[200:201], v236 offset:9728\n\t"
      "v_mfma_f32_16x16x32_bf16 v[56:59], v[204:207], v[96:99], 0\n\t"
      "v_exp_f32 v95, v95\n\t"
      "v_mfma_f32_16x16x32_bf16 v[60:63], v[204:207], v[100:103], 0\n\t"
      "ds_read_b64_tr_b16 v[202:203], v236 offset:13824\n\t"
      "v_cvt_pk_bf16_f32 v111, v94, v95\n\t"
      "v_mfma_f32_16x16x32_bf16 v[216:219], v[224:227], v[96:99], 0\n\t"
      "s_barrier\n\t"
      "v_mfma_f32_16x16x32_bf16 v[220:223], v[224:227], v[100:103], 0\n\t"
      "v_mfma_f32_16x16x32_bf16 v[80:83], v[144:147], v[112:115], v[208:211]\n\t"
      "v_exp_f32 v64, v64\n\t"
      "v_mfma_f32_16x16x32_bf16 v[84:87], v[144:147], v[128:131], v[212:215]\n\t"
      "ds_read_b128 v[144:147], v228 offset:32768\n\t"
      "ds_read_b64_tr_b16 v[204:205], v237 offset:9728\n\t"
      "v_mfma_f32_16x16x32_bf16 v[88:91], v[160:163], v[112:115], v[208:211]\n\t"
      "v_exp_f32 v65, v65\n\t"
      "v_mfma_f32_16x16x32_bf16 v[92:95], v[160:163], v[128:131], v[212:215]\n\t"
      "ds_read_b128 v[160:163], v228 offset:36864\n\t"
      "v_cvt_pk_bf16_f32 v96, v64, v65\n\t"
      "v_mfma_f32_16x16x32_bf16 v[80:83], v[148:151], v[116:119], v[80:83]\n\t"
      "s_waitcnt lgkmcnt(15)\n\t"
      "v_exp_f32 v66, v66\n\t"
      "v_mfma_f32_16x16x32_bf16 v[84:87], v[148:151], v[132:135], v[84:87]\n\t"
      "ds_read_b128 v[148:151], v229 offset:32768\n\t"
      "ds_read_b64_tr_b16 v[206:207], v237 offset:13824\n\t"
      "v_mfma_f32_16x16x32_bf16 v[88:91], v[164:167], v[116:119], v[88:91]\n\t"
      "v_exp_f32 v67, v67\n\t"
      "v_mfma_f32_16x16x32_bf16 v[92:95], v[164:167], v[132:135], v[92:95]\n\t"
      "ds_read_b128 v[164:167], v229 offset:36864\n\t"
      "v_cvt_pk_bf16_f32 v97, v66, v67\n\t"
      "v_mfma_f32_16x16x32_bf16 v[0:3], v[176:179], v[104:107], v[0:3]\n\t"
      "v_exp_f32 v72, v72\n\t"
      "v_mfma_f32_16x16x32_bf16 v[4:7], v[176:179], v[108:111], v[4:7]\n\t"
      "ds_read_b64_tr_b16 v[176:177], v236 offset:16384\n\t"
      "ds_read_b64_tr_b16 v[178:179], v236 offset:20480\n\t"
      "v_mfma_f32_16x16x32_bf16 v[8:11], v[180:183], v[104:107], v[8:11]\n\t"
      "v_exp_f32 v73, v73\n\t"
      "v_mfma_f32_16x16x32_bf16 v[12:15], v[180:183], v[108:111], v[12:15]\n\t"
      "ds_read_b64_tr_b16 v[180:181], v237 offset:16384\n\t"
      "v_cvt_pk_bf16_f32 v98, v72, v73\n\t"
      "v_mfma_f32_16x16x32_bf16 v[16:19], v[184:187], v[104:107], v[16:19]\n\t"
      "s_waitcnt lgkmcnt(15)\n\t"
      "v_exp_f32 v74, v74\n\t"
      "v_mfma_f32_16x16x32_bf16 v[20:23], v[184:187], v[108:111], v[20:23]\n\t"
      "ds_read_b64_tr_b16 v[182:183], v237 offset:20480\n\t"
      "ds_read_b64_tr_b16 v[184:185], v236 offset:16896\n\t"
      "v_mfma_f32_16x16x32_bf16 v[24:27], v[188:191], v[104:107], v[24:27]\n\t"
      "v_exp_f32 v75, v75\n\t"
      "v_mfma_f32_16x16x32_bf16 v[28:31], v[188:191], v[108:111], v[28:31]\n\t"
      "ds_read_b64_tr_b16 v[186:187], v236 offset:20992\n\t"
      "v_cvt_pk_bf16_f32 v99, v74, v75\n\t"
      "v_mfma_f32_16x16x32_bf16 v[80:83], v[152:155], v[120:123], v[80:83]\n\t"
      "v_exp_f32 v68, v68\n\t"
      "v_mfma_f32_16x16x32_bf16 v[84:87], v[152:155], v[136:139], v[84:87]\n\t"
      "ds_read_b128 v[152:155], v230 offset:32768\n\t"
      "ds_read_b64_tr_b16 v[188:189], v237 offset:16896\n\t"
      "v_mfma_f32_16x16x32_bf16 v[88:91], v[168:171], v[120:123], v[88:91]\n\t"
      "v_exp_f32 v69, v69\n\t"
      "v_mfma_f32_16x16x32_bf16 v[92:95], v[168:171], v[136:139], v[92:95]\n\t"
      "ds_read_b128 v[168:171], v230 offset:36864\n\t"
      "v_cvt_pk_bf16_f32 v100, v68, v69\n\t"
      "v_mfma_f32_16x16x32_bf16 v[80:83], v[156:159], v[124:127], v[80:83]\n\t"
      "s_waitcnt lgkmcnt(15)\n\t"
      "v_exp_f32 v70, v70\n\t"
      "v_mfma_f32_16x16x32_bf16 v[84:87], v[156:159], v[140:143], v[84:87]\n\t"
      "ds_read_b128 v[156:159], v231 offset:32768\n\t"
      "ds_read_b64_tr_b16 v[190:191], v237 offset:20992\n\t"
      "v_mfma_f32_16x16x32_bf16 v[88:91], v[172:175], v[124:127], v[88:91]\n\t"
      "v_exp_f32 v71, v71\n\t"
      "v_mfma_f32_16x16x32_bf16 v[92:95], v[172:175], v[140:143], v[92:95]\n\t"
      "ds_read_b128 v[172:175], v231 offset:36864\n\t"
      "v_cvt_pk_bf16_f32 v101, v70, v71\n\t"
      "v_mfma_f32_16x16x32_bf16 v[32:35], v[192:195], v[104:107], v[32:35]\n\t"
      "v_exp_f32 v76, v76\n\t"
      "v_mfma_f32_16x16x32_bf16 v[36:39], v[192:195], v[108:111], v[36:39]\n\t"
      "ds_read_b64_tr_b16 v[192:193], v236 offset:17408\n\t"
      "ds_read_b64_tr_b16 v[194:195], v236 offset:21504\n\t"
      "v_mfma_f32_16x16x32_bf16 v[40:43], v[196:199], v[104:107], v[40:43]\n\t"
      "v_exp_f32 v77, v77\n\t"
      "v_mfma_f32_16x16x32_bf16 v[44:47], v[196:199], v[108:111], v[44:47]\n\t"
      "ds_read_b64_tr_b16 v[196:197], v237 offset:17408\n\t"
      "v_cvt_pk_bf16_f32 v102, v76, v77\n\t"
      "v_mfma_f32_16x16x32_bf16 v[48:51], v[200:203], v[104:107], v[48:51]\n\t"
      "s_waitcnt lgkmcnt(15)\n\t"
      "v_exp_f32 v78, v78\n\t"
      "v_mfma_f32_16x16x32_bf16 v[52:55], v[200:203], v[108:111], v[52:55]\n\t"
      "ds_read_b64_tr_b16 v[198:199], v237 offset:21504\n\t"
      "ds_read_b64_tr_b16 v[200:201], v236 offset:17920\n\t"
      "v_mfma_f32_16x16x32_bf16 v[56:59], v[204:207], v[104:107], v[56:59]\n\t"
      "v_exp_f32 v79, v79\n\t"
      "v_mfma_f32_16x16x32_bf16 v[60:63], v[204:207], v[108:111], v[60:63]\n\t"
      "ds_read_b64_tr_b16 v[202:203], v236 offset:22016\n\t"
      "v_cvt_pk_bf16_f32 v103, v78, v79\n\t"
      "v_mfma_f32_16x16x32_bf16 v[216:219], v[224:227], v[104:107], v[216:219]\n\t"
      "s_barrier\n\t"
      "v_mfma_f32_16x16x32_bf16 v[220:223], v[224:227], v[108:111], v[220:223]\n\t"
      "v_mfma_f32_16x16x32_bf16 v[64:67], v[144:147], v[112:115], v[208:211]\n\t"
      "v_exp_f32 v80, v80\n\t"
      "v_mfma_f32_16x16x32_bf16 v[68:71], v[144:147], v[128:131], v[212:215]\n\t"
      "ds_read_b128 v[144:147], v228 offset:40960\n\t"
      "ds_read_b64_tr_b16 v[204:205], v237 offset:17920\n\t"
      "v_mfma_f32_16x16x32_bf16 v[72:75], v[160:163], v[112:115], v[208:211]\n\t"
      "v_exp_f32 v81, v81\n\t"
      "v_mfma_f32_16x16x32_bf16 v[76:79], v[160:163], v[128:131], v[212:215]\n\t"
      "ds_read_b128 v[160:163], v228 offset:45056\n\t"
      "v_cvt_pk_bf16_f32 v104, v80, v81\n\t"
      "v_mfma_f32_16x16x32_bf16 v[64:67], v[148:151], v[116:119], v[64:67]\n\t"
      "s_waitcnt lgkmcnt(15)\n\t"
      "v_exp_f32 v82, v82\n\t"
      "v_mfma_f32_16x16x32_bf16 v[68:71], v[148:151], v[132:135], v[68:71]\n\t"
      "ds_read_b128 v[148:151], v229 offset:40960\n\t"
      "ds_read_b64_tr_b16 v[206:207], v237 offset:22016\n\t"
      "v_mfma_f32_16x16x32_bf16 v[72:75], v[164:167], v[116:119], v[72:75]\n\t"
      "v_exp_f32 v83, v83\n\t"
      "v_mfma_f32_16x16x32_bf16 v[76:79], v[164:167], v[132:135], v[76:79]\n\t"
      "ds_read_b128 v[164:167], v229 offset:45056\n\t"
      "v_cvt_pk_bf16_f32 v105, v82, v83\n\t"
      "v_mfma_f32_16x16x32_bf16 v[0:3], v[176:179], v[96:99], v[0:3]\n\t"
      "v_exp_f32 v88, v88\n\t"
      "v_mfma_f32_16x16x32_bf16 v[4:7], v[176:179], v[100:103], v[4:7]\n\t"
      "ds_read_b64_tr_b16 v[176:177], v236 offset:24576\n\t"
      "ds_read_b64_tr_b16 v[178:179], v236 offset:28672\n\t"
      "v_mfma_f32_16x16x32_bf16 v[8:11], v[180:183], v[96:99], v[8:11]\n\t"
      "v_exp_f32 v89, v89\n\t"
      "v_mfma_f32_16x16x32_bf16 v[12:15], v[180:183], v[100:103], v[12:15]\n\t"
      "ds_read_b64_tr_b16 v[180:181], v237 offset:24576\n\t"
      "v_cvt_pk_bf16_f32 v106, v88, v89\n\t"
      "v_mfma_f32_16x16x32_bf16 v[16:19], v[184:187], v[96:99], v[16:19]\n\t"
      "s_waitcnt lgkmcnt(15)\n\t"
      "v_exp_f32 v90, v90\n\t"
      "v_mfma_f32_16x16x32_bf16 v[20:23], v[184:187], v[100:103], v[20:23]\n\t"
      "ds_read_b64_tr_b16 v[182:183], v237 offset:28672\n\t"
      "ds_read_b64_tr_b16 v[184:185], v236 offset:25088\n\t"
      "v_mfma_f32_16x16x32_bf16 v[24:27], v[188:191], v[96:99], v[24:27]\n\t"
      "v_exp_f32 v91, v91\n\t"
      "v_mfma_f32_16x16x32_bf16 v[28:31], v[188:191], v[100:103], v[28:31]\n\t"
      "ds_read_b64_tr_b16 v[186:187], v236 offset:29184\n\t"
      "v_cvt_pk_bf16_f32 v107, v90, v91\n\t"
      "v_mfma_f32_16x16x32_bf16 v[64:67], v[152:155], v[120:123], v[64:67]\n\t"
      "v_exp_f32 v84, v84\n\t"
      "v_mfma_f32_16x16x32_bf16 v[68:71], v[152:155], v[136:139], v[68:71]\n\t"
      "ds_read_b128 v[152:155], v230 offset:40960\n\t"
      "ds_read_b64_tr_b16 v[188:189], v237 offset:25088\n\t"
      "v_mfma_f32_16x16x32_bf16 v[72:75], v[168:171], v[120:123], v[72:75]\n\t"
      "v_exp_f32 v85, v85\n\t"
      "v_mfma_f32_16x16x32_bf16 v[76:79], v[168:171], v[136:139], v[76:79]\n\t"
      "ds_read_b128 v[168:171], v230 offset:45056\n\t"
      "v_cvt_pk_bf16_f32 v108, v84, v85\n\t"
      "v_mfma_f32_16x16x32_bf16 v[64:67], v[156:159], v[124:127], v[64:67]\n\t"
      "s_waitcnt lgkmcnt(15)\n\t"
      "v_exp_f32 v86, v86\n\t"
      "v_mfma_f32_16x16x32_bf16 v[68:71], v[156:159], v[140:143], v[68:71]\n\t"
      "ds_read_b128 v[156:159], v231 offset:40960\n\t"
      "ds_read_b64_tr_b16 v[190:191], v237 offset:29184\n\t"
      "v_mfma_f32_16x16x32_bf16 v[72:75], v[172:175], v[124:127], v[72:75]\n\t"
      "v_exp_f32 v87, v87\n\t"
      "v_mfma_f32_16x16x32_bf16 v[76:79], v[172:175], v[140:143], v[76:79]\n\t"
      "ds_read_b128 v[172:175], v231 offset:45056\n\t"
      "v_cvt_pk_bf16_f32 v109, v86, v87\n\t"
      "v_mfma_f32_16x16x32_bf16 v[32:35], v[192:195], v[96:99], v[32:35]\n\t"
      "v_exp_f32 v92, v92\n\t"
      "v_mfma_f32_16x16x32_bf16 v[36:39], v[192:195], v[100:103], v[36:39]\n\t"
      "ds_read_b64_tr_b16 v[192:193], v236 offset:25600\n\t"
      "ds_read_b64_tr_b16 v[194:195], v236 offset:29696\n\t"
      "v_mfma_f32_16x16x32_bf16 v[40:43], v[196:199], v[96:99], v[40:43]\n\t"
      "v_exp_f32 v93, v93\n\t"
      "v_mfma_f32_16x16x32_bf16 v[44:47], v[196:199], v[100:103], v[44:47]\n\t"
      "ds_read_b64_tr_b16 v[196:197], v237 offset:25600\n\t"
      "v_cvt_pk_bf16_f32 v110, v92, v93\n\t"
      "v_mfma_f32_16x16x32_bf16 v[48:51], v[200:203], v[96:99], v[48:51]\n\t"
      "s_waitcnt lgkmcnt(15)\n\t"
      "v_exp_f32 v94, v94\n\t"
      "v_mfma_f32_16x16x32_bf16 v[52:55], v[200:203], v[100:103], v[52:55]\n\t"
      "ds_read_b64_tr_b16 v[198:199], v237 offset:29696\n\t"
      "ds_read_b64_tr_b16 v[200:201], v236 offset:26112\n\t"
      "v_mfma_f32_16x16x32_bf16 v[56:59], v[204:207], v[96:99], v[56:59]\n\t"
      "v_exp_f32 v95, v95\n\t"
      "v_mfma_f32_16x16x32_bf16 v[60:63], v[204:207], v[100:103], v[60:63]\n\t"
      "ds_read_b64_tr_b16 v[202:203], v236 offset:30208\n\t"
      "v_cvt_pk_bf16_f32 v111, v94, v95\n\t"
      "v_mfma_f32_16x16x32_bf16 v[216:219], v[224:227], v[96:99], v[216:219]\n\t"
      "s_barrier\n\t"
      "v_mfma_f32_16x16x32_bf16 v[220:223], v[224:227], v[100:103], v[220:223]\n\t"
      "v_mfma_f32_16x16x32_bf16 v[80:83], v[144:147], v[112:115], v[208:211]\n\t"
      "v_exp_f32 v64, v64\n\t"
      "v_mfma_f32_16x16x32_bf16 v[84:87], v[144:147], v[128:131], v[212:215]\n\t"
      "ds_read_b128 v[144:147], v228 offset:49152\n\t"
      "ds_read_b64_tr_b16 v[204:205], v237 offset:26112\n\t"
      "v_mfma_f32_16x16x32_bf16 v[88:91], v[160:163], v[112:115], v[208:211]\n\t"
      "v_exp_f32 v65, v65\n\t"
      "v_mfma_f32_16x16x32_bf16 v[92:95], v[160:163], v[128:131], v[212:215]\n\t"
      "ds_read_b128 v[160:163], v228 offset:53248\n\t"
      "v_cvt_pk_bf16_f32 v96, v64, v65\n\t"
      "v_mfma_f32_16x16x32_bf16 v[80:83], v[148:151], v[116:119], v[80:83]\n\t"
      "s_waitcnt lgkmcnt(15)\n\t"
      "v_exp_f32 v66, v66\n\t"
      "v_mfma_f32_16x16x32_bf16 v[84:87], v[148:151], v[132:135], v[84:87]\n\t"
      "ds_read_b128 v[148:151], v229 offset:49152\n\t"
      "ds_read_b64_tr_b16 v[206:207], v237 offset:30208\n\t"
      "v_mfma_f32_16x16x32_bf16 v[88:91], v[164:167], v[116:119], v[88:91]\n\t"
      "v_exp_f32 v67, v67\n\t"
      "v_mfma_f32_16x16x32_bf16 v[92:95], v[164:167], v[132:135], v[92:95]\n\t"
      "ds_read_b128 v[164:167], v229 offset:53248\n\t"
      "v_cvt_pk_bf16_f32 v97, v66, v67\n\t"
      "v_mfma_f32_16x16x32_bf16 v[0:3], v[176:179], v[104:107], v[0:3]\n\t"
      "v_exp_f32 v72, v72\n\t"
      "v_mfma_f32_16x16x32_bf16 v[4:7], v[176:179], v[108:111], v[4:7]\n\t"
      "ds_read_b64_tr_b16 v[176:177], v236 offset:32768\n\t"
      "ds_read_b64_tr_b16 v[178:179], v236 offset:36864\n\t"
      "v_mfma_f32_16x16x32_bf16 v[8:11], v[180:183], v[104:107], v[8:11]\n\t"
      "v_exp_f32 v73, v73\n\t"
      "v_mfma_f32_16x16x32_bf16 v[12:15], v[180:183], v[108:111], v[12:15]\n\t"
      "ds_read_b64_tr_b16 v[180:181], v237 offset:32768\n\t"
      "v_cvt_pk_bf16_f32 v98, v72, v73\n\t"
      "v_mfma_f32_16x16x32_bf16 v[16:19], v[184:187], v[104:107], v[16:19]\n\t"
      "s_waitcnt lgkmcnt(15)\n\t"
      "v_exp_f32 v74, v74\n\t"
      "v_mfma_f32_16x16x32_bf16 v[20:23], v[184:187], v[108:111], v[20:23]\n\t"
      "ds_read_b64_tr_b16 v[182:183], v237 offset:36864\n\t"
      "ds_read_b64_tr_b16 v[184:185], v236 offset:33280\n\t"
      "v_mfma_f32_16x16x32_bf16 v[24:27], v[188:191], v[104:107], v[24:27]\n\t"
      "v_exp_f32 v75, v75\n\t"
      "v_mfma_f32_16x16x32_bf16 v[28:31], v[188:191], v[108:111], v[28:31]\n\t"
      "ds_read_b64_tr_b16 v[186:187], v236 offset:37376\n\t"
      "v_cvt_pk_bf16_f32 v99, v74, v75\n\t"
      "v_mfma_f32_16x16x32_bf16 v[80:83], v[152:155], v[120:123], v[80:83]\n\t"
      "v_exp_f32 v68, v68\n\t"
      "v_mfma_f32_16x16x32_bf16 v[84:87], v[152:155], v[136:139], v[84:87]\n\t"
      "ds_read_b128 v[152:155], v230 offset:49152\n\t"
      "ds_read_b64_tr_b16 v[188:189], v237 offset:33280\n\t"
      "v_mfma_f32_16x16x32_bf16 v[88:91], v[168:171], v[120:123], v[88:91]\n\t"
      "v_exp_f32 v69, v69\n\t"
      "v_mfma_f32_16x16x32_bf16 v[92:95], v[168:171], v[136:139], v[92:95]\n\t"
      "ds_read_b128 v[168:171], v230 offset:53248\n\t"
      "v_cvt_pk_bf16_f32 v100, v68, v69\n\t"
      "v_mfma_f32_16x16x32_bf16 v[80:83], v[156:159], v[124:127], v[80:83]\n\t"
      "s_waitcnt lgkmcnt(15)\n\t"
      "v_exp_f32 v70, v70\n\t"
      "v_mfma_f32_16x16x32_bf16 v[84:87], v[156:159], v[140:143], v[84:87]\n\t"
      "ds_read_b128 v[156:159], v231 offset:49152\n\t"
      "ds_read_b64_tr_b16 v[190:191], v237 offset:37376\n\t"
      "v_mfma_f32_16x16x32_bf16 v[88:91], v[172:175], v[124:127], v[88:91]\n\t"
      "v_exp_f32 v71, v71\n\t"
      "v_mfma_f32_16x16x32_bf16 v[92:95], v[172:175], v[140:143], v[92:95]\n\t"
      "ds_read_b128 v[172:175], v231 offset:53248\n\t"
      "v_cvt_pk_bf16_f32 v101, v70, v71\n\t"
      "v_mfma_f32_16x16x32_bf16 v[32:35], v[192:195], v[104:107], v[32:35]\n\t"
      "v_exp_f32 v76, v76\n\t"
      "v_mfma_f32_16x16x32_bf16 v[36:39], v[192:195], v[108:111], v[36:39]\n\t"
      "ds_read_b64_tr_b16 v[192:193], v236 offset:33792\n\t"
      "ds_read_b64_tr_b16 v[194:195], v236 offset:37888\n\t"
      "v_mfma_f32_16x16x32_bf16 v[40:43], v[196:199], v[104:107], v[40:43]\n\t"
      "v_exp_f32 v77, v77\n\t"
      "v_mfma_f32_16x16x32_bf16 v[44:47], v[196:199], v[108:111], v[44:47]\n\t"
      "ds_read_b64_tr_b16 v[196:197], v237 offset:33792\n\t"
      "v_cvt_pk_bf16_f32 v102, v76, v77\n\t"
      "v_mfma_f32_16x16x32_bf16 v[48:51], v[200:203], v[104:107], v[48:51]\n\t"
      "s_waitcnt lgkmcnt(15)\n\t"
      "v_exp_f32 v78, v78\n\t"
      "v_mfma_f32_16x16x32_bf16 v[52:55], v[200:203], v[108:111], v[52:55]\n\t"
      "ds_read_b64_tr_b16 v[198:199], v237 offset:37888\n\t"
      "ds_read_b64_tr_b16 v[200:201], v236 offset:34304\n\t"
      "v_mfma_f32_16x16x32_bf16 v[56:59], v[204:207], v[104:107], v[56:59]\n\t"
      "v_exp_f32 v79, v79\n\t"
      "v_mfma_f32_16x16x32_bf16 v[60:63], v[204:207], v[108:111], v[60:63]\n\t"
      "ds_read_b64_tr_b16 v[202:203], v236 offset:38400\n\t"
      "v_cvt_pk_bf16_f32 v103, v78, v79\n\t"
      "v_mfma_f32_16x16x32_bf16 v[216:219], v[224:227], v[104:107], v[216:219]\n\t"
      "v_mfma_f32_16x16x32_bf16 v[220:223], v[224:227], v[108:111], v[220:223]\n\t"
      "v_mfma_f32_16x16x32_bf16 v[64:67], v[144:147], v[112:115], v[208:211]\n\t"
      "v_exp_f32 v80, v80\n\t"
      "v_mfma_f32_16x16x32_bf16 v[68:71], v[144:147], v[128:131], v[212:215]\n\t"
      "ds_read_b128 v[144:147], v228 offset:57344\n\t"
      "ds_read_b64_tr_b16 v[204:205], v237 offset:34304\n\t"
      "v_mfma_f32_16x16x32_bf16 v[72:75], v[160:163], v[112:115], v[208:211]\n\t"
      "v_exp_f32 v81, v81\n\t"
      "v_mfma_f32_16x16x32_bf16 v[76:79], v[160:163], v[128:131], v[212:215]\n\t"
      "ds_read_b128 v[160:163], v228 offset:61440\n\t"
      "v_cvt_pk_bf16_f32 v104, v80, v81\n\t"
      "v_mfma_f32_16x16x32_bf16 v[64:67], v[148:151], v[116:119], v[64:67]\n\t"
      "s_waitcnt lgkmcnt(15)\n\t"
      "v_exp_f32 v82, v82\n\t"
      "v_mfma_f32_16x16x32_bf16 v[68:71], v[148:151], v[132:135], v[68:71]\n\t"
      "ds_read_b128 v[148:151], v229 offset:57344\n\t"
      "ds_read_b64_tr_b16 v[206:207], v237 offset:38400\n\t"
      "v_mfma_f32_16x16x32_bf16 v[72:75], v[164:167], v[116:119], v[72:75]\n\t"
      "v_exp_f32 v83, v83\n\t"
      "v_mfma_f32_16x16x32_bf16 v[76:79], v[164:167], v[132:135], v[76:79]\n\t"
      "ds_read_b128 v[164:167], v229 offset:61440\n\t"
      "v_cvt_pk_bf16_f32 v105, v82, v83\n\t"
      "v_mfma_f32_16x16x32_bf16 v[0:3], v[176:179], v[96:99], v[0:3]\n\t"
      "v_exp_f32 v88, v88\n\t"
      "v_mfma_f32_16x16x32_bf16 v[4:7], v[176:179], v[100:103], v[4:7]\n\t"
      "ds_read_b64_tr_b16 v[176:177], v236 offset:40960\n\t"
      "ds_read_b64_tr_b16 v[178:179], v236 offset:45056\n\t"
      "v_mfma_f32_16x16x32_bf16 v[8:11], v[180:183], v[96:99], v[8:11]\n\t"
      "v_exp_f32 v89, v89\n\t"
      "v_mfma_f32_16x16x32_bf16 v[12:15], v[180:183], v[100:103], v[12:15]\n\t"
      "ds_read_b64_tr_b16 v[180:181], v237 offset:40960\n\t"
      "v_cvt_pk_bf16_f32 v106, v88, v89\n\t"
      "v_mfma_f32_16x16x32_bf16 v[16:19], v[184:187], v[96:99], v[16:19]\n\t"
      "s_waitcnt lgkmcnt(15)\n\t"
      "v_exp_f32 v90, v90\n\t"
      "v_mfma_f32_16x16x32_bf16 v[20:23], v[184:187], v[100:103], v[20:23]\n\t"
      "ds_read_b64_tr_b16 v[182:183], v237 offset:45056\n\t"
      "ds_read_b64_tr_b16 v[184:185], v236 offset:41472\n\t"
      "v_mfma_f32_16x16x32_bf16 v[24:27], v[188:191], v[96:99], v[24:27]\n\t"
      "v_exp_f32 v91, v91\n\t"
      "v_mfma_f32_16x16x32_bf16 v[28:31], v[188:191], v[100:103], v[28:31]\n\t"
      "ds_read_b64_tr_b16 v[186:187], v236 offset:45568\n\t"
      "v_cvt_pk_bf16_f32 v107, v90, v91\n\t"
      "v_mfma_f32_16x16x32_bf16 v[64:67], v[152:155], v[120:123], v[64:67]\n\t"
      "v_exp_f32 v84, v84\n\t"
      "v_mfma_f32_16x16x32_bf16 v[68:71], v[152:155], v[136:139], v[68:71]\n\t"
      "ds_read_b128 v[152:155], v230 offset:57344\n\t"
      "ds_read_b64_tr_b16 v[188:189], v237 offset:41472\n\t"
      "v_mfma_f32_16x16x32_bf16 v[72:75], v[168:171], v[120:123], v[72:75]\n\t"
      "v_exp_f32 v85, v85\n\t"
      "v_mfma_f32_16x16x32_bf16 v[76:79], v[168:171], v[136:139], v[76:79]\n\t"
      "ds_read_b128 v[168:171], v230 offset:61440\n\t"
      "v_cvt_pk_bf16_f32 v108, v84, v85\n\t"
      "v_mfma_f32_16x16x32_bf16 v[64:67], v[156:159], v[124:127], v[64:67]\n\t"
      "s_waitcnt lgkmcnt(15)\n\t"
      "v_exp_f32 v86, v86\n\t"
      "v_mfma_f32_16x16x32_bf16 v[68:71], v[156:159], v[140:143], v[68:71]\n\t"
      "ds_read_b128 v[156:159], v231 offset:57344\n\t"
      "ds_read_b64_tr_b16 v[190:191], v237 offset:45568\n\t"
      "v_mfma_f32_16x16x32_bf16 v[72:75], v[172:175], v[124:127], v[72:75]\n\t"
      "v_exp_f32 v87, v87\n\t"
      "v_mfma_f32_16x16x32_bf16 v[76:79], v[172:175], v[140:143], v[76:79]\n\t"
      "ds_read_b128 v[172:175], v231 offset:61440\n\t"
      "v_cvt_pk_bf16_f32 v109, v86, v87\n\t"
      "v_mfma_f32_16x16x32_bf16 v[32:35], v[192:195], v[96:99], v[32:35]\n\t"
      "v_exp_f32 v92, v92\n\t"
      "v_mfma_f32_16x16x32_bf16 v[36:39], v[192:195], v[100:103], v[36:39]\n\t"
      "ds_read_b64_tr_b16 v[192:193], v236 offset:41984\n\t"
      "ds_read_b64_tr_b16 v[194:195], v236 offset:46080\n\t"
      "v_mfma_f32_16x16x32_bf16 v[40:43], v[196:199], v[96:99], v[40:43]\n\t"
      "v_exp_f32 v93, v93\n\t"
      "v_mfma_f32_16x16x32_bf16 v[44:47], v[196:199], v[100:103], v[44:47]\n\t"
      "ds_read_b64_tr_b16 v[196:197], v237 offset:41984\n\t"
      "v_cvt_pk_bf16_f32 v110, v92, v93\n\t"
      "v_mfma_f32_16x16x32_bf16 v[48:51], v[200:203], v[96:99], v[48:51]\n\t"
      "s_waitcnt lgkmcnt(15)\n\t"
      "v_exp_f32 v94, v94\n\t"
      "v_mfma_f32_16x16x32_bf16 v[52:55], v[200:203], v[100:103], v[52:55]\n\t"
      "ds_read_b64_tr_b16 v[198:199], v237 offset:46080\n\t"
      "ds_read_b64_tr_b16 v[200:201], v236 offset:42496\n\t"
      "v_mfma_f32_16x16x32_bf16 v[56:59], v[204:207], v[96:99], v[56:59]\n\t"
      "v_exp_f32 v95, v95\n\t"
      "v_mfma_f32_16x16x32_bf16 v[60:63], v[204:207], v[100:103], v[60:63]\n\t"
      "ds_read_b64_tr_b16 v[202:203], v236 offset:46592\n\t"
      "v_cvt_pk_bf16_f32 v111, v94, v95\n\t"
      "v_mfma_f32_16x16x32_bf16 v[216:219], v[224:227], v[96:99], v[216:219]\n\t"
      "s_barrier\n\t"
      "v_mfma_f32_16x16x32_bf16 v[220:223], v[224:227], v[100:103], v[220:223]\n\t"
      "v_mfma_f32_16x16x32_bf16 v[80:83], v[144:147], v[112:115], v[208:211]\n\t"
      "v_exp_f32 v64, v64\n\t"
      "v_mfma_f32_16x16x32_bf16 v[84:87], v[144:147], v[128:131], v[212:215]\n\t"
      "ds_read_b128 v[144:147], v232 offset:0\n\t"
      "ds_read_b64_tr_b16 v[204:205], v237 offset:42496\n\t"
      "v_mfma_f32_16x16x32_bf16 v[88:91], v[160:163], v[112:115], v[208:211]\n\t"
      "v_exp_f32 v65, v65\n\t"
      "v_mfma_f32_16x16x32_bf16 v[92:95], v[160:163], v[128:131], v[212:215]\n\t"
      "ds_read_b128 v[160:163], v232 offset:4096\n\t"
      "v_cvt_pk_bf16_f32 v96, v64, v65\n\t"
      "v_mfma_f32_16x16x32_bf16 v[80:83], v[148:151], v[116:119], v[80:83]\n\t"
      "s_waitcnt lgkmcnt(15)\n\t"
      "v_exp_f32 v66, v66\n\t"
      "v_mfma_f32_16x16x32_bf16 v[84:87], v[148:151], v[132:135], v[84:87]\n\t"
      "ds_read_b128 v[148:151], v233 offset:0\n\t"
      "ds_read_b64_tr_b16 v[206:207], v237 offset:46592\n\t"
      "v_mfma_f32_16x16x32_bf16 v[88:91], v[164:167], v[116:119], v[88:91]\n\t"
      "v_exp_f32 v67, v67\n\t"
      "v_mfma_f32_16x16x32_bf16 v[92:95], v[164:167], v[132:135], v[92:95]\n\t"
      "ds_read_b128 v[164:167], v233 offset:4096\n\t"
      "v_cvt_pk_bf16_f32 v97, v66, v67\n\t"
      "v_mfma_f32_16x16x32_bf16 v[0:3], v[176:179], v[104:107], v[0:3]\n\t"
      "v_exp_f32 v72, v72\n\t"
      "v_mfma_f32_16x16x32_bf16 v[4:7], v[176:179], v[108:111], v[4:7]\n\t"
      "ds_read_b64_tr_b16 v[176:177], v236 offset:49152\n\t"
      "ds_read_b64_tr_b16 v[178:179], v236 offset:53248\n\t"
      "v_mfma_f32_16x16x32_bf16 v[8:11], v[180:183], v[104:107], v[8:11]\n\t"
      "v_exp_f32 v73, v73\n\t"
      "v_mfma_f32_16x16x32_bf16 v[12:15], v[180:183], v[108:111], v[12:15]\n\t"
      "ds_read_b64_tr_b16 v[180:181], v237 offset:49152\n\t"
      "v_cvt_pk_bf16_f32 v98, v72, v73\n\t"
      "v_mfma_f32_16x16x32_bf16 v[16:19], v[184:187], v[104:107], v[16:19]\n\t"
      "s_waitcnt lgkmcnt(15)\n\t"
      "v_exp_f32 v74, v74\n\t"
      "v_mfma_f32_16x16x32_bf16 v[20:23], v[184:187], v[108:111], v[20:23]\n\t"
      "ds_read_b64_tr_b16 v[182:183], v237 offset:53248\n\t"
      "ds_read_b64_tr_b16 v[184:185], v236 offset:49664\n\t"
      "v_mfma_f32_16x16x32_bf16 v[24:27], v[188:191], v[104:107], v[24:27]\n\t"
      "v_exp_f32 v75, v75\n\t"
      "v_mfma_f32_16x16x32_bf16 v[28:31], v[188:191], v[108:111], v[28:31]\n\t"
      "ds_read_b64_tr_b16 v[186:187], v236 offset:53760\n\t"
      "v_cvt_pk_bf16_f32 v99, v74, v75\n\t"
      "v_mfma_f32_16x16x32_bf16 v[80:83], v[152:155], v[120:123], v[80:83]\n\t"
      "v_exp_f32 v68, v68\n\t"
      "v_mfma_f32_16x16x32_bf16 v[84:87], v[152:155], v[136:139], v[84:87]\n\t"
      "ds_read_b128 v[152:155], v234 offset:0\n\t"
      "ds_read_b64_tr_b16 v[188:189], v237 offset:49664\n\t"
      "v_mfma_f32_16x16x32_bf16 v[88:91], v[168:171], v[120:123], v[88:91]\n\t"
      "v_exp_f32 v69, v69\n\t"
      "v_mfma_f32_16x16x32_bf16 v[92:95], v[168:171], v[136:139], v[92:95]\n\t"
      "ds_read_b128 v[168:171], v234 offset:4096\n\t"
      "v_cvt_pk_bf16_f32 v100, v68, v69\n\t"
      "v_mfma_f32_16x16x32_bf16 v[80:83], v[156:159], v[124:127], v[80:83]\n\t"
      "s_waitcnt lgkmcnt(15)\n\t"
      "v_exp_f32 v70, v70\n\t"
      "v_mfma_f32_16x16x32_bf16 v[84:87], v[156:159], v[140:143], v[84:87]\n\t"
      "ds_read_b128 v[156:159], v235 offset:0\n\t"
      "ds_read_b64_tr_b16 v[190:191], v237 offset:53760\n\t"
      "v_mfma_f32_16x16x32_bf16 v[88:91], v[172:175], v[124:127], v[88:91]\n\t"
      "v_exp_f32 v71, v71\n\t"
      "v_mfma_f32_16x16x32_bf16 v[92:95], v[172:175], v[140:143], v[92:95]\n\t"
      "ds_read_b128 v[172:175], v235 offset:4096\n\t"
      "v_cvt_pk_bf16_f32 v101, v70, v71\n\t"
      "v_mfma_f32_16x16x32_bf16 v[32:35], v[192:195], v[104:107], v[32:35]\n\t"
      "v_exp_f32 v76, v76\n\t"
      "v_mfma_f32_16x16x32_bf16 v[36:39], v[192:195], v[108:111], v[36:39]\n\t"
      "ds_read_b64_tr_b16 v[192:193], v236 offset:50176\n\t"
      "ds_read_b64_tr_b16 v[194:195], v236 offset:54272\n\t"
      "v_mfma_f32_16x16x32_bf16 v[40:43], v[196:199], v[104:107], v[40:43]\n\t"
      "v_exp_f32 v77, v77\n\t"
      "v_mfma_f32_16x16x32_bf16 v[44:47], v[196:199], v[108:111], v[44:47]\n\t"
      "ds_read_b64_tr_b16 v[196:197], v237 offset:50176\n\t"
      "v_cvt_pk_bf16_f32 v102, v76, v77\n\t"
      "v_mfma_f32_16x16x32_bf16 v[48:51], v[200:203], v[104:107], v[48:51]\n\t"
      "s_waitcnt lgkmcnt(15)\n\t"
      "v_exp_f32 v78, v78\n\t"
      "v_mfma_f32_16x16x32_bf16 v[52:55], v[200:203], v[108:111], v[52:55]\n\t"
      "ds_read_b64_tr_b16 v[198:199], v237 offset:54272\n\t"
      "ds_read_b64_tr_b16 v[200:201], v236 offset:50688\n\t"
      "v_mfma_f32_16x16x32_bf16 v[56:59], v[204:207], v[104:107], v[56:59]\n\t"
      "v_exp_f32 v79, v79\n\t"
      "v_mfma_f32_16x16x32_bf16 v[60:63], v[204:207], v[108:111], v[60:63]\n\t"
      "ds_read_b64_tr_b16 v[202:203], v236 offset:54784\n\t"
      "v_cvt_pk_bf16_f32 v103, v78, v79\n\t"
      "v_mfma_f32_16x16x32_bf16 v[216:219], v[224:227], v[104:107], v[216:219]\n\t"
      "v_mfma_f32_16x16x32_bf16 v[220:223], v[224:227], v[108:111], v[220:223]\n\t"
      "v_mfma_f32_16x16x32_bf16 v[64:67], v[144:147], v[112:115], v[208:211]\n\t"
      "v_exp_f32 v80, v80\n\t"
      "v_mfma_f32_16x16x32_bf16 v[68:71], v[144:147], v[128:131], v[212:215]\n\t"
      "ds_read_b128 v[144:147], v232 offset:8192\n\t"
      "ds_read_b64_tr_b16 v[204:205], v237 offset:50688\n\t"
      "v_mfma_f32_16x16x32_bf16 v[72:75], v[160:163], v[112:115], v[208:211]\n\t"
      "v_exp_f32 v81, v81\n\t"
      "v_mfma_f32_16x16x32_bf16 v[76:79], v[160:163], v[128:131], v[212:215]\n\t"
      "ds_read_b128 v[160:163], v232 offset:12288\n\t"
      "v_cvt_pk_bf16_f32 v104, v80, v81\n\t"
      "v_mfma_f32_16x16x32_bf16 v[64:67], v[148:151], v[116:119], v[64:67]\n\t"
      "s_waitcnt lgkmcnt(15)\n\t"
      "v_exp_f32 v82, v82\n\t"
      "v_mfma_f32_16x16x32_bf16 v[68:71], v[148:151], v[132:135], v[68:71]\n\t"
      "ds_read_b128 v[148:151], v233 offset:8192\n\t"
      "ds_read_b64_tr_b16 v[206:207], v237 offset:54784\n\t"
      "v_mfma_f32_16x16x32_bf16 v[72:75], v[164:167], v[116:119], v[72:75]\n\t"
      "v_exp_f32 v83, v83\n\t"
      "v_mfma_f32_16x16x32_bf16 v[76:79], v[164:167], v[132:135], v[76:79]\n\t"
      "ds_read_b128 v[164:167], v233 offset:12288\n\t"
      "v_cvt_pk_bf16_f32 v105, v82, v83\n\t"
      "v_mfma_f32_16x16x32_bf16 v[0:3], v[176:179], v[96:99], v[0:3]\n\t"
      "v_exp_f32 v88, v88\n\t"
      "v_mfma_f32_16x16x32_bf16 v[4:7], v[176:179], v[100:103], v[4:7]\n\t"
      "ds_read_b64_tr_b16 v[176:177], v236 offset:57344\n\t"
      "ds_read_b64_tr_b16 v[178:179], v236 offset:61440\n\t"
      "v_mfma_f32_16x16x32_bf16 v[8:11], v[180:183], v[96:99], v[8:11]\n\t"
      "v_exp_f32 v89, v89\n\t"
      "v_mfma_f32_16x16x32_bf16 v[12:15], v[180:183], v[100:103], v[12:15]\n\t"
      "ds_read_b64_tr_b16 v[180:181], v237 offset:57344\n\t"
      "v_cvt_pk_bf16_f32 v106, v88, v89\n\t"
      "v_mfma_f32_16x16x32_bf16 v[16:19], v[184:187], v[96:99], v[16:19]\n\t"
      "s_waitcnt lgkmcnt(15)\n\t"
      "v_exp_f32 v90, v90\n\t"
      "v_mfma_f32_16x16x32_bf16 v[20:23], v[184:187], v[100:103], v[20:23]\n\t"
      "ds_read_b64_tr_b16 v[182:183], v237 offset:61440\n\t"
      "ds_read_b64_tr_b16 v[184:185], v236 offset:57856\n\t"
      "v_mfma_f32_16x16x32_bf16 v[24:27], v[188:191], v[96:99], v[24:27]\n\t"
      "v_exp_f32 v91, v91\n\t"
      "v_mfma_f32_16x16x32_bf16 v[28:31], v[188:191], v[100:103], v[28:31]\n\t"
      "ds_read_b64_tr_b16 v[186:187], v236 offset:61952\n\t"
      "v_cvt_pk_bf16_f32 v107, v90, v91\n\t"
      "v_mfma_f32_16x16x32_bf16 v[64:67], v[152:155], v[120:123], v[64:67]\n\t"
      "v_exp_f32 v84, v84\n\t"
      "v_mfma_f32_16x16x32_bf16 v[68:71], v[152:155], v[136:139], v[68:71]\n\t"
      "ds_read_b128 v[152:155], v234 offset:8192\n\t"
      "ds_read_b64_tr_b16 v[188:189], v237 offset:57856\n\t"
      "v_mfma_f32_16x16x32_bf16 v[72:75], v[168:171], v[120:123], v[72:75]\n\t"
      "v_exp_f32 v85, v85\n\t"
      "v_mfma_f32_16x16x32_bf16 v[76:79], v[168:171], v[136:139], v[76:79]\n\t"
      "ds_read_b128 v[168:171], v234 offset:12288\n\t"
      "v_cvt_pk_bf16_f32 v108, v84, v85\n\t"
      "v_mfma_f32_16x16x32_bf16 v[64:67], v[156:159], v[124:127], v[64:67]\n\t"
      "s_waitcnt lgkmcnt(15)\n\t"
      "v_exp_f32 v86, v86\n\t"
      "v_mfma_f32_16x16x32_bf16 v[68:71], v[156:159], v[140:143], v[68:71]\n\t"
      "ds_read_b128 v[156:159], v235 offset:8192\n\t"
      "ds_read_b64_tr_b16 v[190:191], v237 offset:61952\n\t"
      "v_mfma_f32_16x16x32_bf16 v[72:75], v[172:175], v[124:127], v[72:75]\n\t"
      "v_exp_f32 v87, v87\n\t"
      "v_mfma_f32_16x16x32_bf16 v[76:79], v[172:175], v[140:143], v[76:79]\n\t"
      "ds_read_b128 v[172:175], v235 offset:12288\n\t"
      "v_cvt_pk_bf16_f32 v109, v86, v87\n\t"
      "v_mfma_f32_16x16x32_bf16 v[32:35], v[192:195], v[96:99], v[32:35]\n\t"
      "v_exp_f32 v92, v92\n\t"
      "v_mfma_f32_16x16x32_bf16 v[36:39], v[192:195], v[100:103], v[36:39]\n\t"
      "ds_read_b64_tr_b16 v[192:193], v236 offset:58368\n\t"
      "ds_read_b64_tr_b16 v[194:195], v236 offset:62464\n\t"
      "v_mfma_f32_16x16x32_bf16 v[40:43], v[196:199], v[96:99], v[40:43]\n\t"
      "v_exp_f32 v93, v93\n\t"
      "v_mfma_f32_16x16x32_bf16 v[44:47], v[196:199], v[100:103], v[44:47]\n\t"
      "ds_read_b64_tr_b16 v[196:197], v237 offset:58368\n\t"
      "v_cvt_pk_bf16_f32 v110, v92, v93\n\t"
      "v_mfma_f32_16x16x32_bf16 v[48:51], v[200:203], v[96:99], v[48:51]\n\t"
      "s_waitcnt lgkmcnt(15)\n\t"
      "v_exp_f32 v94, v94\n\t"
      "v_mfma_f32_16x16x32_bf16 v[52:55], v[200:203], v[100:103], v[52:55]\n\t"
      "ds_read_b64_tr_b16 v[198:199], v237 offset:62464\n\t"
      "ds_read_b64_tr_b16 v[200:201], v236 offset:58880\n\t"
      "v_mfma_f32_16x16x32_bf16 v[56:59], v[204:207], v[96:99], v[56:59]\n\t"
      "v_exp_f32 v95, v95\n\t"
      "v_mfma_f32_16x16x32_bf16 v[60:63], v[204:207], v[100:103], v[60:63]\n\t"
      "ds_read_b64_tr_b16 v[202:203], v236 offset:62976\n\t"
      "v_cvt_pk_bf16_f32 v111, v94, v95\n\t"
      "v_mfma_f32_16x16x32_bf16 v[216:219], v[224:227], v[96:99], v[216:219]\n\t"
      "s_barrier\n\t"
      "v_mfma_f32_16x16x32_bf16 v[220:223], v[224:227], v[100:103], v[220:223]\n\t"
      "v_mfma_f32_16x16x32_bf16 v[80:83], v[144:147], v[112:115], v[208:211]\n\t"
      "v_exp_f32 v64, v64\n\t"
      "v_mfma_f32_16x16x32_bf16 v[84:87], v[144:147], v[128:131], v[212:215]\n\t"
      "ds_read_b128 v[144:147], v228 offset:0\n\t"
      "ds_read_b64_tr_b16 v[204:205], v237 offset:58880\n\t"
      "v_mfma_f32_16x16x32_bf16 v[88:91], v[160:163], v[112:115], v[208:211]\n\t"
      "v_exp_f32 v65, v65\n\t"
      "v_mfma_f32_16x16x32_bf16 v[92:95], v[160:163], v[128:131], v[212:215]\n\t"
      "ds_read_b128 v[160:163], v228 offset:4096\n\t"
      "v_cvt_pk_bf16_f32 v96, v64, v65\n\t"
      "v_mfma_f32_16x16x32_bf16 v[80:83], v[148:151], v[116:119], v[80:83]\n\t"
      "s_waitcnt lgkmcnt(15)\n\t"
      "v_exp_f32 v66, v66\n\t"
      "v_mfma_f32_16x16x32_bf16 v[84:87], v[148:151], v[132:135], v[84:87]\n\t"
      "ds_read_b128 v[148:151], v229 offset:0\n\t"
      "ds_read_b64_tr_b16 v[206:207], v237 offset:62976\n\t"
      "v_mfma_f32_16x16x32_bf16 v[88:91], v[164:167], v[116:119], v[88:91]\n\t"
      "v_exp_f32 v67, v67\n\t"
      "v_mfma_f32_16x16x32_bf16 v[92:95], v[164:167], v[132:135], v[92:95]\n\t"
      "ds_read_b128 v[164:167], v229 offset:4096\n\t"
      "v_cvt_pk_bf16_f32 v97, v66, v67\n\t"
      "v_mfma_f32_16x16x32_bf16 v[0:3], v[176:179], v[104:107], v[0:3]\n\t"
      "v_exp_f32 v72, v72\n\t"
      "v_mfma_f32_16x16x32_bf16 v[4:7], v[176:179], v[108:111], v[4:7]\n\t"
      "ds_read_b64_tr_b16 v[176:177], v238 offset:0\n\t"
      "ds_read_b64_tr_b16 v[178:179], v238 offset:4096\n\t"
      "v_mfma_f32_16x16x32_bf16 v[8:11], v[180:183], v[104:107], v[8:11]\n\t"
      "v_exp_f32 v73, v73\n\t"
      "v_mfma_f32_16x16x32_bf16 v[12:15], v[180:183], v[108:111], v[12:15]\n\t"
      "ds_read_b64_tr_b16 v[180:181], v239 offset:0\n\t"
      "v_cvt_pk_bf16_f32 v98, v72, v73\n\t"
      "v_mfma_f32_16x16x32_bf16 v[16:19], v[184:187], v[104:107], v[16:19]\n\t"
      "s_waitcnt lgkmcnt(15)\n\t"
      "v_exp_f32 v74, v74\n\t"
      "v_mfma_f32_16x16x32_bf16 v[20:23], v[184:187], v[108:111], v[20:23]\n\t"
      "ds_read_b64_tr_b16 v[182:183], v239 offset:4096\n\t"
      "ds_read_b64_tr_b16 v[184:185], v238 offset:512\n\t"
      "v_mfma_f32_16x16x32_bf16 v[24:27], v[188:191], v[104:107], v[24:27]\n\t"
      "v_exp_f32 v75, v75\n\t"
      "v_mfma_f32_16x16x32_bf16 v[28:31], v[188:191], v[108:111], v[28:31]\n\t"
      "ds_read_b64_tr_b16 v[186:187], v238 offset:4608\n\t"
      "v_cvt_pk_bf16_f32 v99, v74, v75\n\t"
      "v_mfma_f32_16x16x32_bf16 v[80:83], v[152:155], v[120:123], v[80:83]\n\t"
      "v_exp_f32 v68, v68\n\t"
      "v_mfma_f32_16x16x32_bf16 v[84:87], v[152:155], v[136:139], v[84:87]\n\t"
      "ds_read_b128 v[152:155], v230 offset:0\n\t"
      "ds_read_b64_tr_b16 v[188:189], v239 offset:512\n\t"
      "v_mfma_f32_16x16x32_bf16 v[88:91], v[168:171], v[120:123], v[88:91]\n\t"
      "v_exp_f32 v69, v69\n\t"
      "v_mfma_f32_16x16x32_bf16 v[92:95], v[168:171], v[136:139], v[92:95]\n\t"
      "ds_read_b128 v[168:171], v230 offset:4096\n\t"
      "v_cvt_pk_bf16_f32 v100, v68, v69\n\t"
      "v_mfma_f32_16x16x32_bf16 v[80:83], v[156:159], v[124:127], v[80:83]\n\t"
      "s_waitcnt lgkmcnt(15)\n\t"
      "v_exp_f32 v70, v70\n\t"
      "v_mfma_f32_16x16x32_bf16 v[84:87], v[156:159], v[140:143], v[84:87]\n\t"
      "ds_read_b128 v[156:159], v231 offset:0\n\t"
      "ds_read_b64_tr_b16 v[190:191], v239 offset:4608\n\t"
      "v_mfma_f32_16x16x32_bf16 v[88:91], v[172:175], v[124:127], v[88:91]\n\t"
      "v_exp_f32 v71, v71\n\t"
      "v_mfma_f32_16x16x32_bf16 v[92:95], v[172:175], v[140:143], v[92:95]\n\t"
      "ds_read_b128 v[172:175], v231 offset:4096\n\t"
      "v_cvt_pk_bf16_f32 v101, v70, v71\n\t"
      "v_mfma_f32_16x16x32_bf16 v[32:35], v[192:195], v[104:107], v[32:35]\n\t"
      "v_exp_f32 v76, v76\n\t"
      "v_mfma_f32_16x16x32_bf16 v[36:39], v[192:195], v[108:111], v[36:39]\n\t"
      "ds_read_b64_tr_b16 v[192:193], v238 offset:1024\n\t"
      "ds_read_b64_tr_b16 v[194:195], v238 offset:5120\n\t"
      "v_mfma_f32_16x16x32_bf16 v[40:43], v[196:199], v[104:107], v[40:43]\n\t"
      "v_exp_f32 v77, v77\n\t"
      "v_mfma_f32_16x16x32_bf16 v[44:47], v[196:199], v[108:111], v[44:47]\n\t"
      "ds_read_b64_tr_b16 v[196:197], v239 offset:1024\n\t"
      "v_cvt_pk_bf16_f32 v102, v76, v77\n\t"
      "v_mfma_f32_16x16x32_bf16 v[48:51], v[200:203], v[104:107], v[48:51]\n\t"
      "s_waitcnt lgkmcnt(15)\n\t"
      "v_exp_f32 v78, v78\n\t"
      "v_mfma_f32_16x16x32_bf16 v[52:55], v[200:203], v[108:111], v[52:55]\n\t"
      "ds_read_b64_tr_b16 v[198:199], v239 offset:5120\n\t"
      "ds_read_b64_tr_b16 v[200:201], v238 offset:1536\n\t"
      "v_mfma_f32_16x16x32_bf16 v[56:59], v[204:207], v[104:107], v[56:59]\n\t"
      "v_exp_f32 v79, v79\n\t"
      "v_mfma_f32_16x16x32_bf16 v[60:63], v[204:207], v[108:111], v[60:63]\n\t"
      "ds_read_b64_tr_b16 v[202:203], v238 offset:5632\n\t"
      "v_cvt_pk_bf16_f32 v103, v78, v79\n\t"
      "v_mfma_f32_16x16x32_bf16 v[216:219], v[224:227], v[104:107], v[216:219]\n\t"
      "v_mfma_f32_16x16x32_bf16 v[220:223], v[224:227], v[108:111], v[220:223]\n\t"
      "v_mfma_f32_16x16x32_bf16 v[64:67], v[144:147], v[112:115], v[208:211]\n\t"
      "v_exp_f32 v80, v80\n\t"
      "v_mfma_f32_16x16x32_bf16 v[68:71], v[144:147], v[128:131], v[212:215]\n\t"
      "ds_read_b128 v[144:147], v228 offset:8192\n\t"
      "ds_read_b64_tr_b16 v[204:205], v239 offset:1536\n\t"
      "v_mfma_f32_16x16x32_bf16 v[72:75], v[160:163], v[112:115], v[208:211]\n\t"
      "v_exp_f32 v81, v81\n\t"
      "v_mfma_f32_16x16x32_bf16 v[76:79], v[160:163], v[128:131], v[212:215]\n\t"
      "ds_read_b128 v[160:163], v228 offset:12288\n\t"
      "v_cvt_pk_bf16_f32 v104, v80, v81\n\t"
      "v_mfma_f32_16x16x32_bf16 v[64:67], v[148:151], v[116:119], v[64:67]\n\t"
      "s_waitcnt lgkmcnt(15)\n\t"
      "v_exp_f32 v82, v82\n\t"
      "v_mfma_f32_16x16x32_bf16 v[68:71], v[148:151], v[132:135], v[68:71]\n\t"
      "ds_read_b128 v[148:151], v229 offset:8192\n\t"
      "ds_read_b64_tr_b16 v[206:207], v239 offset:5632\n\t"
      "v_mfma_f32_16x16x32_bf16 v[72:75], v[164:167], v[116:119], v[72:75]\n\t"
      "v_exp_f32 v83, v83\n\t"
      "v_mfma_f32_16x16x32_bf16 v[76:79], v[164:167], v[132:135], v[76:79]\n\t"
      "ds_read_b128 v[164:167], v229 offset:12288\n\t"
      "v_cvt_pk_bf16_f32 v105, v82, v83\n\t"
      "v_mfma_f32_16x16x32_bf16 v[0:3], v[176:179], v[96:99], v[0:3]\n\t"
      "v_exp_f32 v88, v88\n\t"
      "v_mfma_f32_16x16x32_bf16 v[4:7], v[176:179], v[100:103], v[4:7]\n\t"
      "ds_read_b64_tr_b16 v[176:177], v238 offset:8192\n\t"
      "ds_read_b64_tr_b16 v[178:179], v238 offset:12288\n\t"
      "v_mfma_f32_16x16x32_bf16 v[8:11], v[180:183], v[96:99], v[8:11]\n\t"
      "v_exp_f32 v89, v89\n\t"
      "v_mfma_f32_16x16x32_bf16 v[12:15], v[180:183], v[100:103], v[12:15]\n\t"
      "ds_read_b64_tr_b16 v[180:181], v239 offset:8192\n\t"
      "v_cvt_pk_bf16_f32 v106, v88, v89\n\t"
      "v_mfma_f32_16x16x32_bf16 v[16:19], v[184:187], v[96:99], v[16:19]\n\t"
      "s_waitcnt lgkmcnt(15)\n\t"
      "v_exp_f32 v90, v90\n\t"
      "v_mfma_f32_16x16x32_bf16 v[20:23], v[184:187], v[100:103], v[20:23]\n\t"
      "ds_read_b64_tr_b16 v[182:183], v239 offset:12288\n\t"
      "ds_read_b64_tr_b16 v[184:185], v238 offset:8704\n\t"
      "v_mfma_f32_16x16x32_bf16 v[24:27], v[188:191], v[96:99], v[24:27]\n\t"
      "v_exp_f32 v91, v91\n\t"
      "v_mfma_f32_16x16x32_bf16 v[28:31], v[188:191], v[100:103], v[28:31]\n\t"
      "ds_read_b64_tr_b16 v[186:187], v238 offset:12800\n\t"
      "v_cvt_pk_bf16_f32 v107, v90, v91\n\t"
      "v_mfma_f32_16x16x32_bf16 v[64:67], v[152:155], v[120:123], v[64:67]\n\t"
      "v_exp_f32 v84, v84\n\t"
      "v_mfma_f32_16x16x32_bf16 v[68:71], v[152:155], v[136:139], v[68:71]\n\t"
      "ds_read_b128 v[152:155], v230 offset:8192\n\t"
      "ds_read_b64_tr_b16 v[188:189], v239 offset:8704\n\t"
      "v_mfma_f32_16x16x32_bf16 v[72:75], v[168:171], v[120:123], v[72:75]\n\t"
      "v_exp_f32 v85, v85\n\t"
      "v_mfma_f32_16x16x32_bf16 v[76:79], v[168:171], v[136:139], v[76:79]\n\t"
      "ds_read_b128 v[168:171], v230 offset:12288\n\t"
      "v_cvt_pk_bf16_f32 v108, v84, v85\n\t"
      "v_mfma_f32_16x16x32_bf16 v[64:67], v[156:159], v[124:127], v[64:67]\n\t"
      "s_waitcnt lgkmcnt(15)\n\t"
      "v_exp_f32 v86, v86\n\t"
      "v_mfma_f32_16x16x32_bf16 v[68:71], v[156:159], v[140:143], v[68:71]\n\t"
      "ds_read_b128 v[156:159], v231 offset:8192\n\t"
      "ds_read_b64_tr_b16 v[190:191], v239 offset:12800\n\t"
      "v_mfma_f32_16x16x32_bf16 v[72:75], v[172:175], v[124:127], v[72:75]\n\t"
      "v_exp_f32 v87, v87\n\t"
      "v_mfma_f32_16x16x32_bf16 v[76:79], v[172:175], v[140:143], v[76:79]\n\t"
      "ds_read_b128 v[172:175], v231 offset:12288\n\t"
      "v_cvt_pk_bf16_f32 v109, v86, v87\n\t"
      "v_mfma_f32_16x16x32_bf16 v[32:35], v[192:195], v[96:99], v[32:35]\n\t"
      "v_exp_f32 v92, v92\n\t"
      "v_mfma_f32_16x16x32_bf16 v[36:39], v[192:195], v[100:103], v[36:39]\n\t"
      "ds_read_b64_tr_b16 v[192:193], v238 offset:9216\n\t"
      "ds_read_b64_tr_b16 v[194:195], v238 offset:13312\n\t"
      "v_mfma_f32_16x16x32_bf16 v[40:43], v[196:199], v[96:99], v[40:43]\n\t"
      "v_exp_f32 v93, v93\n\t"
      "v_mfma_f32_16x16x32_bf16 v[44:47], v[196:199], v[100:103], v[44:47]\n\t"
      "ds_read_b64_tr_b16 v[196:197], v239 offset:9216\n\t"
      "v_cvt_pk_bf16_f32 v110, v92, v93\n\t"
      "v_mfma_f32_16x16x32_bf16 v[48:51], v[200:203], v[96:99], v[48:51]\n\t"
      "s_waitcnt lgkmcnt(15)\n\t"
      "v_exp_f32 v94, v94\n\t"
      "v_mfma_f32_16x16x32_bf16 v[52:55], v[200:203], v[100:103], v[52:55]\n\t"
      "ds_read_b64_tr_b16 v[198:199], v239 offset:13312\n\t"
      "ds_read_b64_tr_b16 v[200:201], v238 offset:9728\n\t"
      "v_mfma_f32_16x16x32_bf16 v[56:59], v[204:207], v[96:99], v[56:59]\n\t"
      "v_exp_f32 v95, v95\n\t"
      "v_mfma_f32_16x16x32_bf16 v[60:63], v[204:207], v[100:103], v[60:63]\n\t"
      "ds_read_b64_tr_b16 v[202:203], v238 offset:13824\n\t"
      "v_cvt_pk_bf16_f32 v111, v94, v95\n\t"
      "v_mfma_f32_16x16x32_bf16 v[216:219], v[224:227], v[96:99], v[216:219]\n\t"
      "s_barrier\n\t"
      "v_mfma_f32_16x16x32_bf16 v[220:223], v[224:227], v[100:103], v[220:223]\n\t"
      "v_mfma_f32_16x16x32_bf16 v[80:83], v[144:147], v[112:115], v[208:211]\n\t"
      "v_exp_f32 v64, v64\n\t"
      "v_mfma_f32_16x16x32_bf16 v[84:87], v[144:147], v[128:131], v[212:215]\n\t"
      "ds_read_b128 v[144:147], v228 offset:16384\n\t"
      "ds_read_b64_tr_b16 v[204:205], v239 offset:9728\n\t"
      "v_mfma_f32_16x16x32_bf16 v[88:91], v[160:163], v[112:115], v[208:211]\n\t"
      "v_exp_f32 v65, v65\n\t"
      "v_mfma_f32_16x16x32_bf16 v[92:95], v[160:163], v[128:131], v[212:215]\n\t"
      "ds_read_b128 v[160:163], v228 offset:20480\n\t"
      "v_cvt_pk_bf16_f32 v96, v64, v65\n\t"
      "v_mfma_f32_16x16x32_bf16 v[80:83], v[148:151], v[116:119], v[80:83]\n\t"
      "s_waitcnt lgkmcnt(15)\n\t"
      "v_exp_f32 v66, v66\n\t"
      "v_mfma_f32_16x16x32_bf16 v[84:87], v[148:151], v[132:135], v[84:87]\n\t"
      "ds_read_b128 v[148:151], v229 offset:16384\n\t"
      "ds_read_b64_tr_b16 v[206:207], v239 offset:13824\n\t"
      "v_mfma_f32_16x16x32_bf16 v[88:91], v[164:167], v[116:119], v[88:91]\n\t"
      "v_exp_f32 v67, v67\n\t"
      "v_mfma_f32_16x16x32_bf16 v[92:95], v[164:167], v[132:135], v[92:95]\n\t"
      "ds_read_b128 v[164:167], v229 offset:20480\n\t"
      "v_cvt_pk_bf16_f32 v97, v66, v67\n\t"
      "v_mfma_f32_16x16x32_bf16 v[0:3], v[176:179], v[104:107], v[0:3]\n\t"
      "v_exp_f32 v72, v72\n\t"
      "v_mfma_f32_16x16x32_bf16 v[4:7], v[176:179], v[108:111], v[4:7]\n\t"
      "ds_read_b64_tr_b16 v[176:177], v236 offset:0\n\t"
      "ds_read_b64_tr_b16 v[178:179], v236 offset:4096\n\t"
      "v_mfma_f32_16x16x32_bf16 v[8:11], v[180:183], v[104:107], v[8:11]\n\t"
      "v_exp_f32 v73, v73\n\t"
      "v_mfma_f32_16x16x32_bf16 v[12:15], v[180:183], v[108:111], v[12:15]\n\t"
      "ds_read_b64_tr_b16 v[180:181], v237 offset:0\n\t"
      "v_cvt_pk_bf16_f32 v98, v72, v73\n\t"
      "v_mfma_f32_16x16x32_bf16 v[16:19], v[184:187], v[104:107], v[16:19]\n\t"
      "s_waitcnt lgkmcnt(15)\n\t"
      "v_exp_f32 v74, v74\n\t"
      "v_mfma_f32_16x16x32_bf16 v[20:23], v[184:187], v[108:111], v[20:23]\n\t"
      "ds_read_b64_tr_b16 v[182:183], v237 offset:4096\n\t"
      "ds_read_b64_tr_b16 v[184:185], v236 offset:512\n\t"
      "v_mfma_f32_16x16x32_bf16 v[24:27], v[188:191], v[104:107], v[24:27]\n\t"
      "v_exp_f32 v75, v75\n\t"
      "v_mfma_f32_16x16x32_bf16 v[28:31], v[188:191], v[108:111], v[28:31]\n\t"
      "ds_read_b64_tr_b16 v[186:187], v236 offset:4608\n\t"
      "v_cvt_pk_bf16_f32 v99, v74, v75\n\t"
      "v_mfma_f32_16x16x32_bf16 v[80:83], v[152:155], v[120:123], v[80:83]\n\t"
      "v_exp_f32 v68, v68\n\t"
      "v_mfma_f32_16x16x32_bf16 v[84:87], v[152:155], v[136:139], v[84:87]\n\t"
      "ds_read_b128 v[152:155], v230 offset:16384\n\t"
      "ds_read_b64_tr_b16 v[188:189], v237 offset:512\n\t"
      "v_mfma_f32_16x16x32_bf16 v[88:91], v[168:171], v[120:123], v[88:91]\n\t"
      "v_exp_f32 v69, v69\n\t"
      "v_mfma_f32_16x16x32_bf16 v[92:95], v[168:171], v[136:139], v[92:95]\n\t"
      "ds_read_b128 v[168:171], v230 offset:20480\n\t"
      "v_cvt_pk_bf16_f32 v100, v68, v69\n\t"
      "v_mfma_f32_16x16x32_bf16 v[80:83], v[156:159], v[124:127], v[80:83]\n\t"
      "s_waitcnt lgkmcnt(15)\n\t"
      "v_exp_f32 v70, v70\n\t"
      "v_mfma_f32_16x16x32_bf16 v[84:87], v[156:159], v[140:143], v[84:87]\n\t"
      "ds_read_b128 v[156:159], v231 offset:16384\n\t"
      "ds_read_b64_tr_b16 v[190:191], v237 offset:4608\n\t"
      "v_mfma_f32_16x16x32_bf16 v[88:91], v[172:175], v[124:127], v[88:91]\n\t"
      "v_exp_f32 v71, v71\n\t"
      "v_mfma_f32_16x16x32_bf16 v[92:95], v[172:175], v[140:143], v[92:95]\n\t"
      "ds_read_b128 v[172:175], v231 offset:20480\n\t"
      "v_cvt_pk_bf16_f32 v101, v70, v71\n\t"
      "v_mfma_f32_16x16x32_bf16 v[32:35], v[192:195], v[104:107], v[32:35]\n\t"
      "v_exp_f32 v76, v76\n\t"
      "v_mfma_f32_16x16x32_bf16 v[36:39], v[192:195], v[108:111], v[36:39]\n\t"
      "ds_read_b64_tr_b16 v[192:193], v236 offset:1024\n\t"
      "ds_read_b64_tr_b16 v[194:195], v236 offset:5120\n\t"
      "v_mfma_f32_16x16x32_bf16 v[40:43], v[196:199], v[104:107], v[40:43]\n\t"
      "v_exp_f32 v77, v77\n\t"
      "v_mfma_f32_16x16x32_bf16 v[44:47], v[196:199], v[108:111], v[44:47]\n\t"
      "ds_read_b64_tr_b16 v[196:197], v237 offset:1024\n\t"
      "v_cvt_pk_bf16_f32 v102, v76, v77\n\t"
      "v_mfma_f32_16x16x32_bf16 v[48:51], v[200:203], v[104:107], v[48:51]\n\t"
      "s_waitcnt lgkmcnt(15)\n\t"
      "v_exp_f32 v78, v78\n\t"
      "v_mfma_f32_16x16x32_bf16 v[52:55], v[200:203], v[108:111], v[52:55]\n\t"
      "ds_read_b64_tr_b16 v[198:199], v237 offset:5120\n\t"
      "ds_read_b64_tr_b16 v[200:201], v236 offset:1536\n\t"
      "v_mfma_f32_16x16x32_bf16 v[56:59], v[204:207], v[104:107], v[56:59]\n\t"
      "v_exp_f32 v79, v79\n\t"
      "v_mfma_f32_16x16x32_bf16 v[60:63], v[204:207], v[108:111], v[60:63]\n\t"
      "ds_read_b64_tr_b16 v[202:203], v236 offset:5632\n\t"
      "v_cvt_pk_bf16_f32 v103, v78, v79\n\t"
      "v_mfma_f32_16x16x32_bf16 v[216:219], v[224:227], v[104:107], v[216:219]\n\t"
      "v_mfma_f32_16x16x32_bf16 v[220:223], v[224:227], v[108:111], v[220:223]\n\t"
      "v_mfma_f32_16x16x32_bf16 v[64:67], v[144:147], v[112:115], v[208:211]\n\t"
      "v_exp_f32 v80, v80\n\t"
      "v_mfma_f32_16x16x32_bf16 v[68:71], v[144:147], v[128:131], v[212:215]\n\t"
      "ds_read_b128 v[144:147], v228 offset:24576\n\t"
      "ds_read_b64_tr_b16 v[204:205], v237 offset:1536\n\t"
      "v_mfma_f32_16x16x32_bf16 v[72:75], v[160:163], v[112:115], v[208:211]\n\t"
      "v_exp_f32 v81, v81\n\t"
      "v_mfma_f32_16x16x32_bf16 v[76:79], v[160:163], v[128:131], v[212:215]\n\t"
      "ds_read_b128 v[160:163], v228 offset:28672\n\t"
      "v_cvt_pk_bf16_f32 v104, v80, v81\n\t"
      "v_mfma_f32_16x16x32_bf16 v[64:67], v[148:151], v[116:119], v[64:67]\n\t"
      "s_waitcnt lgkmcnt(15)\n\t"
      "v_exp_f32 v82, v82\n\t"
      "v_mfma_f32_16x16x32_bf16 v[68:71], v[148:151], v[132:135], v[68:71]\n\t"
      "ds_read_b128 v[148:151], v229 offset:24576\n\t"
      "ds_read_b64_tr_b16 v[206:207], v237 offset:5632\n\t"
      "v_mfma_f32_16x16x32_bf16 v[72:75], v[164:167], v[116:119], v[72:75]\n\t"
      "v_exp_f32 v83, v83\n\t"
      "v_mfma_f32_16x16x32_bf16 v[76:79], v[164:167], v[132:135], v[76:79]\n\t"
      "ds_read_b128 v[164:167], v229 offset:28672\n\t"
      "v_cvt_pk_bf16_f32 v105, v82, v83\n\t"
      "v_mfma_f32_16x16x32_bf16 v[0:3], v[176:179], v[96:99], v[0:3]\n\t"
      "v_exp_f32 v88, v88\n\t"
      "v_mfma_f32_16x16x32_bf16 v[4:7], v[176:179], v[100:103], v[4:7]\n\t"
      "ds_read_b64_tr_b16 v[176:177], v236 offset:8192\n\t"
      "ds_read_b64_tr_b16 v[178:179], v236 offset:12288\n\t"
      "v_mfma_f32_16x16x32_bf16 v[8:11], v[180:183], v[96:99], v[8:11]\n\t"
      "v_exp_f32 v89, v89\n\t"
      "v_mfma_f32_16x16x32_bf16 v[12:15], v[180:183], v[100:103], v[12:15]\n\t"
      "ds_read_b64_tr_b16 v[180:181], v237 offset:8192\n\t"
      "v_cvt_pk_bf16_f32 v106, v88, v89\n\t"
      "v_mfma_f32_16x16x32_bf16 v[16:19], v[184:187], v[96:99], v[16:19]\n\t"
      "s_waitcnt lgkmcnt(15)\n\t"
      "v_exp_f32 v90, v90\n\t"
      "v_mfma_f32_16x16x32_bf16 v[20:23], v[184:187], v[100:103], v[20:23]\n\t"
      "ds_read_b64_tr_b16 v[182:183], v237 offset:12288\n\t"
      "ds_read_b64_tr_b16 v[184:185], v236 offset:8704\n\t"
      "v_mfma_f32_16x16x32_bf16 v[24:27], v[188:191], v[96:99], v[24:27]\n\t"
      "v_exp_f32 v91, v91\n\t"
      "v_mfma_f32_16x16x32_bf16 v[28:31], v[188:191], v[100:103], v[28:31]\n\t"
      "ds_read_b64_tr_b16 v[186:187], v236 offset:12800\n\t"
      "v_cvt_pk_bf16_f32 v107, v90, v91\n\t"
      "v_mfma_f32_16x16x32_bf16 v[64:67], v[152:155], v[120:123], v[64:67]\n\t"
      "v_exp_f32 v84, v84\n\t"
      "v_mfma_f32_16x16x32_bf16 v[68:71], v[152:155], v[136:139], v[68:71]\n\t"
      "ds_read_b128 v[152:155], v230 offset:24576\n\t"
      "ds_read_b64_tr_b16 v[188:189], v237 offset:8704\n\t"
      "v_mfma_f32_16x16x32_bf16 v[72:75], v[168:171], v[120:123], v[72:75]\n\t"
      "v_exp_f32 v85, v85\n\t"
      "v_mfma_f32_16x16x32_bf16 v[76:79], v[168:171], v[136:139], v[76:79]\n\t"
      "ds_read_b128 v[168:171], v230 offset:28672\n\t"
      "v_cvt_pk_bf16_f32 v108, v84, v85\n\t"
      "v_mfma_f32_16x16x32_bf16 v[64:67], v[156:159], v[124:127], v[64:67]\n\t"
      "s_waitcnt lgkmcnt(15)\n\t"
      "v_exp_f32 v86, v86\n\t"
      "v_mfma_f32_16x16x32_bf16 v[68:71], v[156:159], v[140:143], v[68:71]\n\t"
      "ds_read_b128 v[156:159], v231 offset:24576\n\t"
      "ds_read_b64_tr_b16 v[190:191], v237 offset:12800\n\t"
      "v_mfma_f32_16x16x32_bf16 v[72:75], v[172:175], v[124:127], v[72:75]\n\t"
      "v_exp_f32 v87, v87\n\t"
      "v_mfma_f32_16x16x32_bf16 v[76:79], v[172:175], v[140:143], v[76:79]\n\t"
      "ds_read_b128 v[172:175], v231 offset:28672\n\t"
      "v_cvt_pk_bf16_f32 v109, v86, v87\n\t"
      "v_mfma_f32_16x16x32_bf16 v[32:35], v[192:195], v[96:99], v[32:35]\n\t"
      "v_exp_f32 v92, v92\n\t"
      "v_mfma_f32_16x16x32_bf16 v[36:39], v[192:195], v[100:103], v[36:39]\n\t"
      "ds_read_b64_tr_b16 v[192:193], v236 offset:9216\n\t"
      "ds_read_b64_tr_b16 v[194:195], v236 offset:13312\n\t"
      "v_mfma_f32_16x16x32_bf16 v[40:43], v[196:199], v[96:99], v[40:43]\n\t"
      "v_exp_f32 v93, v93\n\t"
      "v_mfma_f32_16x16x32_bf16 v[44:47], v[196:199], v[100:103], v[44:47]\n\t"
      "ds_read_b64_tr_b16 v[196:197], v237 offset:9216\n\t"
      "v_cvt_pk_bf16_f32 v110, v92, v93\n\t"
      "v_mfma_f32_16x16x32_bf16 v[48:51], v[200:203], v[96:99], v[48:51]\n\t"
      "s_waitcnt lgkmcnt(15)\n\t"
      "v_exp_f32 v94, v94\n\t"
      "v_mfma_f32_16x16x32_bf16 v[52:55], v[200:203], v[100:103], v[52:55]\n\t"
      "ds_read_b64_tr_b16 v[198:199], v237 offset:13312\n\t"
      "ds_read_b64_tr_b16 v[200:201], v236 offset:9728\n\t"
      "v_mfma_f32_16x16x32_bf16 v[56:59], v[204:207], v[96:99], v[56:59]\n\t"
      "v_exp_f32 v95, v95\n\t"
      "v_mfma_f32_16x16x32_bf16 v[60:63], v[204:207], v[100:103], v[60:63]\n\t"
      "ds_read_b64_tr_b16 v[202:203], v236 offset:13824\n\t"
      "v_cvt_pk_bf16_f32 v111, v94, v95\n\t"
      "v_mfma_f32_16x16x32_bf16 v[216:219], v[224:227], v[96:99], v[216:219]\n\t"
      "s_barrier\n\t"
      "v_mfma_f32_16x16x32_bf16 v[220:223], v[224:227], v[100:103], v[220:223]\n\t"
      "v_mfma_f32_16x16x32_bf16 v[80:83], v[144:147], v[112:115], v[208:211]\n\t"
      "v_exp_f32 v64, v64\n\t"
      "v_mfma_f32_16x16x32_bf16 v[84:87], v[144:147], v[128:131], v[212:215]\n\t"
      "ds_read_b128 v[144:147], v228 offset:32768\n\t"
      "ds_read_b64_tr_b16 v[204:205], v237 offset:9728\n\t"
      "v_mfma_f32_16x16x32_bf16 v[88:91], v[160:163], v[112:115], v[208:211]\n\t"
      "v_exp_f32 v65, v65\n\t"
      "v_mfma_f32_16x16x32_bf16 v[92:95], v[160:163], v[128:131], v[212:215]\n\t"
      "ds_read_b128 v[160:163], v228 offset:36864\n\t"
      "v_cvt_pk_bf16_f32 v96, v64, v65\n\t"
      "v_mfma_f32_16x16x32_bf16 v[80:83], v[148:151], v[116:119], v[80:83]\n\t"
      "s_waitcnt lgkmcnt(15)\n\t"
      "v_exp_f32 v66, v66\n\t"
      "v_mfma_f32_16x16x32_bf16 v[84:87], v[148:151], v[132:135], v[84:87]\n\t"
      "ds_read_b128 v[148:151], v229 offset:32768\n\t"
      "ds_read_b64_tr_b16 v[206:207], v237 offset:13824\n\t"
      "v_mfma_f32_16x16x32_bf16 v[88:91], v[164:167], v[116:119], v[88:91]\n\t"
      "v_exp_f32 v67, v67\n\t"
      "v_mfma_f32_16x16x32_bf16 v[92:95], v[164:167], v[132:135], v[92:95]\n\t"
      "ds_read_b128 v[164:167], v229 offset:36864\n\t"
      "v_cvt_pk_bf16_f32 v97, v66, v67\n\t"
      "v_mfma_f32_16x16x32_bf16 v[0:3], v[176:179], v[104:107], v[0:3]\n\t"
      "v_exp_f32 v72, v72\n\t"
      "v_mfma_f32_16x16x32_bf16 v[4:7], v[176:179], v[108:111], v[4:7]\n\t"
      "ds_read_b64_tr_b16 v[176:177], v236 offset:16384\n\t"
      "ds_read_b64_tr_b16 v[178:179], v236 offset:20480\n\t"
      "v_mfma_f32_16x16x32_bf16 v[8:11], v[180:183], v[104:107], v[8:11]\n\t"
      "v_exp_f32 v73, v73\n\t"
      "v_mfma_f32_16x16x32_bf16 v[12:15], v[180:183], v[108:111], v[12:15]\n\t"
      "ds_read_b64_tr_b16 v[180:181], v237 offset:16384\n\t"
      "v_cvt_pk_bf16_f32 v98, v72, v73\n\t"
      "v_mfma_f32_16x16x32_bf16 v[16:19], v[184:187], v[104:107], v[16:19]\n\t"
      "s_waitcnt lgkmcnt(15)\n\t"
      "v_exp_f32 v74, v74\n\t"
      "v_mfma_f32_16x16x32_bf16 v[20:23], v[184:187], v[108:111], v[20:23]\n\t"
      "ds_read_b64_tr_b16 v[182:183], v237 offset:20480\n\t"
      "ds_read_b64_tr_b16 v[184:185], v236 offset:16896\n\t"
      "v_mfma_f32_16x16x32_bf16 v[24:27], v[188:191], v[104:107], v[24:27]\n\t"
      "v_exp_f32 v75, v75\n\t"
      "v_mfma_f32_16x16x32_bf16 v[28:31], v[188:191], v[108:111], v[28:31]\n\t"
      "ds_read_b64_tr_b16 v[186:187], v236 offset:20992\n\t"
      "v_cvt_pk_bf16_f32 v99, v74, v75\n\t"
      "v_mfma_f32_16x16x32_bf16 v[80:83], v[152:155], v[120:123], v[80:83]\n\t"
      "v_exp_f32 v68, v68\n\t"
      "v_mfma_f32_16x16x32_bf16 v[84:87], v[152:155], v[136:139], v[84:87]\n\t"
      "ds_read_b128 v[152:155], v230 offset:32768\n\t"
      "ds_read_b64_tr_b16 v[188:189], v237 offset:16896\n\t"
      "v_mfma_f32_16x16x32_bf16 v[88:91], v[168:171], v[120:123], v[88:91]\n\t"
      "v_exp_f32 v69, v69\n\t"
      "v_mfma_f32_16x16x32_bf16 v[92:95], v[168:171], v[136:139], v[92:95]\n\t"
      "ds_read_b128 v[168:171], v230 offset:36864\n\t"
      "v_cvt_pk_bf16_f32 v100, v68, v69\n\t"
      "v_mfma_f32_16x16x32_bf16 v[80:83], v[156:159], v[124:127], v[80:83]\n\t"
      "s_waitcnt lgkmcnt(15)\n\t"
      "v_exp_f32 v70, v70\n\t"
      "v_mfma_f32_16x16x32_bf16 v[84:87], v[156:159], v[140:143], v[84:87]\n\t"
      "ds_read_b128 v[156:159], v231 offset:32768\n\t"
      "ds_read_b64_tr_b16 v[190:191], v237 offset:20992\n\t"
      "v_mfma_f32_16x16x32_bf16 v[88:91], v[172:175], v[124:127], v[88:91]\n\t"
      "v_exp_f32 v71, v71\n\t"
      "v_mfma_f32_16x16x32_bf16 v[92:95], v[172:175], v[140:143], v[92:95]\n\t"
      "ds_read_b128 v[172:175], v231 offset:36864\n\t"
      "v_cvt_pk_bf16_f32 v101, v70, v71\n\t"
      "v_mfma_f32_16x16x32_bf16 v[32:35], v[192:195], v[104:107], v[32:35]\n\t"
      "v_exp_f32 v76, v76\n\t"
      "v_mfma_f32_16x16x32_bf16 v[36:39], v[192:195], v[108:111], v[36:39]\n\t"
      "ds_read_b64_tr_b16 v[192:193], v236 offset:17408\n\t"
      "ds_read_b64_tr_b16 v[194:195], v236 offset:21504\n\t"
      "v_mfma_f32_16x16x32_bf16 v[40:43], v[196:199], v[104:107], v[40:43]\n\t"
      "v_exp_f32 v77, v77\n\t"
      "v_mfma_f32_16x16x32_bf16 v[44:47], v[196:199], v[108:111], v[44:47]\n\t"
      "ds_read_b64_tr_b16 v[196:197], v237 offset:17408\n\t"
      "v_cvt_pk_bf16_f32 v102, v76, v77\n\t"
      "v_mfma_f32_16x16x32_bf16 v[48:51], v[200:203], v[104:107], v[48:51]\n\t"
      "s_waitcnt lgkmcnt(15)\n\t"
      "v_exp_f32 v78, v78\n\t"
      "v_mfma_f32_16x16x32_bf16 v[52:55], v[200:203], v[108:111], v[52:55]\n\t"
      "ds_read_b64_tr_b16 v[198:199], v237 offset:21504\n\t"
      "ds_read_b64_tr_b16 v[200:201], v236 offset:17920\n\t"
      "v_mfma_f32_16x16x32_bf16 v[56:59], v[204:207], v[104:107], v[56:59]\n\t"
      "v_exp_f32 v79, v79\n\t"
      "v_mfma_f32_16x16x32_bf16 v[60:63], v[204:207], v[108:111], v[60:63]\n\t"
      "ds_read_b64_tr_b16 v[202:203], v236 offset:22016\n\t"
      "v_cvt_pk_bf16_f32 v103, v78, v79\n\t"
      "v_mfma_f32_16x16x32_bf16 v[216:219], v[224:227], v[104:107], v[216:219]\n\t"
      "v_mfma_f32_16x16x32_bf16 v[220:223], v[224:227], v[108:111], v[220:223]\n\t"
      "v_mfma_f32_16x16x32_bf16 v[64:67], v[144:147], v[112:115], v[208:211]\n\t"
      "v_exp_f32 v80, v80\n\t"
      "v_mfma_f32_16x16x32_bf16 v[68:71], v[144:147], v[128:131], v[212:215]\n\t"
      "ds_read_b128 v[144:147], v228 offset:40960\n\t"
      "ds_read_b64_tr_b16 v[204:205], v237 offset:17920\n\t"
      "v_mfma_f32_16x16x32_bf16 v[72:75], v[160:163], v[112:115], v[208:211]\n\t"
      "v_exp_f32 v81, v81\n\t"
      "v_mfma_f32_16x16x32_bf16 v[76:79], v[160:163], v[128:131], v[212:215]\n\t"
      "ds_read_b128 v[160:163], v228 offset:45056\n\t"
      "v_cvt_pk_bf16_f32 v104, v80, v81\n\t"
      "v_mfma_f32_16x16x32_bf16 v[64:67], v[148:151], v[116:119], v[64:67]\n\t"
      "s_waitcnt lgkmcnt(15)\n\t"
      "v_exp_f32 v82, v82\n\t"
      "v_mfma_f32_16x16x32_bf16 v[68:71], v[148:151], v[132:135], v[68:71]\n\t"
      "ds_read_b128 v[148:151], v229 offset:40960\n\t"
      "ds_read_b64_tr_b16 v[206:207], v237 offset:22016\n\t"
      "v_mfma_f32_16x16x32_bf16 v[72:75], v[164:167], v[116:119], v[72:75]\n\t"
      "v_exp_f32 v83, v83\n\t"
      "v_mfma_f32_16x16x32_bf16 v[76:79], v[164:167], v[132:135], v[76:79]\n\t"
      "ds_read_b128 v[164:167], v229 offset:45056\n\t"
      "v_cvt_pk_bf16_f32 v105, v82, v83\n\t"
      "v_mfma_f32_16x16x32_bf16 v[0:3], v[176:179], v[96:99], v[0:3]\n\t"
      "v_exp_f32 v88, v88\n\t"
      "v_mfma_f32_16x16x32_bf16 v[4:7], v[176:179], v[100:103], v[4:7]\n\t"
      "ds_read_b64_tr_b16 v[176:177], v236 offset:24576\n\t"
      "ds_read_b64_tr_b16 v[178:179], v236 offset:28672\n\t"
      "v_mfma_f32_16x16x32_bf16 v[8:11], v[180:183], v[96:99], v[8:11]\n\t"
      "v_exp_f32 v89, v89\n\t"
      "v_mfma_f32_16x16x32_bf16 v[12:15], v[180:183], v[100:103], v[12:15]\n\t"
      "ds_read_b64_tr_b16 v[180:181], v237 offset:24576\n\t"
      "v_cvt_pk_bf16_f32 v106, v88, v89\n\t"
      "v_mfma_f32_16x16x32_bf16 v[16:19], v[184:187], v[96:99], v[16:19]\n\t"
      "s_waitcnt lgkmcnt(15)\n\t"
      "v_exp_f32 v90, v90\n\t"
      "v_mfma_f32_16x16x32_bf16 v[20:23], v[184:187], v[100:103], v[20:23]\n\t"
      "ds_read_b64_tr_b16 v[182:183], v237 offset:28672\n\t"
      "ds_read_b64_tr_b16 v[184:185], v236 offset:25088\n\t"
      "v_mfma_f32_16x16x32_bf16 v[24:27], v[188:191], v[96:99], v[24:27]\n\t"
      "v_exp_f32 v91, v91\n\t"
      "v_mfma_f32_16x16x32_bf16 v[28:31], v[188:191], v[100:103], v[28:31]\n\t"
      "ds_read_b64_tr_b16 v[186:187], v236 offset:29184\n\t"
      "v_cvt_pk_bf16_f32 v107, v90, v91\n\t"
      "v_mfma_f32_16x16x32_bf16 v[64:67], v[152:155], v[120:123], v[64:67]\n\t"
      "v_exp_f32 v84, v84\n\t"
      "v_mfma_f32_16x16x32_bf16 v[68:71], v[152:155], v[136:139], v[68:71]\n\t"
      "ds_read_b128 v[152:155], v230 offset:40960\n\t"
      "ds_read_b64_tr_b16 v[188:189], v237 offset:25088\n\t"
      "v_mfma_f32_16x16x32_bf16 v[72:75], v[168:171], v[120:123], v[72:75]\n\t"
      "v_exp_f32 v85, v85\n\t"
      "v_mfma_f32_16x16x32_bf16 v[76:79], v[168:171], v[136:139], v[76:79]\n\t"
      "ds_read_b128 v[168:171], v230 offset:45056\n\t"
      "v_cvt_pk_bf16_f32 v108, v84, v85\n\t"
      "v_mfma_f32_16x16x32_bf16 v[64:67], v[156:159], v[124:127], v[64:67]\n\t"
      "s_waitcnt lgkmcnt(15)\n\t"
      "v_exp_f32 v86, v86\n\t"
      "v_mfma_f32_16x16x32_bf16 v[68:71], v[156:159], v[140:143], v[68:71]\n\t"
      "ds_read_b128 v[156:159], v231 offset:40960\n\t"
      "ds_read_b64_tr_b16 v[190:191], v237 offset:29184\n\t"
      "v_mfma_f32_16x16x32_bf16 v[72:75], v[172:175], v[124:127], v[72:75]\n\t"
      "v_exp_f32 v87, v87\n\t"
      "v_mfma_f32_16x16x32_bf16 v[76:79], v[172:175], v[140:143], v[76:79]\n\t"
      "ds_read_b128 v[172:175], v231 offset:45056\n\t"
      "v_cvt_pk_bf16_f32 v109, v86, v87\n\t"
      "v_mfma_f32_16x16x32_bf16 v[32:35], v[192:195], v[96:99], v[32:35]\n\t"
      "v_exp_f32 v92, v92\n\t"
      "v_mfma_f32_16x16x32_bf16 v[36:39], v[192:195], v[100:103], v[36:39]\n\t"
      "ds_read_b64_tr_b16 v[192:193], v236 offset:25600\n\t"
      "ds_read_b64_tr_b16 v[194:195], v236 offset:29696\n\t"
      "v_mfma_f32_16x16x32_bf16 v[40:43], v[196:199], v[96:99], v[40:43]\n\t"
      "v_exp_f32 v93, v93\n\t"
      "v_mfma_f32_16x16x32_bf16 v[44:47], v[196:199], v[100:103], v[44:47]\n\t"
      "ds_read_b64_tr_b16 v[196:197], v237 offset:25600\n\t"
      "v_cvt_pk_bf16_f32 v110, v92, v93\n\t"
      "v_mfma_f32_16x16x32_bf16 v[48:51], v[200:203], v[96:99], v[48:51]\n\t"
      "s_waitcnt lgkmcnt(15)\n\t"
      "v_exp_f32 v94, v94\n\t"
      "v_mfma_f32_16x16x32_bf16 v[52:55], v[200:203], v[100:103], v[52:55]\n\t"
      "ds_read_b64_tr_b16 v[198:199], v237 offset:29696\n\t"
      "ds_read_b64_tr_b16 v[200:201], v236 offset:26112\n\t"
      "v_mfma_f32_16x16x32_bf16 v[56:59], v[204:207], v[96:99], v[56:59]\n\t"
      "v_exp_f32 v95, v95\n\t"
      "v_mfma_f32_16x16x32_bf16 v[60:63], v[204:207], v[100:103], v[60:63]\n\t"
      "ds_read_b64_tr_b16 v[202:203], v236 offset:30208\n\t"
      "v_cvt_pk_bf16_f32 v111, v94, v95\n\t"
      "v_mfma_f32_16x16x32_bf16 v[216:219], v[224:227], v[96:99], v[216:219]\n\t"
      "s_barrier\n\t"
      "v_mfma_f32_16x16x32_bf16 v[220:223], v[224:227], v[100:103], v[220:223]\n\t"
      "v_mfma_f32_16x16x32_bf16 v[80:83], v[144:147], v[112:115], v[208:211]\n\t"
      "v_exp_f32 v64, v64\n\t"
      "v_mfma_f32_16x16x32_bf16 v[84:87], v[144:147], v[128:131], v[212:215]\n\t"
      "ds_read_b128 v[144:147], v228 offset:49152\n\t"
      "ds_read_b64_tr_b16 v[204:205], v237 offset:26112\n\t"
      "v_mfma_f32_16x16x32_bf16 v[88:91], v[160:163], v[112:115], v[208:211]\n\t"
      "v_exp_f32 v65, v65\n\t"
      "v_mfma_f32_16x16x32_bf16 v[92:95], v[160:163], v[128:131], v[212:215]\n\t"
      "ds_read_b128 v[160:163], v228 offset:53248\n\t"
      "v_cvt_pk_bf16_f32 v96, v64, v65\n\t"
      "v_mfma_f32_16x16x32_bf16 v[80:83], v[148:151], v[116:119], v[80:83]\n\t"
      "s_waitcnt lgkmcnt(15)\n\t"
      "v_exp_f32 v66, v66\n\t"
      "v_mfma_f32_16x16x32_bf16 v[84:87], v[148:151], v[132:135], v[84:87]\n\t"
      "ds_read_b128 v[148:151], v229 offset:49152\n\t"
      "ds_read_b64_tr_b16 v[206:207], v237 offset:30208\n\t"
      "v_mfma_f32_16x16x32_bf16 v[88:91], v[164:167], v[116:119], v[88:91]\n\t"
      "v_exp_f32 v67, v67\n\t"
      "v_mfma_f32_16x16x32_bf16 v[92:95], v[164:167], v[132:135], v[92:95]\n\t"
      "ds_read_b128 v[164:167], v229 offset:53248\n\t"
      "v_cvt_pk_bf16_f32 v97, v66, v67\n\t"
      "v_mfma_f32_16x16x32_bf16 v[0:3], v[176:179], v[104:107], v[0:3]\n\t"
      "v_exp_f32 v72, v72\n\t"
      "v_mfma_f32_16x16x32_bf16 v[4:7], v[176:179], v[108:111], v[4:7]\n\t"
      "ds_read_b64_tr_b16 v[176:177], v236 offset:32768\n\t"
      "ds_read_b64_tr_b16 v[178:179], v236 offset:36864\n\t"
      "v_mfma_f32_16x16x32_bf16 v[8:11], v[180:183], v[104:107], v[8:11]\n\t"
      "v_exp_f32 v73, v73\n\t"
      "v_mfma_f32_16x16x32_bf16 v[12:15], v[180:183], v[108:111], v[12:15]\n\t"
      "ds_read_b64_tr_b16 v[180:181], v237 offset:32768\n\t"
      "v_cvt_pk_bf16_f32 v98, v72, v73\n\t"
      "v_mfma_f32_16x16x32_bf16 v[16:19], v[184:187], v[104:107], v[16:19]\n\t"
      "s_waitcnt lgkmcnt(15)\n\t"
      "v_exp_f32 v74, v74\n\t"
      "v_mfma_f32_16x16x32_bf16 v[20:23], v[184:187], v[108:111], v[20:23]\n\t"
      "ds_read_b64_tr_b16 v[182:183], v237 offset:36864\n\t"
      "ds_read_b64_tr_b16 v[184:185], v236 offset:33280\n\t"
      "v_mfma_f32_16x16x32_bf16 v[24:27], v[188:191], v[104:107], v[24:27]\n\t"
      "v_exp_f32 v75, v75\n\t"
      "v_mfma_f32_16x16x32_bf16 v[28:31], v[188:191], v[108:111], v[28:31]\n\t"
      "ds_read_b64_tr_b16 v[186:187], v236 offset:37376\n\t"
      "v_cvt_pk_bf16_f32 v99, v74, v75\n\t"
      "v_mfma_f32_16x16x32_bf16 v[80:83], v[152:155], v[120:123], v[80:83]\n\t"
      "v_exp_f32 v68, v68\n\t"
      "v_mfma_f32_16x16x32_bf16 v[84:87], v[152:155], v[136:139], v[84:87]\n\t"
      "ds_read_b128 v[152:155], v230 offset:49152\n\t"
      "ds_read_b64_tr_b16 v[188:189], v237 offset:33280\n\t"
      "v_mfma_f32_16x16x32_bf16 v[88:91], v[168:171], v[120:123], v[88:91]\n\t"
      "v_exp_f32 v69, v69\n\t"
      "v_mfma_f32_16x16x32_bf16 v[92:95], v[168:171], v[136:139], v[92:95]\n\t"
      "ds_read_b128 v[168:171], v230 offset:53248\n\t"
      "v_cvt_pk_bf16_f32 v100, v68, v69\n\t"
      "v_mfma_f32_16x16x32_bf16 v[80:83], v[156:159], v[124:127], v[80:83]\n\t"
      "s_waitcnt lgkmcnt(15)\n\t"
      "v_exp_f32 v70, v70\n\t"
      "v_mfma_f32_16x16x32_bf16 v[84:87], v[156:159], v[140:143], v[84:87]\n\t"
      "ds_read_b128 v[156:159], v231 offset:49152\n\t"
      "ds_read_b64_tr_b16 v[190:191], v237 offset:37376\n\t"
      "v_mfma_f32_16x16x32_bf16 v[88:91], v[172:175], v[124:127], v[88:91]\n\t"
      "v_exp_f32 v71, v71\n\t"
      "v_mfma_f32_16x16x32_bf16 v[92:95], v[172:175], v[140:143], v[92:95]\n\t"
      "ds_read_b128 v[172:175], v231 offset:53248\n\t"
      "v_cvt_pk_bf16_f32 v101, v70, v71\n\t"
      "v_mfma_f32_16x16x32_bf16 v[32:35], v[192:195], v[104:107], v[32:35]\n\t"
      "v_exp_f32 v76, v76\n\t"
      "v_mfma_f32_16x16x32_bf16 v[36:39], v[192:195], v[108:111], v[36:39]\n\t"
      "ds_read_b64_tr_b16 v[192:193], v236 offset:33792\n\t"
      "ds_read_b64_tr_b16 v[194:195], v236 offset:37888\n\t"
      "v_mfma_f32_16x16x32_bf16 v[40:43], v[196:199], v[104:107], v[40:43]\n\t"
      "v_exp_f32 v77, v77\n\t"
      "v_mfma_f32_16x16x32_bf16 v[44:47], v[196:199], v[108:111], v[44:47]\n\t"
      "ds_read_b64_tr_b16 v[196:197], v237 offset:33792\n\t"
      "v_cvt_pk_bf16_f32 v102, v76, v77\n\t"
      "v_mfma_f32_16x16x32_bf16 v[48:51], v[200:203], v[104:107], v[48:51]\n\t"
      "s_waitcnt lgkmcnt(15)\n\t"
      "v_exp_f32 v78, v78\n\t"
      "v_mfma_f32_16x16x32_bf16 v[52:55], v[200:203], v[108:111], v[52:55]\n\t"
      "ds_read_b64_tr_b16 v[198:199], v237 offset:37888\n\t"
      "ds_read_b64_tr_b16 v[200:201], v236 offset:34304\n\t"
      "v_mfma_f32_16x16x32_bf16 v[56:59], v[204:207], v[104:107], v[56:59]\n\t"
      "v_exp_f32 v79, v79\n\t"
      "v_mfma_f32_16x16x32_bf16 v[60:63], v[204:207], v[108:111], v[60:63]\n\t"
      "ds_read_b64_tr_b16 v[202:203], v236 offset:38400\n\t"
      "v_cvt_pk_bf16_f32 v103, v78, v79\n\t"
      "v_mfma_f32_16x16x32_bf16 v[216:219], v[224:227], v[104:107], v[216:219]\n\t"
      "v_mfma_f32_16x16x32_bf16 v[220:223], v[224:227], v[108:111], v[220:223]\n\t"
      "v_mfma_f32_16x16x32_bf16 v[64:67], v[144:147], v[112:115], v[208:211]\n\t"
      "v_exp_f32 v80, v80\n\t"
      "v_mfma_f32_16x16x32_bf16 v[68:71], v[144:147], v[128:131], v[212:215]\n\t"
      "ds_read_b128 v[144:147], v228 offset:57344\n\t"
      "ds_read_b64_tr_b16 v[204:205], v237 offset:34304\n\t"
      "v_mfma_f32_16x16x32_bf16 v[72:75], v[160:163], v[112:115], v[208:211]\n\t"
      "v_exp_f32 v81, v81\n\t"
      "v_mfma_f32_16x16x32_bf16 v[76:79], v[160:163], v[128:131], v[212:215]\n\t"
      "ds_read_b128 v[160:163], v228 offset:61440\n\t"
      "v_cvt_pk_bf16_f32 v104, v80, v81\n\t"
      "v_mfma_f32_16x16x32_bf16 v[64:67], v[148:151], v[116:119], v[64:67]\n\t"
      "s_waitcnt lgkmcnt(15)\n\t"
      "v_exp_f32 v82, v82\n\t"
      "v_mfma_f32_16x16x32_bf16 v[68:71], v[148:151], v[132:135], v[68:71]\n\t"
      "ds_read_b128 v[148:151], v229 offset:57344\n\t"
      "ds_read_b64_tr_b16 v[206:207], v237 offset:38400\n\t"
      "v_mfma_f32_16x16x32_bf16 v[72:75], v[164:167], v[116:119], v[72:75]\n\t"
      "v_exp_f32 v83, v83\n\t"
      "v_mfma_f32_16x16x32_bf16 v[76:79], v[164:167], v[132:135], v[76:79]\n\t"
      "ds_read_b128 v[164:167], v229 offset:61440\n\t"
      "v_cvt_pk_bf16_f32 v105, v82, v83\n\t"
      "v_mfma_f32_16x16x32_bf16 v[0:3], v[176:179], v[96:99], v[0:3]\n\t"
      "v_exp_f32 v88, v88\n\t"
      "v_mfma_f32_16x16x32_bf16 v[4:7], v[176:179], v[100:103], v[4:7]\n\t"
      "ds_read_b64_tr_b16 v[176:177], v236 offset:40960\n\t"
      "ds_read_b64_tr_b16 v[178:179], v236 offset:45056\n\t"
      "v_mfma_f32_16x16x32_bf16 v[8:11], v[180:183], v[96:99], v[8:11]\n\t"
      "v_exp_f32 v89, v89\n\t"
      "v_mfma_f32_16x16x32_bf16 v[12:15], v[180:183], v[100:103], v[12:15]\n\t"
      "ds_read_b64_tr_b16 v[180:181], v237 offset:40960\n\t"
      "v_cvt_pk_bf16_f32 v106, v88, v89\n\t"
      "v_mfma_f32_16x16x32_bf16 v[16:19], v[184:187], v[96:99], v[16:19]\n\t"
      "s_waitcnt lgkmcnt(15)\n\t"
      "v_exp_f32 v90, v90\n\t"
      "v_mfma_f32_16x16x32_bf16 v[20:23], v[184:187], v[100:103], v[20:23]\n\t"
      "ds_read_b64_tr_b16 v[182:183], v237 offset:45056\n\t"
      "ds_read_b64_tr_b16 v[184:185], v236 offset:41472\n\t"
      "v_mfma_f32_16x16x32_bf16 v[24:27], v[188:191], v[96:99], v[24:27]\n\t"
      "v_exp_f32 v91, v91\n\t"
      "v_mfma_f32_16x16x32_bf16 v[28:31], v[188:191], v[100:103], v[28:31]\n\t"
      "ds_read_b64_tr_b16 v[186:187], v236 offset:45568\n\t"
      "v_cvt_pk_bf16_f32 v107, v90, v91\n\t"
      "v_mfma_f32_16x16x32_bf16 v[64:67], v[152:155], v[120:123], v[64:67]\n\t"
      "v_exp_f32 v84, v84\n\t"
      "v_mfma_f32_16x16x32_bf16 v[68:71], v[152:155], v[136:139], v[68:71]\n\t"
      "ds_read_b128 v[152:155], v230 offset:57344\n\t"
      "ds_read_b64_tr_b16 v[188:189], v237 offset:41472\n\t"
      "v_mfma_f32_16x16x32_bf16 v[72:75], v[168:171], v[120:123], v[72:75]\n\t"
      "v_exp_f32 v85, v85\n\t"
      "v_mfma_f32_16x16x32_bf16 v[76:79], v[168:171], v[136:139], v[76:79]\n\t"
      "ds_read_b128 v[168:171], v230 offset:61440\n\t"
      "v_cvt_pk_bf16_f32 v108, v84, v85\n\t"
      "v_mfma_f32_16x16x32_bf16 v[64:67], v[156:159], v[124:127], v[64:67]\n\t"
      "s_waitcnt lgkmcnt(15)\n\t"
      "v_exp_f32 v86, v86\n\t"
      "v_mfma_f32_16x16x32_bf16 v[68:71], v[156:159], v[140:143], v[68:71]\n\t"
      "ds_read_b128 v[156:159], v231 offset:57344\n\t"
      "ds_read_b64_tr_b16 v[190:191], v237 offset:45568\n\t"
      "v_mfma_f32_16x16x32_bf16 v[72:75], v[172:175], v[124:127], v[72:75]\n\t"
      "v_exp_f32 v87, v87\n\t"
      "v_mfma_f32_16x16x32_bf16 v[76:79], v[172:175], v[140:143], v[76:79]\n\t"
      "ds_read_b128 v[172:175], v231 offset:61440\n\t"
      "v_cvt_pk_bf16_f32 v109, v86, v87\n\t"
      "v_mfma_f32_16x16x32_bf16 v[32:35], v[192:195], v[96:99], v[32:35]\n\t"
      "v_exp_f32 v92, v92\n\t"
      "v_mfma_f32_16x16x32_bf16 v[36:39], v[192:195], v[100:103], v[36:39]\n\t"
      "ds_read_b64_tr_b16 v[192:193], v236 offset:41984\n\t"
      "ds_read_b64_tr_b16 v[194:195], v236 offset:46080\n\t"
      "v_mfma_f32_16x16x32_bf16 v[40:43], v[196:199], v[96:99], v[40:43]\n\t"
      "v_exp_f32 v93, v93\n\t"
      "v_mfma_f32_16x16x32_bf16 v[44:47], v[196:199], v[100:103], v[44:47]\n\t"
      "ds_read_b64_tr_b16 v[196:197], v237 offset:41984\n\t"
      "v_cvt_pk_bf16_f32 v110, v92, v93\n\t"
      "v_mfma_f32_16x16x32_bf16 v[48:51], v[200:203], v[96:99], v[48:51]\n\t"
      "s_waitcnt lgkmcnt(15)\n\t"
      "v_exp_f32 v94, v94\n\t"
      "v_mfma_f32_16x16x32_bf16 v[52:55], v[200:203], v[100:103], v[52:55]\n\t"
      "ds_read_b64_tr_b16 v[198:199], v237 offset:46080\n\t"
      "ds_read_b64_tr_b16 v[200:201], v236 offset:42496\n\t"
      "v_mfma_f32_16x16x32_bf16 v[56:59], v[204:207], v[96:99], v[56:59]\n\t"
      "v_exp_f32 v95, v95\n\t"
      "v_mfma_f32_16x16x32_bf16 v[60:63], v[204:207], v[100:103], v[60:63]\n\t"
      "ds_read_b64_tr_b16 v[202:203], v236 offset:46592\n\t"
      "v_cvt_pk_bf16_f32 v111, v94, v95\n\t"
      "v_mfma_f32_16x16x32_bf16 v[216:219], v[224:227], v[96:99], v[216:219]\n\t"
      "s_barrier\n\t"
      "v_mfma_f32_16x16x32_bf16 v[220:223], v[224:227], v[100:103], v[220:223]\n\t"
      "v_mfma_f32_16x16x32_bf16 v[80:83], v[144:147], v[112:115], v[208:211]\n\t"
      "v_exp_f32 v64, v64\n\t"
      "v_mfma_f32_16x16x32_bf16 v[84:87], v[144:147], v[128:131], v[212:215]\n\t"
      "ds_read_b128 v[144:147], v232 offset:0\n\t"
      "ds_read_b64_tr_b16 v[204:205], v237 offset:42496\n\t"
      "v_mfma_f32_16x16x32_bf16 v[88:91], v[160:163], v[112:115], v[208:211]\n\t"
      "v_exp_f32 v65, v65\n\t"
      "v_mfma_f32_16x16x32_bf16 v[92:95], v[160:163], v[128:131], v[212:215]\n\t"
      "ds_read_b128 v[160:163], v232 offset:4096\n\t"
      "v_cvt_pk_bf16_f32 v96, v64, v65\n\t"
      "v_mfma_f32_16x16x32_bf16 v[80:83], v[148:151], v[116:119], v[80:83]\n\t"
      "s_waitcnt lgkmcnt(15)\n\t"
      "v_exp_f32 v66, v66\n\t"
      "v_mfma_f32_16x16x32_bf16 v[84:87], v[148:151], v[132:135], v[84:87]\n\t"
      "ds_read_b128 v[148:151], v233 offset:0\n\t"
      "ds_read_b64_tr_b16 v[206:207], v237 offset:46592\n\t"
      "v_mfma_f32_16x16x32_bf16 v[88:91], v[164:167], v[116:119], v[88:91]\n\t"
      "v_exp_f32 v67, v67\n\t"
      "v_mfma_f32_16x16x32_bf16 v[92:95], v[164:167], v[132:135], v[92:95]\n\t"
      "ds_read_b128 v[164:167], v233 offset:4096\n\t"
      "v_cvt_pk_bf16_f32 v97, v66, v67\n\t"
      "v_mfma_f32_16x16x32_bf16 v[0:3], v[176:179], v[104:107], v[0:3]\n\t"
      "v_exp_f32 v72, v72\n\t"
      "v_mfma_f32_16x16x32_bf16 v[4:7], v[176:179], v[108:111], v[4:7]\n\t"
      "ds_read_b64_tr_b16 v[176:177], v236 offset:49152\n\t"
      "ds_read_b64_tr_b16 v[178:179], v236 offset:53248\n\t"
      "v_mfma_f32_16x16x32_bf16 v[8:11], v[180:183], v[104:107], v[8:11]\n\t"
      "v_exp_f32 v73, v73\n\t"
      "v_mfma_f32_16x16x32_bf16 v[12:15], v[180:183], v[108:111], v[12:15]\n\t"
      "ds_read_b64_tr_b16 v[180:181], v237 offset:49152\n\t"
      "v_cvt_pk_bf16_f32 v98, v72, v73\n\t"
      "v_mfma_f32_16x16x32_bf16 v[16:19], v[184:187], v[104:107], v[16:19]\n\t"
      "s_waitcnt lgkmcnt(15)\n\t"
      "v_exp_f32 v74, v74\n\t"
      "v_mfma_f32_16x16x32_bf16 v[20:23], v[184:187], v[108:111], v[20:23]\n\t"
      "ds_read_b64_tr_b16 v[182:183], v237 offset:53248\n\t"
      "ds_read_b64_tr_b16 v[184:185], v236 offset:49664\n\t"
      "v_mfma_f32_16x16x32_bf16 v[24:27], v[188:191], v[104:107], v[24:27]\n\t"
      "v_exp_f32 v75, v75\n\t"
      "v_mfma_f32_16x16x32_bf16 v[28:31], v[188:191], v[108:111], v[28:31]\n\t"
      "ds_read_b64_tr_b16 v[186:187], v236 offset:53760\n\t"
      "v_cvt_pk_bf16_f32 v99, v74, v75\n\t"
      "v_mfma_f32_16x16x32_bf16 v[80:83], v[152:155], v[120:123], v[80:83]\n\t"
      "v_exp_f32 v68, v68\n\t"
      "v_mfma_f32_16x16x32_bf16 v[84:87], v[152:155], v[136:139], v[84:87]\n\t"
      "ds_read_b128 v[152:155], v234 offset:0\n\t"
      "ds_read_b64_tr_b16 v[188:189], v237 offset:49664\n\t"
      "v_mfma_f32_16x16x32_bf16 v[88:91], v[168:171], v[120:123], v[88:91]\n\t"
      "v_exp_f32 v69, v69\n\t"
      "v_mfma_f32_16x16x32_bf16 v[92:95], v[168:171], v[136:139], v[92:95]\n\t"
      "ds_read_b128 v[168:171], v234 offset:4096\n\t"
      "v_cvt_pk_bf16_f32 v100, v68, v69\n\t"
      "v_mfma_f32_16x16x32_bf16 v[80:83], v[156:159], v[124:127], v[80:83]\n\t"
      "s_waitcnt lgkmcnt(15)\n\t"
      "v_exp_f32 v70, v70\n\t"
      "v_mfma_f32_16x16x32_bf16 v[84:87], v[156:159], v[140:143], v[84:87]\n\t"
      "ds_read_b128 v[156:159], v235 offset:0\n\t"
      "ds_read_b64_tr_b16 v[190:191], v237 offset:53760\n\t"
      "v_mfma_f32_16x16x32_bf16 v[88:91], v[172:175], v[124:127], v[88:91]\n\t"
      "v_exp_f32 v71, v71\n\t"
      "v_mfma_f32_16x16x32_bf16 v[92:95], v[172:175], v[140:143], v[92:95]\n\t"
      "ds_read_b128 v[172:175], v235 offset:4096\n\t"
      "v_cvt_pk_bf16_f32 v101, v70, v71\n\t"
      "v_mfma_f32_16x16x32_bf16 v[32:35], v[192:195], v[104:107], v[32:35]\n\t"
      "v_exp_f32 v76, v76\n\t"
      "v_mfma_f32_16x16x32_bf16 v[36:39], v[192:195], v[108:111], v[36:39]\n\t"
      "ds_read_b64_tr_b16 v[192:193], v236 offset:50176\n\t"
      "ds_read_b64_tr_b16 v[194:195], v236 offset:54272\n\t"
      "v_mfma_f32_16x16x32_bf16 v[40:43], v[196:199], v[104:107], v[40:43]\n\t"
      "v_exp_f32 v77, v77\n\t"
      "v_mfma_f32_16x16x32_bf16 v[44:47], v[196:199], v[108:111], v[44:47]\n\t"
      "ds_read_b64_tr_b16 v[196:197], v237 offset:50176\n\t"
      "v_cvt_pk_bf16_f32 v102, v76, v77\n\t"
      "v_mfma_f32_16x16x32_bf16 v[48:51], v[200:203], v[104:107], v[48:51]\n\t"
      "s_waitcnt lgkmcnt(15)\n\t"
      "v_exp_f32 v78, v78\n\t"
      "v_mfma_f32_16x16x32_bf16 v[52:55], v[200:203], v[108:111], v[52:55]\n\t"
      "ds_read_b64_tr_b16 v[198:199], v237 offset:54272\n\t"
      "ds_read_b64_tr_b16 v[200:201], v236 offset:50688\n\t"
      "v_mfma_f32_16x16x32_bf16 v[56:59], v[204:207], v[104:107], v[56:59]\n\t"
      "v_exp_f32 v79, v79\n\t"
      "v_mfma_f32_16x16x32_bf16 v[60:63], v[204:207], v[108:111], v[60:63]\n\t"
      "ds_read_b64_tr_b16 v[202:203], v236 offset:54784\n\t"
      "v_cvt_pk_bf16_f32 v103, v78, v79\n\t"
      "v_mfma_f32_16x16x32_bf16 v[216:219], v[224:227], v[104:107], v[216:219]\n\t"
      "v_mfma_f32_16x16x32_bf16 v[220:223], v[224:227], v[108:111], v[220:223]\n\t"
      "v_mfma_f32_16x16x32_bf16 v[64:67], v[144:147], v[112:115], v[208:211]\n\t"
      "v_exp_f32 v80, v80\n\t"
      "v_mfma_f32_16x16x32_bf16 v[68:71], v[144:147], v[128:131], v[212:215]\n\t"
      "ds_read_b128 v[144:147], v232 offset:8192\n\t"
      "ds_read_b64_tr_b16 v[204:205], v237 offset:50688\n\t"
      "v_mfma_f32_16x16x32_bf16 v[72:75], v[160:163], v[112:115], v[208:211]\n\t"
      "v_exp_f32 v81, v81\n\t"
      "v_mfma_f32_16x16x32_bf16 v[76:79], v[160:163], v[128:131], v[212:215]\n\t"
      "ds_read_b128 v[160:163], v232 offset:12288\n\t"
      "v_cvt_pk_bf16_f32 v104, v80, v81\n\t"
      "v_mfma_f32_16x16x32_bf16 v[64:67], v[148:151], v[116:119], v[64:67]\n\t"
      "s_waitcnt lgkmcnt(15)\n\t"
      "v_exp_f32 v82, v82\n\t"
      "v_mfma_f32_16x16x32_bf16 v[68:71], v[148:151], v[132:135], v[68:71]\n\t"
      "ds_read_b128 v[148:151], v233 offset:8192\n\t"
      "ds_read_b64_tr_b16 v[206:207], v237 offset:54784\n\t"
      "v_mfma_f32_16x16x32_bf16 v[72:75], v[164:167], v[116:119], v[72:75]\n\t"
      "v_exp_f32 v83, v83\n\t"
      "v_mfma_f32_16x16x32_bf16 v[76:79], v[164:167], v[132:135], v[76:79]\n\t"
      "ds_read_b128 v[164:167], v233 offset:12288\n\t"
      "v_cvt_pk_bf16_f32 v105, v82, v83\n\t"
      "v_mfma_f32_16x16x32_bf16 v[0:3], v[176:179], v[96:99], v[0:3]\n\t"
      "v_exp_f32 v88, v88\n\t"
      "v_mfma_f32_16x16x32_bf16 v[4:7], v[176:179], v[100:103], v[4:7]\n\t"
      "ds_read_b64_tr_b16 v[176:177], v236 offset:57344\n\t"
      "ds_read_b64_tr_b16 v[178:179], v236 offset:61440\n\t"
      "v_mfma_f32_16x16x32_bf16 v[8:11], v[180:183], v[96:99], v[8:11]\n\t"
      "v_exp_f32 v89, v89\n\t"
      "v_mfma_f32_16x16x32_bf16 v[12:15], v[180:183], v[100:103], v[12:15]\n\t"
      "ds_read_b64_tr_b16 v[180:181], v237 offset:57344\n\t"
      "v_cvt_pk_bf16_f32 v106, v88, v89\n\t"
      "v_mfma_f32_16x16x32_bf16 v[16:19], v[184:187], v[96:99], v[16:19]\n\t"
      "s_waitcnt lgkmcnt(15)\n\t"
      "v_exp_f32 v90, v90\n\t"
      "v_mfma_f32_16x16x32_bf16 v[20:23], v[184:187], v[100:103], v[20:23]\n\t"
      "ds_read_b64_tr_b16 v[182:183], v237 offset:61440\n\t"
      "ds_read_b64_tr_b16 v[184:185], v236 offset:57856\n\t"
      "v_mfma_f32_16x16x32_bf16 v[24:27], v[188:191], v[96:99], v[24:27]\n\t"
      "v_exp_f32 v91, v91\n\t"
      "v_mfma_f32_16x16x32_bf16 v[28:31], v[188:191], v[100:103], v[28:31]\n\t"
      "ds_read_b64_tr_b16 v[186:187], v236 offset:61952\n\t"
      "v_cvt_pk_bf16_f32 v107, v90, v91\n\t"
      "v_mfma_f32_16x16x32_bf16 v[64:67], v[152:155], v[120:123], v[64:67]\n\t"
      "v_exp_f32 v84, v84\n\t"
      "v_mfma_f32_16x16x32_bf16 v[68:71], v[152:155], v[136:139], v[68:71]\n\t"
      "ds_read_b128 v[152:155], v234 offset:8192\n\t"
      "ds_read_b64_tr_b16 v[188:189], v237 offset:57856\n\t"
      "v_mfma_f32_16x16x32_bf16 v[72:75], v[168:171], v[120:123], v[72:75]\n\t"
      "v_exp_f32 v85, v85\n\t"
      "v_mfma_f32_16x16x32_bf16 v[76:79], v[168:171], v[136:139], v[76:79]\n\t"
      "ds_read_b128 v[168:171], v234 offset:12288\n\t"
      "v_cvt_pk_bf16_f32 v108, v84, v85\n\t"
      "v_mfma_f32_16x16x32_bf16 v[64:67], v[156:159], v[124:127], v[64:67]\n\t"
      "s_waitcnt lgkmcnt(15)\n\t"
      "v_exp_f32 v86, v86\n\t"
      "v_mfma_f32_16x16x32_bf16 v[68:71], v[156:159], v[140:143], v[68:71]\n\t"
      "ds_read_b128 v[156:159], v235 offset:8192\n\t"
      "ds_read_b64_tr_b16 v[190:191], v237 offset:61952\n\t"
      "v_mfma_f32_16x16x32_bf16 v[72:75], v[172:175], v[124:127], v[72:75]\n\t"
      "v_exp_f32 v87, v87\n\t"
      "v_mfma_f32_16x16x32_bf16 v[76:79], v[172:175], v[140:143], v[76:79]\n\t"
      "ds_read_b128 v[172:175], v235 offset:12288\n\t"
      "v_cvt_pk_bf16_f32 v109, v86, v87\n\t"
      "v_mfma_f32_16x16x32_bf16 v[32:35], v[192:195], v[96:99], v[32:35]\n\t"
      "v_exp_f32 v92, v92\n\t"
      "v_mfma_f32_16x16x32_bf16 v[36:39], v[192:195], v[100:103], v[36:39]\n\t"
      "ds_read_b64_tr_b16 v[192:193], v236 offset:58368\n\t"
      "ds_read_b64_tr_b16 v[194:195], v236 offset:62464\n\t"
      "v_mfma_f32_16x16x32_bf16 v[40:43], v[196:199], v[96:99], v[40:43]\n\t"
      "v_exp_f32 v93, v93\n\t"
      "v_mfma_f32_16x16x32_bf16 v[44:47], v[196:199], v[100:103], v[44:47]\n\t"
      "ds_read_b64_tr_b16 v[196:197], v237 offset:58368\n\t"
      "v_cvt_pk_bf16_f32 v110, v92, v93\n\t"
      "v_mfma_f32_16x16x32_bf16 v[48:51], v[200:203], v[96:99], v[48:51]\n\t"
      "s_waitcnt lgkmcnt(15)\n\t"
      "v_exp_f32 v94, v94\n\t"
      "v_mfma_f32_16x16x32_bf16 v[52:55], v[200:203], v[100:103], v[52:55]\n\t"
      "ds_read_b64_tr_b16 v[198:199], v237 offset:62464\n\t"
      "ds_read_b64_tr_b16 v[200:201], v236 offset:58880\n\t"
      "v_mfma_f32_16x16x32_bf16 v[56:59], v[204:207], v[96:99], v[56:59]\n\t"
      "v_exp_f32 v95, v95\n\t"
      "v_mfma_f32_16x16x32_bf16 v[60:63], v[204:207], v[100:103], v[60:63]\n\t"
      "ds_read_b64_tr_b16 v[202:203], v236 offset:62976\n\t"
      "v_cvt_pk_bf16_f32 v111, v94, v95\n\t"
      "v_mfma_f32_16x16x32_bf16 v[216:219], v[224:227], v[96:99], v[216:219]\n\t"
      "s_barrier\n\t"
      "v_mfma_f32_16x16x32_bf16 v[220:223], v[224:227], v[100:103], v[220:223]\n\t"
      "v_mfma_f32_16x16x32_bf16 v[80:83], v[144:147], v[112:115], v[208:211]\n\t"
      "v_exp_f32 v64, v64\n\t"
      "v_mfma_f32_16x16x32_bf16 v[84:87], v[144:147], v[128:131], v[212:215]\n\t"
      "ds_read_b128 v[144:147], v228 offset:0\n\t"
      "ds_read_b64_tr_b16 v[204:205], v237 offset:58880\n\t"
      "v_mfma_f32_16x16x32_bf16 v[88:91], v[160:163], v[112:115], v[208:211]\n\t"
      "v_exp_f32 v65, v65\n\t"
      "v_mfma_f32_16x16x32_bf16 v[92:95], v[160:163], v[128:131], v[212:215]\n\t"
      "ds_read_b128 v[160:163], v228 offset:4096\n\t"
      "v_cvt_pk_bf16_f32 v96, v64, v65\n\t"
      "v_mfma_f32_16x16x32_bf16 v[80:83], v[148:151], v[116:119], v[80:83]\n\t"
      "s_waitcnt lgkmcnt(15)\n\t"
      "v_exp_f32 v66, v66\n\t"
      "v_mfma_f32_16x16x32_bf16 v[84:87], v[148:151], v[132:135], v[84:87]\n\t"
      "ds_read_b128 v[148:151], v229 offset:0\n\t"
      "ds_read_b64_tr_b16 v[206:207], v237 offset:62976\n\t"
      "v_mfma_f32_16x16x32_bf16 v[88:91], v[164:167], v[116:119], v[88:91]\n\t"
      "v_exp_f32 v67, v67\n\t"
      "v_mfma_f32_16x16x32_bf16 v[92:95], v[164:167], v[132:135], v[92:95]\n\t"
      "ds_read_b128 v[164:167], v229 offset:4096\n\t"
      "v_cvt_pk_bf16_f32 v97, v66, v67\n\t"
      "v_mfma_f32_16x16x32_bf16 v[0:3], v[176:179], v[104:107], v[0:3]\n\t"
      "v_exp_f32 v72, v72\n\t"
      "v_mfma_f32_16x16x32_bf16 v[4:7], v[176:179], v[108:111], v[4:7]\n\t"
      "ds_read_b64_tr_b16 v[176:177], v238 offset:0\n\t"
      "ds_read_b64_tr_b16 v[178:179], v238 offset:4096\n\t"
      "v_mfma_f32_16x16x32_bf16 v[8:11], v[180:183], v[104:107], v[8:11]\n\t"
      "v_exp_f32 v73, v73\n\t"
      "v_mfma_f32_16x16x32_bf16 v[12:15], v[180:183], v[108:111], v[12:15]\n\t"
      "ds_read_b64_tr_b16 v[180:181], v239 offset:0\n\t"
      "v_cvt_pk_bf16_f32 v98, v72, v73\n\t"
      "v_mfma_f32_16x16x32_bf16 v[16:19], v[184:187], v[104:107], v[16:19]\n\t"
      "s_waitcnt lgkmcnt(15)\n\t"
      "v_exp_f32 v74, v74\n\t"
      "v_mfma_f32_16x16x32_bf16 v[20:23], v[184:187], v[108:111], v[20:23]\n\t"
      "ds_read_b64_tr_b16 v[182:183], v239 offset:4096\n\t"
      "ds_read_b64_tr_b16 v[184:185], v238 offset:512\n\t"
      "v_mfma_f32_16x16x32_bf16 v[24:27], v[188:191], v[104:107], v[24:27]\n\t"
      "v_exp_f32 v75, v75\n\t"
      "v_mfma_f32_16x16x32_bf16 v[28:31], v[188:191], v[108:111], v[28:31]\n\t"
      "ds_read_b64_tr_b16 v[186:187], v238 offset:4608\n\t"
      "v_cvt_pk_bf16_f32 v99, v74, v75\n\t"
      "v_mfma_f32_16x16x32_bf16 v[80:83], v[152:155], v[120:123], v[80:83]\n\t"
      "v_exp_f32 v68, v68\n\t"
      "v_mfma_f32_16x16x32_bf16 v[84:87], v[152:155], v[136:139], v[84:87]\n\t"
      "ds_read_b128 v[152:155], v230 offset:0\n\t"
      "ds_read_b64_tr_b16 v[188:189], v239 offset:512\n\t"
      "v_mfma_f32_16x16x32_bf16 v[88:91], v[168:171], v[120:123], v[88:91]\n\t"
      "v_exp_f32 v69, v69\n\t"
      "v_mfma_f32_16x16x32_bf16 v[92:95], v[168:171], v[136:139], v[92:95]\n\t"
      "ds_read_b128 v[168:171], v230 offset:4096\n\t"
      "v_cvt_pk_bf16_f32 v100, v68, v69\n\t"
      "v_mfma_f32_16x16x32_bf16 v[80:83], v[156:159], v[124:127], v[80:83]\n\t"
      "s_waitcnt lgkmcnt(15)\n\t"
      "v_exp_f32 v70, v70\n\t"
      "v_mfma_f32_16x16x32_bf16 v[84:87], v[156:159], v[140:143], v[84:87]\n\t"
      "ds_read_b128 v[156:159], v231 offset:0\n\t"
      "ds_read_b64_tr_b16 v[190:191], v239 offset:4608\n\t"
      "v_mfma_f32_16x16x32_bf16 v[88:91], v[172:175], v[124:127], v[88:91]\n\t"
      "v_exp_f32 v71, v71\n\t"
      "v_mfma_f32_16x16x32_bf16 v[92:95], v[172:175], v[140:143], v[92:95]\n\t"
      "ds_read_b128 v[172:175], v231 offset:4096\n\t"
      "v_cvt_pk_bf16_f32 v101, v70, v71\n\t"
      "v_mfma_f32_16x16x32_bf16 v[32:35], v[192:195], v[104:107], v[32:35]\n\t"
      "v_exp_f32 v76, v76\n\t"
      "v_mfma_f32_16x16x32_bf16 v[36:39], v[192:195], v[108:111], v[36:39]\n\t"
      "ds_read_b64_tr_b16 v[192:193], v238 offset:1024\n\t"
      "ds_read_b64_tr_b16 v[194:195], v238 offset:5120\n\t"
      "v_mfma_f32_16x16x32_bf16 v[40:43], v[196:199], v[104:107], v[40:43]\n\t"
      "v_exp_f32 v77, v77\n\t"
      "v_mfma_f32_16x16x32_bf16 v[44:47], v[196:199], v[108:111], v[44:47]\n\t"
      "ds_read_b64_tr_b16 v[196:197], v239 offset:1024\n\t"
      "v_cvt_pk_bf16_f32 v102, v76, v77\n\t"
      "v_mfma_f32_16x16x32_bf16 v[48:51], v[200:203], v[104:107], v[48:51]\n\t"
      "s_waitcnt lgkmcnt(15)\n\t"
      "v_exp_f32 v78, v78\n\t"
      "v_mfma_f32_16x16x32_bf16 v[52:55], v[200:203], v[108:111], v[52:55]\n\t"
      "ds_read_b64_tr_b16 v[198:199], v239 offset:5120\n\t"
      "ds_read_b64_tr_b16 v[200:201], v238 offset:1536\n\t"
      "v_mfma_f32_16x16x32_bf16 v[56:59], v[204:207], v[104:107], v[56:59]\n\t"
      "v_exp_f32 v79, v79\n\t"
      "v_mfma_f32_16x16x32_bf16 v[60:63], v[204:207], v[108:111], v[60:63]\n\t"
      "ds_read_b64_tr_b16 v[202:203], v238 offset:5632\n\t"
      "v_cvt_pk_bf16_f32 v103, v78, v79\n\t"
      "v_mfma_f32_16x16x32_bf16 v[216:219], v[224:227], v[104:107], v[216:219]\n\t"
      "v_mfma_f32_16x16x32_bf16 v[220:223], v[224:227], v[108:111], v[220:223]\n\t"
      "v_mfma_f32_16x16x32_bf16 v[64:67], v[144:147], v[112:115], v[208:211]\n\t"
      "v_exp_f32 v80, v80\n\t"
      "v_mfma_f32_16x16x32_bf16 v[68:71], v[144:147], v[128:131], v[212:215]\n\t"
      "ds_read_b128 v[144:147], v228 offset:8192\n\t"
      "ds_read_b64_tr_b16 v[204:205], v239 offset:1536\n\t"
      "v_mfma_f32_16x16x32_bf16 v[72:75], v[160:163], v[112:115], v[208:211]\n\t"
      "v_exp_f32 v81, v81\n\t"
      "v_mfma_f32_16x16x32_bf16 v[76:79], v[160:163], v[128:131], v[212:215]\n\t"
      "ds_read_b128 v[160:163], v228 offset:12288\n\t"
      "v_cvt_pk_bf16_f32 v104, v80, v81\n\t"
      "v_mfma_f32_16x16x32_bf16 v[64:67], v[148:151], v[116:119], v[64:67]\n\t"
      "s_waitcnt lgkmcnt(15)\n\t"
      "v_exp_f32 v82, v82\n\t"
      "v_mfma_f32_16x16x32_bf16 v[68:71], v[148:151], v[132:135], v[68:71]\n\t"
      "ds_read_b128 v[148:151], v229 offset:8192\n\t"
      "ds_read_b64_tr_b16 v[206:207], v239 offset:5632\n\t"
      "v_mfma_f32_16x16x32_bf16 v[72:75], v[164:167], v[116:119], v[72:75]\n\t"
      "v_exp_f32 v83, v83\n\t"
      "v_mfma_f32_16x16x32_bf16 v[76:79], v[164:167], v[132:135], v[76:79]\n\t"
      "ds_read_b128 v[164:167], v229 offset:12288\n\t"
      "v_cvt_pk_bf16_f32 v105, v82, v83\n\t"
      "v_mfma_f32_16x16x32_bf16 v[0:3], v[176:179], v[96:99], v[0:3]\n\t"
      "v_exp_f32 v88, v88\n\t"
      "v_mfma_f32_16x16x32_bf16 v[4:7], v[176:179], v[100:103], v[4:7]\n\t"
      "ds_read_b64_tr_b16 v[176:177], v238 offset:8192\n\t"
      "ds_read_b64_tr_b16 v[178:179], v238 offset:12288\n\t"
      "v_mfma_f32_16x16x32_bf16 v[8:11], v[180:183], v[96:99], v[8:11]\n\t"
      "v_exp_f32 v89, v89\n\t"
      "v_mfma_f32_16x16x32_bf16 v[12:15], v[180:183], v[100:103], v[12:15]\n\t"
      "ds_read_b64_tr_b16 v[180:181], v239 offset:8192\n\t"
      "v_cvt_pk_bf16_f32 v106, v88, v89\n\t"
      "v_mfma_f32_16x16x32_bf16 v[16:19], v[184:187], v[96:99], v[16:19]\n\t"
      "s_waitcnt lgkmcnt(15)\n\t"
      "v_exp_f32 v90, v90\n\t"
      "v_mfma_f32_16x16x32_bf16 v[20:23], v[184:187], v[100:103], v[20:23]\n\t"
      "ds_read_b64_tr_b16 v[182:183], v239 offset:12288\n\t"
      "ds_read_b64_tr_b16 v[184:185], v238 offset:8704\n\t"
      "v_mfma_f32_16x16x32_bf16 v[24:27], v[188:191], v[96:99], v[24:27]\n\t"
      "v_exp_f32 v91, v91\n\t"
      "v_mfma_f32_16x16x32_bf16 v[28:31], v[188:191], v[100:103], v[28:31]\n\t"
      "ds_read_b64_tr_b16 v[186:187], v238 offset:12800\n\t"
      "v_cvt_pk_bf16_f32 v107, v90, v91\n\t"
      "v_mfma_f32_16x16x32_bf16 v[64:67], v[152:155], v[120:123], v[64:67]\n\t"
      "v_exp_f32 v84, v84\n\t"
      "v_mfma_f32_16x16x32_bf16 v[68:71], v[152:155], v[136:139], v[68:71]\n\t"
      "ds_read_b128 v[152:155], v230 offset:8192\n\t"
      "ds_read_b64_tr_b16 v[188:189], v239 offset:8704\n\t"
      "v_mfma_f32_16x16x32_bf16 v[72:75], v[168:171], v[120:123], v[72:75]\n\t"
      "v_exp_f32 v85, v85\n\t"
      "v_mfma_f32_16x16x32_bf16 v[76:79], v[168:171], v[136:139], v[76:79]\n\t"
      "ds_read_b128 v[168:171], v230 offset:12288\n\t"
      "v_cvt_pk_bf16_f32 v108, v84, v85\n\t"
      "v_mfma_f32_16x16x32_bf16 v[64:67], v[156:159], v[124:127], v[64:67]\n\t"
      "s_waitcnt lgkmcnt(15)\n\t"
      "v_exp_f32 v86, v86\n\t"
      "v_mfma_f32_16x16x32_bf16 v[68:71], v[156:159], v[140:143], v[68:71]\n\t"
      "ds_read_b128 v[156:159], v231 offset:8192\n\t"
      "ds_read_b64_tr_b16 v[190:191], v239 offset:12800\n\t"
      "v_mfma_f32_16x16x32_bf16 v[72:75], v[172:175], v[124:127], v[72:75]\n\t"
      "v_exp_f32 v87, v87\n\t"
      "v_mfma_f32_16x16x32_bf16 v[76:79], v[172:175], v[140:143], v[76:79]\n\t"
      "ds_read_b128 v[172:175], v231 offset:12288\n\t"
      "v_cvt_pk_bf16_f32 v109, v86, v87\n\t"
      "v_mfma_f32_16x16x32_bf16 v[32:35], v[192:195], v[96:99], v[32:35]\n\t"
      "v_exp_f32 v92, v92\n\t"
      "v_mfma_f32_16x16x32_bf16 v[36:39], v[192:195], v[100:103], v[36:39]\n\t"
      "ds_read_b64_tr_b16 v[192:193], v238 offset:9216\n\t"
      "ds_read_b64_tr_b16 v[194:195], v238 offset:13312\n\t"
      "v_mfma_f32_16x16x32_bf16 v[40:43], v[196:199], v[96:99], v[40:43]\n\t"
      "v_exp_f32 v93, v93\n\t"
      "v_mfma_f32_16x16x32_bf16 v[44:47], v[196:199], v[100:103], v[44:47]\n\t"
      "ds_read_b64_tr_b16 v[196:197], v239 offset:9216\n\t"
      "v_cvt_pk_bf16_f32 v110, v92, v93\n\t"
      "v_mfma_f32_16x16x32_bf16 v[48:51], v[200:203], v[96:99], v[48:51]\n\t"
      "s_waitcnt lgkmcnt(15)\n\t"
      "v_exp_f32 v94, v94\n\t"
      "v_mfma_f32_16x16x32_bf16 v[52:55], v[200:203], v[100:103], v[52:55]\n\t"
      "ds_read_b64_tr_b16 v[198:199], v239 offset:13312\n\t"
      "ds_read_b64_tr_b16 v[200:201], v238 offset:9728\n\t"
      "v_mfma_f32_16x16x32_bf16 v[56:59], v[204:207], v[96:99], v[56:59]\n\t"
      "v_exp_f32 v95, v95\n\t"
      "v_mfma_f32_16x16x32_bf16 v[60:63], v[204:207], v[100:103], v[60:63]\n\t"
      "ds_read_b64_tr_b16 v[202:203], v238 offset:13824\n\t"
      "v_cvt_pk_bf16_f32 v111, v94, v95\n\t"
      "v_mfma_f32_16x16x32_bf16 v[216:219], v[224:227], v[96:99], v[216:219]\n\t"
      "s_barrier\n\t"
      "v_mfma_f32_16x16x32_bf16 v[220:223], v[224:227], v[100:103], v[220:223]\n\t"
      "v_mfma_f32_16x16x32_bf16 v[80:83], v[144:147], v[112:115], v[208:211]\n\t"
      "v_exp_f32 v64, v64\n\t"
      "v_mfma_f32_16x16x32_bf16 v[84:87], v[144:147], v[128:131], v[212:215]\n\t"
      "ds_read_b128 v[144:147], v228 offset:16384\n\t"
      "ds_read_b64_tr_b16 v[204:205], v239 offset:9728\n\t"
      "v_mfma_f32_16x16x32_bf16 v[88:91], v[160:163], v[112:115], v[208:211]\n\t"
      "v_exp_f32 v65, v65\n\t"
      "v_mfma_f32_16x16x32_bf16 v[92:95], v[160:163], v[128:131], v[212:215]\n\t"
      "ds_read_b128 v[160:163], v228 offset:20480\n\t"
      "v_cvt_pk_bf16_f32 v96, v64, v65\n\t"
      "v_mfma_f32_16x16x32_bf16 v[80:83], v[148:151], v[116:119], v[80:83]\n\t"
      "s_waitcnt lgkmcnt(15)\n\t"
      "v_exp_f32 v66, v66\n\t"
      "v_mfma_f32_16x16x32_bf16 v[84:87], v[148:151], v[132:135], v[84:87]\n\t"
      "ds_read_b128 v[148:151], v229 offset:16384\n\t"
      "ds_read_b64_tr_b16 v[206:207], v239 offset:13824\n\t"
      "v_mfma_f32_16x16x32_bf16 v[88:91], v[164:167], v[116:119], v[88:91]\n\t"
      "v_exp_f32 v67, v67\n\t"
      "v_mfma_f32_16x16x32_bf16 v[92:95], v[164:167], v[132:135], v[92:95]\n\t"
      "ds_read_b128 v[164:167], v229 offset:20480\n\t"
      "v_cvt_pk_bf16_f32 v97, v66, v67\n\t"
      "v_mfma_f32_16x16x32_bf16 v[0:3], v[176:179], v[104:107], v[0:3]\n\t"
      "v_exp_f32 v72, v72\n\t"
      "v_mfma_f32_16x16x32_bf16 v[4:7], v[176:179], v[108:111], v[4:7]\n\t"
      "ds_read_b64_tr_b16 v[176:177], v236 offset:0\n\t"
      "ds_read_b64_tr_b16 v[178:179], v236 offset:4096\n\t"
      "v_mfma_f32_16x16x32_bf16 v[8:11], v[180:183], v[104:107], v[8:11]\n\t"
      "v_exp_f32 v73, v73\n\t"
      "v_mfma_f32_16x16x32_bf16 v[12:15], v[180:183], v[108:111], v[12:15]\n\t"
      "ds_read_b64_tr_b16 v[180:181], v237 offset:0\n\t"
      "v_cvt_pk_bf16_f32 v98, v72, v73\n\t"
      "v_mfma_f32_16x16x32_bf16 v[16:19], v[184:187], v[104:107], v[16:19]\n\t"
      "s_waitcnt lgkmcnt(15)\n\t"
      "v_exp_f32 v74, v74\n\t"
      "v_mfma_f32_16x16x32_bf16 v[20:23], v[184:187], v[108:111], v[20:23]\n\t"
      "ds_read_b64_tr_b16 v[182:183], v237 offset:4096\n\t"
      "ds_read_b64_tr_b16 v[184:185], v236 offset:512\n\t"
      "v_mfma_f32_16x16x32_bf16 v[24:27], v[188:191], v[104:107], v[24:27]\n\t"
      "v_exp_f32 v75, v75\n\t"
      "v_mfma_f32_16x16x32_bf16 v[28:31], v[188:191], v[108:111], v[28:31]\n\t"
      "ds_read_b64_tr_b16 v[186:187], v236 offset:4608\n\t"
      "v_cvt_pk_bf16_f32 v99, v74, v75\n\t"
      "v_mfma_f32_16x16x32_bf16 v[80:83], v[152:155], v[120:123], v[80:83]\n\t"
      "v_exp_f32 v68, v68\n\t"
      "v_mfma_f32_16x16x32_bf16 v[84:87], v[152:155], v[136:139], v[84:87]\n\t"
      "ds_read_b128 v[152:155], v230 offset:16384\n\t"
      "ds_read_b64_tr_b16 v[188:189], v237 offset:512\n\t"
      "v_mfma_f32_16x16x32_bf16 v[88:91], v[168:171], v[120:123], v[88:91]\n\t"
      "v_exp_f32 v69, v69\n\t"
      "v_mfma_f32_16x16x32_bf16 v[92:95], v[168:171], v[136:139], v[92:95]\n\t"
      "ds_read_b128 v[168:171], v230 offset:20480\n\t"
      "v_cvt_pk_bf16_f32 v100, v68, v69\n\t"
      "v_mfma_f32_16x16x32_bf16 v[80:83], v[156:159], v[124:127], v[80:83]\n\t"
      "s_waitcnt lgkmcnt(15)\n\t"
      "v_exp_f32 v70, v70\n\t"
      "v_mfma_f32_16x16x32_bf16 v[84:87], v[156:159], v[140:143], v[84:87]\n\t"
      "ds_read_b128 v[156:159], v231 offset:16384\n\t"
      "ds_read_b64_tr_b16 v[190:191], v237 offset:4608\n\t"
      "v_mfma_f32_16x16x32_bf16 v[88:91], v[172:175], v[124:127], v[88:91]\n\t"
      "v_exp_f32 v71, v71\n\t"
      "v_mfma_f32_16x16x32_bf16 v[92:95], v[172:175], v[140:143], v[92:95]\n\t"
      "ds_read_b128 v[172:175], v231 offset:20480\n\t"
      "v_cvt_pk_bf16_f32 v101, v70, v71\n\t"
      "v_mfma_f32_16x16x32_bf16 v[32:35], v[192:195], v[104:107], v[32:35]\n\t"
      "v_exp_f32 v76, v76\n\t"
      "v_mfma_f32_16x16x32_bf16 v[36:39], v[192:195], v[108:111], v[36:39]\n\t"
      "ds_read_b64_tr_b16 v[192:193], v236 offset:1024\n\t"
      "ds_read_b64_tr_b16 v[194:195], v236 offset:5120\n\t"
      "v_mfma_f32_16x16x32_bf16 v[40:43], v[196:199], v[104:107], v[40:43]\n\t"
      "v_exp_f32 v77, v77\n\t"
      "v_mfma_f32_16x16x32_bf16 v[44:47], v[196:199], v[108:111], v[44:47]\n\t"
      "ds_read_b64_tr_b16 v[196:197], v237 offset:1024\n\t"
      "v_cvt_pk_bf16_f32 v102, v76, v77\n\t"
      "v_mfma_f32_16x16x32_bf16 v[48:51], v[200:203], v[104:107], v[48:51]\n\t"
      "s_waitcnt lgkmcnt(15)\n\t"
      "v_exp_f32 v78, v78\n\t"
      "v_mfma_f32_16x16x32_bf16 v[52:55], v[200:203], v[108:111], v[52:55]\n\t"
      "ds_read_b64_tr_b16 v[198:199], v237 offset:5120\n\t"
      "ds_read_b64_tr_b16 v[200:201], v236 offset:1536\n\t"
      "v_mfma_f32_16x16x32_bf16 v[56:59], v[204:207], v[104:107], v[56:59]\n\t"
      "v_exp_f32 v79, v79\n\t"
      "v_mfma_f32_16x16x32_bf16 v[60:63], v[204:207], v[108:111], v[60:63]\n\t"
      "ds_read_b64_tr_b16 v[202:203], v236 offset:5632\n\t"
      "v_cvt_pk_bf16_f32 v103, v78, v79\n\t"
      "v_mfma_f32_16x16x32_bf16 v[216:219], v[224:227], v[104:107], v[216:219]\n\t"
      "v_mfma_f32_16x16x32_bf16 v[220:223], v[224:227], v[108:111], v[220:223]\n\t"
      "v_mfma_f32_16x16x32_bf16 v[64:67], v[144:147], v[112:115], v[208:211]\n\t"
      "v_exp_f32 v80, v80\n\t"
      "v_mfma_f32_16x16x32_bf16 v[68:71], v[144:147], v[128:131], v[212:215]\n\t"
      "ds_read_b128 v[144:147], v228 offset:24576\n\t"
      "ds_read_b64_tr_b16 v[204:205], v237 offset:1536\n\t"
      "v_mfma_f32_16x16x32_bf16 v[72:75], v[160:163], v[112:115], v[208:211]\n\t"
      "v_exp_f32 v81, v81\n\t"
      "v_mfma_f32_16x16x32_bf16 v[76:79], v[160:163], v[128:131], v[212:215]\n\t"
      "ds_read_b128 v[160:163], v228 offset:28672\n\t"
      "v_cvt_pk_bf16_f32 v104, v80, v81\n\t"
      "v_mfma_f32_16x16x32_bf16 v[64:67], v[148:151], v[116:119], v[64:67]\n\t"
      "s_waitcnt lgkmcnt(15)\n\t"
      "v_exp_f32 v82, v82\n\t"
      "v_mfma_f32_16x16x32_bf16 v[68:71], v[148:151], v[132:135], v[68:71]\n\t"
      "ds_read_b128 v[148:151], v229 offset:24576\n\t"
      "ds_read_b64_tr_b16 v[206:207], v237 offset:5632\n\t"
      "v_mfma_f32_16x16x32_bf16 v[72:75], v[164:167], v[116:119], v[72:75]\n\t"
      "v_exp_f32 v83, v83\n\t"
      "v_mfma_f32_16x16x32_bf16 v[76:79], v[164:167], v[132:135], v[76:79]\n\t"
      "ds_read_b128 v[164:167], v229 offset:28672\n\t"
      "v_cvt_pk_bf16_f32 v105, v82, v83\n\t"
      "v_mfma_f32_16x16x32_bf16 v[0:3], v[176:179], v[96:99], v[0:3]\n\t"
      "v_exp_f32 v88, v88\n\t"
      "v_mfma_f32_16x16x32_bf16 v[4:7], v[176:179], v[100:103], v[4:7]\n\t"
      "ds_read_b64_tr_b16 v[176:177], v236 offset:8192\n\t"
      "ds_read_b64_tr_b16 v[178:179], v236 offset:12288\n\t"
      "v_mfma_f32_16x16x32_bf16 v[8:11], v[180:183], v[96:99], v[8:11]\n\t"
      "v_exp_f32 v89, v89\n\t"
      "v_mfma_f32_16x16x32_bf16 v[12:15], v[180:183], v[100:103], v[12:15]\n\t"
      "ds_read_b64_tr_b16 v[180:181], v237 offset:8192\n\t"
      "v_cvt_pk_bf16_f32 v106, v88, v89\n\t"
      "v_mfma_f32_16x16x32_bf16 v[16:19], v[184:187], v[96:99], v[16:19]\n\t"
      "s_waitcnt lgkmcnt(15)\n\t"
      "v_exp_f32 v90, v90\n\t"
      "v_mfma_f32_16x16x32_bf16 v[20:23], v[184:187], v[100:103], v[20:23]\n\t"
      "ds_read_b64_tr_b16 v[182:183], v237 offset:12288\n\t"
      "ds_read_b64_tr_b16 v[184:185], v236 offset:8704\n\t"
      "v_mfma_f32_16x16x32_bf16 v[24:27], v[188:191], v[96:99], v[24:27]\n\t"
      "v_exp_f32 v91, v91\n\t"
      "v_mfma_f32_16x16x32_bf16 v[28:31], v[188:191], v[100:103], v[28:31]\n\t"
      "ds_read_b64_tr_b16 v[186:187], v236 offset:12800\n\t"
      "v_cvt_pk_bf16_f32 v107, v90, v91\n\t"
      "v_mfma_f32_16x16x32_bf16 v[64:67], v[152:155], v[120:123], v[64:67]\n\t"
      "v_exp_f32 v84, v84\n\t"
      "v_mfma_f32_16x16x32_bf16 v[68:71], v[152:155], v[136:139], v[68:71]\n\t"
      "ds_read_b128 v[152:155], v230 offset:24576\n\t"
      "ds_read_b64_tr_b16 v[188:189], v237 offset:8704\n\t"
      "v_mfma_f32_16x16x32_bf16 v[72:75], v[168:171], v[120:123], v[72:75]\n\t"
      "v_exp_f32 v85, v85\n\t"
      "v_mfma_f32_16x16x32_bf16 v[76:79], v[168:171], v[136:139], v[76:79]\n\t"
      "ds_read_b128 v[168:171], v230 offset:28672\n\t"
      "v_cvt_pk_bf16_f32 v108, v84, v85\n\t"
      "v_mfma_f32_16x16x32_bf16 v[64:67], v[156:159], v[124:127], v[64:67]\n\t"
      "s_waitcnt lgkmcnt(15)\n\t"
      "v_exp_f32 v86, v86\n\t"
      "v_mfma_f32_16x16x32_bf16 v[68:71], v[156:159], v[140:143], v[68:71]\n\t"
      "ds_read_b128 v[156:159], v231 offset:24576\n\t"
      "ds_read_b64_tr_b16 v[190:191], v237 offset:12800\n\t"
      "v_mfma_f32_16x16x32_bf16 v[72:75], v[172:175], v[124:127], v[72:75]\n\t"
      "v_exp_f32 v87, v87\n\t"
      "v_mfma_f32_16x16x32_bf16 v[76:79], v[172:175], v[140:143], v[76:79]\n\t"
      "ds_read_b128 v[172:175], v231 offset:28672\n\t"
      "v_cvt_pk_bf16_f32 v109, v86, v87\n\t"
      "v_mfma_f32_16x16x32_bf16 v[32:35], v[192:195], v[96:99], v[32:35]\n\t"
      "v_exp_f32 v92, v92\n\t"
      "v_mfma_f32_16x16x32_bf16 v[36:39], v[192:195], v[100:103], v[36:39]\n\t"
      "ds_read_b64_tr_b16 v[192:193], v236 offset:9216\n\t"
      "ds_read_b64_tr_b16 v[194:195], v236 offset:13312\n\t"
      "v_mfma_f32_16x16x32_bf16 v[40:43], v[196:199], v[96:99], v[40:43]\n\t"
      "v_exp_f32 v93, v93\n\t"
      "v_mfma_f32_16x16x32_bf16 v[44:47], v[196:199], v[100:103], v[44:47]\n\t"
      "ds_read_b64_tr_b16 v[196:197], v237 offset:9216\n\t"
      "v_cvt_pk_bf16_f32 v110, v92, v93\n\t"
      "v_mfma_f32_16x16x32_bf16 v[48:51], v[200:203], v[96:99], v[48:51]\n\t"
      "s_waitcnt lgkmcnt(15)\n\t"
      "v_exp_f32 v94, v94\n\t"
      "v_mfma_f32_16x16x32_bf16 v[52:55], v[200:203], v[100:103], v[52:55]\n\t"
      "ds_read_b64_tr_b16 v[198:199], v237 offset:13312\n\t"
      "ds_read_b64_tr_b16 v[200:201], v236 offset:9728\n\t"
      "v_mfma_f32_16x16x32_bf16 v[56:59], v[204:207], v[96:99], v[56:59]\n\t"
      "v_exp_f32 v95, v95\n\t"
      "v_mfma_f32_16x16x32_bf16 v[60:63], v[204:207], v[100:103], v[60:63]\n\t"
      "ds_read_b64_tr_b16 v[202:203], v236 offset:13824\n\t"
      "v_cvt_pk_bf16_f32 v111, v94, v95\n\t"
      "v_mfma_f32_16x16x32_bf16 v[216:219], v[224:227], v[96:99], v[216:219]\n\t"
      "s_barrier\n\t"
      "v_mfma_f32_16x16x32_bf16 v[220:223], v[224:227], v[100:103], v[220:223]\n\t"
      "v_mfma_f32_16x16x32_bf16 v[80:83], v[144:147], v[112:115], v[208:211]\n\t"
      "v_exp_f32 v64, v64\n\t"
      "v_mfma_f32_16x16x32_bf16 v[84:87], v[144:147], v[128:131], v[212:215]\n\t"
      "ds_read_b128 v[144:147], v228 offset:32768\n\t"
      "ds_read_b64_tr_b16 v[204:205], v237 offset:9728\n\t"
      "v_mfma_f32_16x16x32_bf16 v[88:91], v[160:163], v[112:115], v[208:211]\n\t"
      "v_exp_f32 v65, v65\n\t"
      "v_mfma_f32_16x16x32_bf16 v[92:95], v[160:163], v[128:131], v[212:215]\n\t"
      "ds_read_b128 v[160:163], v228 offset:36864\n\t"
      "v_cvt_pk_bf16_f32 v96, v64, v65\n\t"
      "v_mfma_f32_16x16x32_bf16 v[80:83], v[148:151], v[116:119], v[80:83]\n\t"
      "s_waitcnt lgkmcnt(15)\n\t"
      "v_exp_f32 v66, v66\n\t"
      "v_mfma_f32_16x16x32_bf16 v[84:87], v[148:151], v[132:135], v[84:87]\n\t"
      "ds_read_b128 v[148:151], v229 offset:32768\n\t"
      "ds_read_b64_tr_b16 v[206:207], v237 offset:13824\n\t"
      "v_mfma_f32_16x16x32_bf16 v[88:91], v[164:167], v[116:119], v[88:91]\n\t"
      "v_exp_f32 v67, v67\n\t"
      "v_mfma_f32_16x16x32_bf16 v[92:95], v[164:167], v[132:135], v[92:95]\n\t"
      "ds_read_b128 v[164:167], v229 offset:36864\n\t"
      "v_cvt_pk_bf16_f32 v97, v66, v67\n\t"
      "v_mfma_f32_16x16x32_bf16 v[0:3], v[176:179], v[104:107], v[0:3]\n\t"
      "v_exp_f32 v72, v72\n\t"
      "v_mfma_f32_16x16x32_bf16 v[4:7], v[176:179], v[108:111], v[4:7]\n\t"
      "ds_read_b64_tr_b16 v[176:177], v236 offset:16384\n\t"
      "ds_read_b64_tr_b16 v[178:179], v236 offset:20480\n\t"
      "v_mfma_f32_16x16x32_bf16 v[8:11], v[180:183], v[104:107], v[8:11]\n\t"
      "v_exp_f32 v73, v73\n\t"
      "v_mfma_f32_16x16x32_bf16 v[12:15], v[180:183], v[108:111], v[12:15]\n\t"
      "ds_read_b64_tr_b16 v[180:181], v237 offset:16384\n\t"
      "v_cvt_pk_bf16_f32 v98, v72, v73\n\t"
      "v_mfma_f32_16x16x32_bf16 v[16:19], v[184:187], v[104:107], v[16:19]\n\t"
      "s_waitcnt lgkmcnt(15)\n\t"
      "v_exp_f32 v74, v74\n\t"
      "v_mfma_f32_16x16x32_bf16 v[20:23], v[184:187], v[108:111], v[20:23]\n\t"
      "ds_read_b64_tr_b16 v[182:183], v237 offset:20480\n\t"
      "ds_read_b64_tr_b16 v[184:185], v236 offset:16896\n\t"
      "v_mfma_f32_16x16x32_bf16 v[24:27], v[188:191], v[104:107], v[24:27]\n\t"
      "v_exp_f32 v75, v75\n\t"
      "v_mfma_f32_16x16x32_bf16 v[28:31], v[188:191], v[108:111], v[28:31]\n\t"
      "ds_read_b64_tr_b16 v[186:187], v236 offset:20992\n\t"
      "v_cvt_pk_bf16_f32 v99, v74, v75\n\t"
      "v_mfma_f32_16x16x32_bf16 v[80:83], v[152:155], v[120:123], v[80:83]\n\t"
      "v_exp_f32 v68, v68\n\t"
      "v_mfma_f32_16x16x32_bf16 v[84:87], v[152:155], v[136:139], v[84:87]\n\t"
      "ds_read_b128 v[152:155], v230 offset:32768\n\t"
      "ds_read_b64_tr_b16 v[188:189], v237 offset:16896\n\t"
      "v_mfma_f32_16x16x32_bf16 v[88:91], v[168:171], v[120:123], v[88:91]\n\t"
      "v_exp_f32 v69, v69\n\t"
      "v_mfma_f32_16x16x32_bf16 v[92:95], v[168:171], v[136:139], v[92:95]\n\t"
      "ds_read_b128 v[168:171], v230 offset:36864\n\t"
      "v_cvt_pk_bf16_f32 v100, v68, v69\n\t"
      "v_mfma_f32_16x16x32_bf16 v[80:83], v[156:159], v[124:127], v[80:83]\n\t"
      "s_waitcnt lgkmcnt(15)\n\t"
      "v_exp_f32 v70, v70\n\t"
      "v_mfma_f32_16x16x32_bf16 v[84:87], v[156:159], v[140:143], v[84:87]\n\t"
      "ds_read_b128 v[156:159], v231 offset:32768\n\t"
      "ds_read_b64_tr_b16 v[190:191], v237 offset:20992\n\t"
      "v_mfma_f32_16x16x32_bf16 v[88:91], v[172:175], v[124:127], v[88:91]\n\t"
      "v_exp_f32 v71, v71\n\t"
      "v_mfma_f32_16x16x32_bf16 v[92:95], v[172:175], v[140:143], v[92:95]\n\t"
      "ds_read_b128 v[172:175], v231 offset:36864\n\t"
      "v_cvt_pk_bf16_f32 v101, v70, v71\n\t"
      "v_mfma_f32_16x16x32_bf16 v[32:35], v[192:195], v[104:107], v[32:35]\n\t"
      "v_exp_f32 v76, v76\n\t"
      "v_mfma_f32_16x16x32_bf16 v[36:39], v[192:195], v[108:111], v[36:39]\n\t"
      "ds_read_b64_tr_b16 v[192:193], v236 offset:17408\n\t"
      "ds_read_b64_tr_b16 v[194:195], v236 offset:21504\n\t"
      "v_mfma_f32_16x16x32_bf16 v[40:43], v[196:199], v[104:107], v[40:43]\n\t"
      "v_exp_f32 v77, v77\n\t"
      "v_mfma_f32_16x16x32_bf16 v[44:47], v[196:199], v[108:111], v[44:47]\n\t"
      "ds_read_b64_tr_b16 v[196:197], v237 offset:17408\n\t"
      "v_cvt_pk_bf16_f32 v102, v76, v77\n\t"
      "v_mfma_f32_16x16x32_bf16 v[48:51], v[200:203], v[104:107], v[48:51]\n\t"
      "s_waitcnt lgkmcnt(15)\n\t"
      "v_exp_f32 v78, v78\n\t"
      "v_mfma_f32_16x16x32_bf16 v[52:55], v[200:203], v[108:111], v[52:55]\n\t"
      "ds_read_b64_tr_b16 v[198:199], v237 offset:21504\n\t"
      "ds_read_b64_tr_b16 v[200:201], v236 offset:17920\n\t"
      "v_mfma_f32_16x16x32_bf16 v[56:59], v[204:207], v[104:107], v[56:59]\n\t"
      "v_exp_f32 v79, v79\n\t"
      "v_mfma_f32_16x16x32_bf16 v[60:63], v[204:207], v[108:111], v[60:63]\n\t"
      "ds_read_b64_tr_b16 v[202:203], v236 offset:22016\n\t"
      "v_cvt_pk_bf16_f32 v103, v78, v79\n\t"
      "v_mfma_f32_16x16x32_bf16 v[216:219], v[224:227], v[104:107], v[216:219]\n\t"
      "v_mfma_f32_16x16x32_bf16 v[220:223], v[224:227], v[108:111], v[220:223]\n\t"
      "v_mfma_f32_16x16x32_bf16 v[64:67], v[144:147], v[112:115], v[208:211]\n\t"
      "v_exp_f32 v80, v80\n\t"
      "v_mfma_f32_16x16x32_bf16 v[68:71], v[144:147], v[128:131], v[212:215]\n\t"
      "ds_read_b128 v[144:147], v228 offset:40960\n\t"
      "ds_read_b64_tr_b16 v[204:205], v237 offset:17920\n\t"
      "v_mfma_f32_16x16x32_bf16 v[72:75], v[160:163], v[112:115], v[208:211]\n\t"
      "v_exp_f32 v81, v81\n\t"
      "v_mfma_f32_16x16x32_bf16 v[76:79], v[160:163], v[128:131], v[212:215]\n\t"
      "ds_read_b128 v[160:163], v228 offset:45056\n\t"
      "v_cvt_pk_bf16_f32 v104, v80, v81\n\t"
      "v_mfma_f32_16x16x32_bf16 v[64:67], v[148:151], v[116:119], v[64:67]\n\t"
      "s_waitcnt lgkmcnt(15)\n\t"
      "v_exp_f32 v82, v82\n\t"
      "v_mfma_f32_16x16x32_bf16 v[68:71], v[148:151], v[132:135], v[68:71]\n\t"
      "ds_read_b128 v[148:151], v229 offset:40960\n\t"
      "ds_read_b64_tr_b16 v[206:207], v237 offset:22016\n\t"
      "v_mfma_f32_16x16x32_bf16 v[72:75], v[164:167], v[116:119], v[72:75]\n\t"
      "v_exp_f32 v83, v83\n\t"
      "v_mfma_f32_16x16x32_bf16 v[76:79], v[164:167], v[132:135], v[76:79]\n\t"
      "ds_read_b128 v[164:167], v229 offset:45056\n\t"
      "v_cvt_pk_bf16_f32 v105, v82, v83\n\t"
      "v_mfma_f32_16x16x32_bf16 v[0:3], v[176:179], v[96:99], v[0:3]\n\t"
      "v_exp_f32 v88, v88\n\t"
      "v_mfma_f32_16x16x32_bf16 v[4:7], v[176:179], v[100:103], v[4:7]\n\t"
      "ds_read_b64_tr_b16 v[176:177], v236 offset:24576\n\t"
      "ds_read_b64_tr_b16 v[178:179], v236 offset:28672\n\t"
      "v_mfma_f32_16x16x32_bf16 v[8:11], v[180:183], v[96:99], v[8:11]\n\t"
      "v_exp_f32 v89, v89\n\t"
      "v_mfma_f32_16x16x32_bf16 v[12:15], v[180:183], v[100:103], v[12:15]\n\t"
      "ds_read_b64_tr_b16 v[180:181], v237 offset:24576\n\t"
      "v_cvt_pk_bf16_f32 v106, v88, v89\n\t"
      "v_mfma_f32_16x16x32_bf16 v[16:19], v[184:187], v[96:99], v[16:19]\n\t"
      "s_waitcnt lgkmcnt(15)\n\t"
      "v_exp_f32 v90, v90\n\t"
      "v_mfma_f32_16x16x32_bf16 v[20:23], v[184:187], v[100:103], v[20:23]\n\t"
      "ds_read_b64_tr_b16 v[182:183], v237 offset:28672\n\t"
      "ds_read_b64_tr_b16 v[184:185], v236 offset:25088\n\t"
      "v_mfma_f32_16x16x32_bf16 v[24:27], v[188:191], v[96:99], v[24:27]\n\t"
      "v_exp_f32 v91, v91\n\t"
      "v_mfma_f32_16x16x32_bf16 v[28:31], v[188:191], v[100:103], v[28:31]\n\t"
      "ds_read_b64_tr_b16 v[186:187], v236 offset:29184\n\t"
      "v_cvt_pk_bf16_f32 v107, v90, v91\n\t"
      "v_mfma_f32_16x16x32_bf16 v[64:67], v[152:155], v[120:123], v[64:67]\n\t"
      "v_exp_f32 v84, v84\n\t"
      "v_mfma_f32_16x16x32_bf16 v[68:71], v[152:155], v[136:139], v[68:71]\n\t"
      "ds_read_b128 v[152:155], v230 offset:40960\n\t"
      "ds_read_b64_tr_b16 v[188:189], v237 offset:25088\n\t"
      "v_mfma_f32_16x16x32_bf16 v[72:75], v[168:171], v[120:123], v[72:75]\n\t"
      "v_exp_f32 v85, v85\n\t"
      "v_mfma_f32_16x16x32_bf16 v[76:79], v[168:171], v[136:139], v[76:79]\n\t"
      "ds_read_b128 v[168:171], v230 offset:45056\n\t"
      "v_cvt_pk_bf16_f32 v108, v84, v85\n\t"
      "v_mfma_f32_16x16x32_bf16 v[64:67], v[156:159], v[124:127], v[64:67]\n\t"
      "s_waitcnt lgkmcnt(15)\n\t"
      "v_exp_f32 v86, v86\n\t"
      "v_mfma_f32_16x16x32_bf16 v[68:71], v[156:159], v[140:143], v[68:71]\n\t"
      "ds_read_b128 v[156:159], v231 offset:40960\n\t"
      "ds_read_b64_tr_b16 v[190:191], v237 offset:29184\n\t"
      "v_mfma_f32_16x16x32_bf16 v[72:75], v[172:175], v[124:127], v[72:75]\n\t"
      "v_exp_f32 v87, v87\n\t"
      "v_mfma_f32_16x16x32_bf16 v[76:79], v[172:175], v[140:143], v[76:79]\n\t"
      "ds_read_b128 v[172:175], v231 offset:45056\n\t"
      "v_cvt_pk_bf16_f32 v109, v86, v87\n\t"
      "v_mfma_f32_16x16x32_bf16 v[32:35], v[192:195], v[96:99], v[32:35]\n\t"
      "v_exp_f32 v92, v92\n\t"
      "v_mfma_f32_16x16x32_bf16 v[36:39], v[192:195], v[100:103], v[36:39]\n\t"
      "ds_read_b64_tr_b16 v[192:193], v236 offset:25600\n\t"
      "ds_read_b64_tr_b16 v[194:195], v236 offset:29696\n\t"
      "v_mfma_f32_16x16x32_bf16 v[40:43], v[196:199], v[96:99], v[40:43]\n\t"
      "v_exp_f32 v93, v93\n\t"
      "v_mfma_f32_16x16x32_bf16 v[44:47], v[196:199], v[100:103], v[44:47]\n\t"
      "ds_read_b64_tr_b16 v[196:197], v237 offset:25600\n\t"
      "v_cvt_pk_bf16_f32 v110, v92, v93\n\t"
      "v_mfma_f32_16x16x32_bf16 v[48:51], v[200:203], v[96:99], v[48:51]\n\t"
      "s_waitcnt lgkmcnt(15)\n\t"
      "v_exp_f32 v94, v94\n\t"
      "v_mfma_f32_16x16x32_bf16 v[52:55], v[200:203], v[100:103], v[52:55]\n\t"
      "ds_read_b64_tr_b16 v[198:199], v237 offset:29696\n\t"
      "ds_read_b64_tr_b16 v[200:201], v236 offset:26112\n\t"
      "v_mfma_f32_16x16x32_bf16 v[56:59], v[204:207], v[96:99], v[56:59]\n\t"
      "v_exp_f32 v95, v95\n\t"
      "v_mfma_f32_16x16x32_bf16 v[60:63], v[204:207], v[100:103], v[60:63]\n\t"
      "ds_read_b64_tr_b16 v[202:203], v236 offset:30208\n\t"
      "v_cvt_pk_bf16_f32 v111, v94, v95\n\t"
      "v_mfma_f32_16x16x32_bf16 v[216:219], v[224:227], v[96:99], v[216:219]\n\t"
      "s_barrier\n\t"
      "v_mfma_f32_16x16x32_bf16 v[220:223], v[224:227], v[100:103], v[220:223]\n\t"
      "v_mfma_f32_16x16x32_bf16 v[80:83], v[144:147], v[112:115], v[208:211]\n\t"
      "v_exp_f32 v64, v64\n\t"
      "v_mfma_f32_16x16x32_bf16 v[84:87], v[144:147], v[128:131], v[212:215]\n\t"
      "ds_read_b128 v[144:147], v228 offset:49152\n\t"
      "ds_read_b64_tr_b16 v[204:205], v237 offset:26112\n\t"
      "v_mfma_f32_16x16x32_bf16 v[88:91], v[160:163], v[112:115], v[208:211]\n\t"
      "v_exp_f32 v65, v65\n\t"
      "v_mfma_f32_16x16x32_bf16 v[92:95], v[160:163], v[128:131], v[212:215]\n\t"
      "ds_read_b128 v[160:163], v228 offset:53248\n\t"
      "v_cvt_pk_bf16_f32 v96, v64, v65\n\t"
      "v_mfma_f32_16x16x32_bf16 v[80:83], v[148:151], v[116:119], v[80:83]\n\t"
      "s_waitcnt lgkmcnt(15)\n\t"
      "v_exp_f32 v66, v66\n\t"
      "v_mfma_f32_16x16x32_bf16 v[84:87], v[148:151], v[132:135], v[84:87]\n\t"
      "ds_read_b128 v[148:151], v229 offset:49152\n\t"
      "ds_read_b64_tr_b16 v[206:207], v237 offset:30208\n\t"
      "v_mfma_f32_16x16x32_bf16 v[88:91], v[164:167], v[116:119], v[88:91]\n\t"
      "v_exp_f32 v67, v67\n\t"
      "v_mfma_f32_16x16x32_bf16 v[92:95], v[164:167], v[132:135], v[92:95]\n\t"
      "ds_read_b128 v[164:167], v229 offset:53248\n\t"
      "v_cvt_pk_bf16_f32 v97, v66, v67\n\t"
      "v_mfma_f32_16x16x32_bf16 v[0:3], v[176:179], v[104:107], v[0:3]\n\t"
      "v_exp_f32 v72, v72\n\t"
      "v_mfma_f32_16x16x32_bf16 v[4:7], v[176:179], v[108:111], v[4:7]\n\t"
      "ds_read_b64_tr_b16 v[176:177], v236 offset:32768\n\t"
      "ds_read_b64_tr_b16 v[178:179], v236 offset:36864\n\t"
      "v_mfma_f32_16x16x32_bf16 v[8:11], v[180:183], v[104:107], v[8:11]\n\t"
      "v_exp_f32 v73, v73\n\t"
      "v_mfma_f32_16x16x32_bf16 v[12:15], v[180:183], v[108:111], v[12:15]\n\t"
      "ds_read_b64_tr_b16 v[180:181], v237 offset:32768\n\t"
      "v_cvt_pk_bf16_f32 v98, v72, v73\n\t"
      "v_mfma_f32_16x16x32_bf16 v[16:19], v[184:187], v[104:107], v[16:19]\n\t"
      "s_waitcnt lgkmcnt(15)\n\t"
      "v_exp_f32 v74, v74\n\t"
      "v_mfma_f32_16x16x32_bf16 v[20:23], v[184:187], v[108:111], v[20:23]\n\t"
      "ds_read_b64_tr_b16 v[182:183], v237 offset:36864\n\t"
      "ds_read_b64_tr_b16 v[184:185], v236 offset:33280\n\t"
      "v_mfma_f32_16x16x32_bf16 v[24:27], v[188:191], v[104:107], v[24:27]\n\t"
      "v_exp_f32 v75, v75\n\t"
      "v_mfma_f32_16x16x32_bf16 v[28:31], v[188:191], v[108:111], v[28:31]\n\t"
      "ds_read_b64_tr_b16 v[186:187], v236 offset:37376\n\t"
      "v_cvt_pk_bf16_f32 v99, v74, v75\n\t"
      "v_mfma_f32_16x16x32_bf16 v[80:83], v[152:155], v[120:123], v[80:83]\n\t"
      "v_exp_f32 v68, v68\n\t"
      "v_mfma_f32_16x16x32_bf16 v[84:87], v[152:155], v[136:139], v[84:87]\n\t"
      "ds_read_b128 v[152:155], v230 offset:49152\n\t"
      "ds_read_b64_tr_b16 v[188:189], v237 offset:33280\n\t"
      "v_mfma_f32_16x16x32_bf16 v[88:91], v[168:171], v[120:123], v[88:91]\n\t"
      "v_exp_f32 v69, v69\n\t"
      "v_mfma_f32_16x16x32_bf16 v[92:95], v[168:171], v[136:139], v[92:95]\n\t"
      "ds_read_b128 v[168:171], v230 offset:53248\n\t"
      "v_cvt_pk_bf16_f32 v100, v68, v69\n\t"
      "v_mfma_f32_16x16x32_bf16 v[80:83], v[156:159], v[124:127], v[80:83]\n\t"
      "s_waitcnt lgkmcnt(15)\n\t"
      "v_exp_f32 v70, v70\n\t"
      "v_mfma_f32_16x16x32_bf16 v[84:87], v[156:159], v[140:143], v[84:87]\n\t"
      "ds_read_b128 v[156:159], v231 offset:49152\n\t"
      "ds_read_b64_tr_b16 v[190:191], v237 offset:37376\n\t"
      "v_mfma_f32_16x16x32_bf16 v[88:91], v[172:175], v[124:127], v[88:91]\n\t"
      "v_exp_f32 v71, v71\n\t"
      "v_mfma_f32_16x16x32_bf16 v[92:95], v[172:175], v[140:143], v[92:95]\n\t"
      "ds_read_b128 v[172:175], v231 offset:53248\n\t"
      "v_cvt_pk_bf16_f32 v101, v70, v71\n\t"
      "v_mfma_f32_16x16x32_bf16 v[32:35], v[192:195], v[104:107], v[32:35]\n\t"
      "v_exp_f32 v76, v76\n\t"
      "v_mfma_f32_16x16x32_bf16 v[36:39], v[192:195], v[108:111], v[36:39]\n\t"
      "ds_read_b64_tr_b16 v[192:193], v236 offset:33792\n\t"
      "ds_read_b64_tr_b16 v[194:195], v236 offset:37888\n\t"
      "v_mfma_f32_16x16x32_bf16 v[40:43], v[196:199], v[104:107], v[40:43]\n\t"
      "v_exp_f32 v77, v77\n\t"
      "v_mfma_f32_16x16x32_bf16 v[44:47], v[196:199], v[108:111], v[44:47]\n\t"
      "ds_read_b64_tr_b16 v[196:197], v237 offset:33792\n\t"
      "v_cvt_pk_bf16_f32 v102, v76, v77\n\t"
      "v_mfma_f32_16x16x32_bf16 v[48:51], v[200:203], v[104:107], v[48:51]\n\t"
      "s_waitcnt lgkmcnt(15)\n\t"
      "v_exp_f32 v78, v78\n\t"
      "v_mfma_f32_16x16x32_bf16 v[52:55], v[200:203], v[108:111], v[52:55]\n\t"
      "ds_read_b64_tr_b16 v[198:199], v237 offset:37888\n\t"
      "ds_read_b64_tr_b16 v[200:201], v236 offset:34304\n\t"
      "v_mfma_f32_16x16x32_bf16 v[56:59], v[204:207], v[104:107], v[56:59]\n\t"
      "v_exp_f32 v79, v79\n\t"
      "v_mfma_f32_16x16x32_bf16 v[60:63], v[204:207], v[108:111], v[60:63]\n\t"
      "ds_read_b64_tr_b16 v[202:203], v236 offset:38400\n\t"
      "v_cvt_pk_bf16_f32 v103, v78, v79\n\t"
      "v_mfma_f32_16x16x32_bf16 v[216:219], v[224:227], v[104:107], v[216:219]\n\t"
      "v_mfma_f32_16x16x32_bf16 v[220:223], v[224:227], v[108:111], v[220:223]\n\t"
      "v_mfma_f32_16x16x32_bf16 v[64:67], v[144:147], v[112:115], v[208:211]\n\t"
      "v_exp_f32 v80, v80\n\t"
      "v_mfma_f32_16x16x32_bf16 v[68:71], v[144:147], v[128:131], v[212:215]\n\t"
      "ds_read_b128 v[144:147], v228 offset:57344\n\t"
      "ds_read_b64_tr_b16 v[204:205], v237 offset:34304\n\t"
      "v_mfma_f32_16x16x32_bf16 v[72:75], v[160:163], v[112:115], v[208:211]\n\t"
      "v_exp_f32 v81, v81\n\t"
      "v_mfma_f32_16x16x32_bf16 v[76:79], v[160:163], v[128:131], v[212:215]\n\t"
      "ds_read_b128 v[160:163], v228 offset:61440\n\t"
      "v_cvt_pk_bf16_f32 v104, v80, v81\n\t"
      "v_mfma_f32_16x16x32_bf16 v[64:67], v[148:151], v[116:119], v[64:67]\n\t"
      "s_waitcnt lgkmcnt(15)\n\t"
      "v_exp_f32 v82, v82\n\t"
      "v_mfma_f32_16x16x32_bf16 v[68:71], v[148:151], v[132:135], v[68:71]\n\t"
      "ds_read_b128 v[148:151], v229 offset:57344\n\t"
      "ds_read_b64_tr_b16 v[206:207], v237 offset:38400\n\t"
      "v_mfma_f32_16x16x32_bf16 v[72:75], v[164:167], v[116:119], v[72:75]\n\t"
      "v_exp_f32 v83, v83\n\t"
      "v_mfma_f32_16x16x32_bf16 v[76:79], v[164:167], v[132:135], v[76:79]\n\t"
      "ds_read_b128 v[164:167], v229 offset:61440\n\t"
      "v_cvt_pk_bf16_f32 v105, v82, v83\n\t"
      "v_mfma_f32_16x16x32_bf16 v[0:3], v[176:179], v[96:99], v[0:3]\n\t"
      "v_exp_f32 v88, v88\n\t"
      "v_mfma_f32_16x16x32_bf16 v[4:7], v[176:179], v[100:103], v[4:7]\n\t"
      "ds_read_b64_tr_b16 v[176:177], v236 offset:40960\n\t"
      "ds_read_b64_tr_b16 v[178:179], v236 offset:45056\n\t"
      "v_mfma_f32_16x16x32_bf16 v[8:11], v[180:183], v[96:99], v[8:11]\n\t"
      "v_exp_f32 v89, v89\n\t"
      "v_mfma_f32_16x16x32_bf16 v[12:15], v[180:183], v[100:103], v[12:15]\n\t"
      "ds_read_b64_tr_b16 v[180:181], v237 offset:40960\n\t"
      "v_cvt_pk_bf16_f32 v106, v88, v89\n\t"
      "v_mfma_f32_16x16x32_bf16 v[16:19], v[184:187], v[96:99], v[16:19]\n\t"
      "s_waitcnt lgkmcnt(15)\n\t"
      "v_exp_f32 v90, v90\n\t"
      "v_mfma_f32_16x16x32_bf16 v[20:23], v[184:187], v[100:103], v[20:23]\n\t"
      "ds_read_b64_tr_b16 v[182:183], v237 offset:45056\n\t"
      "ds_read_b64_tr_b16 v[184:185], v236 offset:41472\n\t"
      "v_mfma_f32_16x16x32_bf16 v[24:27], v[188:191], v[96:99], v[24:27]\n\t"
      "v_exp_f32 v91, v91\n\t"
      "v_mfma_f32_16x16x32_bf16 v[28:31], v[188:191], v[100:103], v[28:31]\n\t"
      "ds_read_b64_tr_b16 v[186:187], v236 offset:45568\n\t"
      "v_cvt_pk_bf16_f32 v107, v90, v91\n\t"
      "v_mfma_f32_16x16x32_bf16 v[64:67], v[152:155], v[120:123], v[64:67]\n\t"
      "v_exp_f32 v84, v84\n\t"
      "v_mfma_f32_16x16x32_bf16 v[68:71], v[152:155], v[136:139], v[68:71]\n\t"
      "ds_read_b128 v[152:155], v230 offset:57344\n\t"
      "ds_read_b64_tr_b16 v[188:189], v237 offset:41472\n\t"
      "v_mfma_f32_16x16x32_bf16 v[72:75], v[168:171], v[120:123], v[72:75]\n\t"
      "v_exp_f32 v85, v85\n\t"
      "v_mfma_f32_16x16x32_bf16 v[76:79], v[168:171], v[136:139], v[76:79]\n\t"
      "ds_read_b128 v[168:171], v230 offset:61440\n\t"
      "v_cvt_pk_bf16_f32 v108, v84, v85\n\t"
      "v_mfma_f32_16x16x32_bf16 v[64:67], v[156:159], v[124:127], v[64:67]\n\t"
      "s_waitcnt lgkmcnt(15)\n\t"
      "v_exp_f32 v86, v86\n\t"
      "v_mfma_f32_16x16x32_bf16 v[68:71], v[156:159], v[140:143], v[68:71]\n\t"
      "ds_read_b128 v[156:159], v231 offset:57344\n\t"
      "ds_read_b64_tr_b16 v[190:191], v237 offset:45568\n\t"
      "v_mfma_f32_16x16x32_bf16 v[72:75], v[172:175], v[124:127], v[72:75]\n\t"
      "v_exp_f32 v87, v87\n\t"
      "v_mfma_f32_16x16x32_bf16 v[76:79], v[172:175], v[140:143], v[76:79]\n\t"
      "ds_read_b128 v[172:175], v231 offset:61440\n\t"
      "v_cvt_pk_bf16_f32 v109, v86, v87\n\t"
      "v_mfma_f32_16x16x32_bf16 v[32:35], v[192:195], v[96:99], v[32:35]\n\t"
      "v_exp_f32 v92, v92\n\t"
      "v_mfma_f32_16x16x32_bf16 v[36:39], v[192:195], v[100:103], v[36:39]\n\t"
      "ds_read_b64_tr_b16 v[192:193], v236 offset:41984\n\t"
      "ds_read_b64_tr_b16 v[194:195], v236 offset:46080\n\t"
      "v_mfma_f32_16x16x32_bf16 v[40:43], v[196:199], v[96:99], v[40:43]\n\t"
      "v_exp_f32 v93, v93\n\t"
      "v_mfma_f32_16x16x32_bf16 v[44:47], v[196:199], v[100:103], v[44:47]\n\t"
      "ds_read_b64_tr_b16 v[196:197], v237 offset:41984\n\t"
      "v_cvt_pk_bf16_f32 v110, v92, v93\n\t"
      "v_mfma_f32_16x16x32_bf16 v[48:51], v[200:203], v[96:99], v[48:51]\n\t"
      "s_waitcnt lgkmcnt(15)\n\t"
      "v_exp_f32 v94, v94\n\t"
      "v_mfma_f32_16x16x32_bf16 v[52:55], v[200:203], v[100:103], v[52:55]\n\t"
      "ds_read_b64_tr_b16 v[198:199], v237 offset:46080\n\t"
      "ds_read_b64_tr_b16 v[200:201], v236 offset:42496\n\t"
      "v_mfma_f32_16x16x32_bf16 v[56:59], v[204:207], v[96:99], v[56:59]\n\t"
      "v_exp_f32 v95, v95\n\t"
      "v_mfma_f32_16x16x32_bf16 v[60:63], v[204:207], v[100:103], v[60:63]\n\t"
      "ds_read_b64_tr_b16 v[202:203], v236 offset:46592\n\t"
      "v_cvt_pk_bf16_f32 v111, v94, v95\n\t"
      "v_mfma_f32_16x16x32_bf16 v[216:219], v[224:227], v[96:99], v[216:219]\n\t"
      "s_barrier\n\t"
      "v_mfma_f32_16x16x32_bf16 v[220:223], v[224:227], v[100:103], v[220:223]\n\t"
      "v_mfma_f32_16x16x32_bf16 v[80:83], v[144:147], v[112:115], v[208:211]\n\t"
      "v_exp_f32 v64, v64\n\t"
      "v_mfma_f32_16x16x32_bf16 v[84:87], v[144:147], v[128:131], v[212:215]\n\t"
      "ds_read_b128 v[144:147], v232 offset:0\n\t"
      "ds_read_b64_tr_b16 v[204:205], v237 offset:42496\n\t"
      "v_mfma_f32_16x16x32_bf16 v[88:91], v[160:163], v[112:115], v[208:211]\n\t"
      "v_exp_f32 v65, v65\n\t"
      "v_mfma_f32_16x16x32_bf16 v[92:95], v[160:163], v[128:131], v[212:215]\n\t"
      "ds_read_b128 v[160:163], v232 offset:4096\n\t"
      "v_cvt_pk_bf16_f32 v96, v64, v65\n\t"
      "v_mfma_f32_16x16x32_bf16 v[80:83], v[148:151], v[116:119], v[80:83]\n\t"
      "s_waitcnt lgkmcnt(15)\n\t"
      "v_exp_f32 v66, v66\n\t"
      "v_mfma_f32_16x16x32_bf16 v[84:87], v[148:151], v[132:135], v[84:87]\n\t"
      "ds_read_b128 v[148:151], v233 offset:0\n\t"
      "ds_read_b64_tr_b16 v[206:207], v237 offset:46592\n\t"
      "v_mfma_f32_16x16x32_bf16 v[88:91], v[164:167], v[116:119], v[88:91]\n\t"
      "v_exp_f32 v67, v67\n\t"
      "v_mfma_f32_16x16x32_bf16 v[92:95], v[164:167], v[132:135], v[92:95]\n\t"
      "ds_read_b128 v[164:167], v233 offset:4096\n\t"
      "v_cvt_pk_bf16_f32 v97, v66, v67\n\t"
      "v_mfma_f32_16x16x32_bf16 v[0:3], v[176:179], v[104:107], v[0:3]\n\t"
      "v_exp_f32 v72, v72\n\t"
      "v_mfma_f32_16x16x32_bf16 v[4:7], v[176:179], v[108:111], v[4:7]\n\t"
      "ds_read_b64_tr_b16 v[176:177], v236 offset:49152\n\t"
      "ds_read_b64_tr_b16 v[178:179], v236 offset:53248\n\t"
      "v_mfma_f32_16x16x32_bf16 v[8:11], v[180:183], v[104:107], v[8:11]\n\t"
      "v_exp_f32 v73, v73\n\t"
      "v_mfma_f32_16x16x32_bf16 v[12:15], v[180:183], v[108:111], v[12:15]\n\t"
      "ds_read_b64_tr_b16 v[180:181], v237 offset:49152\n\t"
      "v_cvt_pk_bf16_f32 v98, v72, v73\n\t"
      "v_mfma_f32_16x16x32_bf16 v[16:19], v[184:187], v[104:107], v[16:19]\n\t"
      "s_waitcnt lgkmcnt(15)\n\t"
      "v_exp_f32 v74, v74\n\t"
      "v_mfma_f32_16x16x32_bf16 v[20:23], v[184:187], v[108:111], v[20:23]\n\t"
      "ds_read_b64_tr_b16 v[182:183], v237 offset:53248\n\t"
      "ds_read_b64_tr_b16 v[184:185], v236 offset:49664\n\t"
      "v_mfma_f32_16x16x32_bf16 v[24:27], v[188:191], v[104:107], v[24:27]\n\t"
      "v_exp_f32 v75, v75\n\t"
      "v_mfma_f32_16x16x32_bf16 v[28:31], v[188:191], v[108:111], v[28:31]\n\t"
      "ds_read_b64_tr_b16 v[186:187], v236 offset:53760\n\t"
      "v_cvt_pk_bf16_f32 v99, v74, v75\n\t"
      "v_mfma_f32_16x16x32_bf16 v[80:83], v[152:155], v[120:123], v[80:83]\n\t"
      "v_exp_f32 v68, v68\n\t"
      "v_mfma_f32_16x16x32_bf16 v[84:87], v[152:155], v[136:139], v[84:87]\n\t"
      "ds_read_b128 v[152:155], v234 offset:0\n\t"
      "ds_read_b64_tr_b16 v[188:189], v237 offset:49664\n\t"
      "v_mfma_f32_16x16x32_bf16 v[88:91], v[168:171], v[120:123], v[88:91]\n\t"
      "v_exp_f32 v69, v69\n\t"
      "v_mfma_f32_16x16x32_bf16 v[92:95], v[168:171], v[136:139], v[92:95]\n\t"
      "ds_read_b128 v[168:171], v234 offset:4096\n\t"
      "v_cvt_pk_bf16_f32 v100, v68, v69\n\t"
      "v_mfma_f32_16x16x32_bf16 v[80:83], v[156:159], v[124:127], v[80:83]\n\t"
      "s_waitcnt lgkmcnt(15)\n\t"
      "v_exp_f32 v70, v70\n\t"
      "v_mfma_f32_16x16x32_bf16 v[84:87], v[156:159], v[140:143], v[84:87]\n\t"
      "ds_read_b128 v[156:159], v235 offset:0\n\t"
      "ds_read_b64_tr_b16 v[190:191], v237 offset:53760\n\t"
      "v_mfma_f32_16x16x32_bf16 v[88:91], v[172:175], v[124:127], v[88:91]\n\t"
      "v_exp_f32 v71, v71\n\t"
      "v_mfma_f32_16x16x32_bf16 v[92:95], v[172:175], v[140:143], v[92:95]\n\t"
      "ds_read_b128 v[172:175], v235 offset:4096\n\t"
      "v_cvt_pk_bf16_f32 v101, v70, v71\n\t"
      "v_mfma_f32_16x16x32_bf16 v[32:35], v[192:195], v[104:107], v[32:35]\n\t"
      "v_exp_f32 v76, v76\n\t"
      "v_mfma_f32_16x16x32_bf16 v[36:39], v[192:195], v[108:111], v[36:39]\n\t"
      "ds_read_b64_tr_b16 v[192:193], v236 offset:50176\n\t"
      "ds_read_b64_tr_b16 v[194:195], v236 offset:54272\n\t"
      "v_mfma_f32_16x16x32_bf16 v[40:43], v[196:199], v[104:107], v[40:43]\n\t"
      "v_exp_f32 v77, v77\n\t"
      "v_mfma_f32_16x16x32_bf16 v[44:47], v[196:199], v[108:111], v[44:47]\n\t"
      "ds_read_b64_tr_b16 v[196:197], v237 offset:50176\n\t"
      "v_cvt_pk_bf16_f32 v102, v76, v77\n\t"
      "v_mfma_f32_16x16x32_bf16 v[48:51], v[200:203], v[104:107], v[48:51]\n\t"
      "s_waitcnt lgkmcnt(15)\n\t"
      "v_exp_f32 v78, v78\n\t"
      "v_mfma_f32_16x16x32_bf16 v[52:55], v[200:203], v[108:111], v[52:55]\n\t"
      "ds_read_b64_tr_b16 v[198:199], v237 offset:54272\n\t"
      "ds_read_b64_tr_b16 v[200:201], v236 offset:50688\n\t"
      "v_mfma_f32_16x16x32_bf16 v[56:59], v[204:207], v[104:107], v[56:59]\n\t"
      "v_exp_f32 v79, v79\n\t"
      "v_mfma_f32_16x16x32_bf16 v[60:63], v[204:207], v[108:111], v[60:63]\n\t"
      "ds_read_b64_tr_b16 v[202:203], v236 offset:54784\n\t"
      "v_cvt_pk_bf16_f32 v103, v78, v79\n\t"
      "v_mfma_f32_16x16x32_bf16 v[216:219], v[224:227], v[104:107], v[216:219]\n\t"
      "v_mfma_f32_16x16x32_bf16 v[220:223], v[224:227], v[108:111], v[220:223]\n\t"
      "v_mfma_f32_16x16x32_bf16 v[64:67], v[144:147], v[112:115], v[208:211]\n\t"
      "v_exp_f32 v80, v80\n\t"
      "v_mfma_f32_16x16x32_bf16 v[68:71], v[144:147], v[128:131], v[212:215]\n\t"
      "ds_read_b128 v[144:147], v232 offset:8192\n\t"
      "ds_read_b64_tr_b16 v[204:205], v237 offset:50688\n\t"
      "v_mfma_f32_16x16x32_bf16 v[72:75], v[160:163], v[112:115], v[208:211]\n\t"
      "v_exp_f32 v81, v81\n\t"
      "v_mfma_f32_16x16x32_bf16 v[76:79], v[160:163], v[128:131], v[212:215]\n\t"
      "ds_read_b128 v[160:163], v232 offset:12288\n\t"
      "v_cvt_pk_bf16_f32 v104, v80, v81\n\t"
      "v_mfma_f32_16x16x32_bf16 v[64:67], v[148:151], v[116:119], v[64:67]\n\t"
      "s_waitcnt lgkmcnt(15)\n\t"
      "v_exp_f32 v82, v82\n\t"
      "v_mfma_f32_16x16x32_bf16 v[68:71], v[148:151], v[132:135], v[68:71]\n\t"
      "ds_read_b128 v[148:151], v233 offset:8192\n\t"
      "ds_read_b64_tr_b16 v[206:207], v237 offset:54784\n\t"
      "v_mfma_f32_16x16x32_bf16 v[72:75], v[164:167], v[116:119], v[72:75]\n\t"
      "v_exp_f32 v83, v83\n\t"
      "v_mfma_f32_16x16x32_bf16 v[76:79], v[164:167], v[132:135], v[76:79]\n\t"
      "ds_read_b128 v[164:167], v233 offset:12288\n\t"
      "v_cvt_pk_bf16_f32 v105, v82, v83\n\t"
      "v_mfma_f32_16x16x32_bf16 v[0:3], v[176:179], v[96:99], v[0:3]\n\t"
      "v_exp_f32 v88, v88\n\t"
      "v_mfma_f32_16x16x32_bf16 v[4:7], v[176:179], v[100:103], v[4:7]\n\t"
      "ds_read_b64_tr_b16 v[176:177], v236 offset:57344\n\t"
      "ds_read_b64_tr_b16 v[178:179], v236 offset:61440\n\t"
      "v_mfma_f32_16x16x32_bf16 v[8:11], v[180:183], v[96:99], v[8:11]\n\t"
      "v_exp_f32 v89, v89\n\t"
      "v_mfma_f32_16x16x32_bf16 v[12:15], v[180:183], v[100:103], v[12:15]\n\t"
      "ds_read_b64_tr_b16 v[180:181], v237 offset:57344\n\t"
      "v_cvt_pk_bf16_f32 v106, v88, v89\n\t"
      "v_mfma_f32_16x16x32_bf16 v[16:19], v[184:187], v[96:99], v[16:19]\n\t"
      "s_waitcnt lgkmcnt(15)\n\t"
      "v_exp_f32 v90, v90\n\t"
      "v_mfma_f32_16x16x32_bf16 v[20:23], v[184:187], v[100:103], v[20:23]\n\t"
      "ds_read_b64_tr_b16 v[182:183], v237 offset:61440\n\t"
      "ds_read_b64_tr_b16 v[184:185], v236 offset:57856\n\t"
      "v_mfma_f32_16x16x32_bf16 v[24:27], v[188:191], v[96:99], v[24:27]\n\t"
      "v_exp_f32 v91, v91\n\t"
      "v_mfma_f32_16x16x32_bf16 v[28:31], v[188:191], v[100:103], v[28:31]\n\t"
      "ds_read_b64_tr_b16 v[186:187], v236 offset:61952\n\t"
      "v_cvt_pk_bf16_f32 v107, v90, v91\n\t"
      "v_mfma_f32_16x16x32_bf16 v[64:67], v[152:155], v[120:123], v[64:67]\n\t"
      "v_exp_f32 v84, v84\n\t"
      "v_mfma_f32_16x16x32_bf16 v[68:71], v[152:155], v[136:139], v[68:71]\n\t"
      "ds_read_b128 v[152:155], v234 offset:8192\n\t"
      "ds_read_b64_tr_b16 v[188:189], v237 offset:57856\n\t"
      "v_mfma_f32_16x16x32_bf16 v[72:75], v[168:171], v[120:123], v[72:75]\n\t"
      "v_exp_f32 v85, v85\n\t"
      "v_mfma_f32_16x16x32_bf16 v[76:79], v[168:171], v[136:139], v[76:79]\n\t"
      "ds_read_b128 v[168:171], v234 offset:12288\n\t"
      "v_cvt_pk_bf16_f32 v108, v84, v85\n\t"
      "v_mfma_f32_16x16x32_bf16 v[64:67], v[156:159], v[124:127], v[64:67]\n\t"
      "s_waitcnt lgkmcnt(15)\n\t"
      "v_exp_f32 v86, v86\n\t"
      "v_mfma_f32_16x16x32_bf16 v[68:71], v[156:159], v[140:143], v[68:71]\n\t"
      "ds_read_b128 v[156:159], v235 offset:8192\n\t"
      "ds_read_b64_tr_b16 v[190:191], v237 offset:61952\n\t"
      "v_mfma_f32_16x16x32_bf16 v[72:75], v[172:175], v[124:127], v[72:75]\n\t"
      "v_exp_f32 v87, v87\n\t"
      "v_mfma_f32_16x16x32_bf16 v[76:79], v[172:175], v[140:143], v[76:79]\n\t"
      "ds_read_b128 v[172:175], v235 offset:12288\n\t"
      "v_cvt_pk_bf16_f32 v109, v86, v87\n\t"
      "v_mfma_f32_16x16x32_bf16 v[32:35], v[192:195], v[96:99], v[32:35]\n\t"
      "v_exp_f32 v92, v92\n\t"
      "v_mfma_f32_16x16x32_bf16 v[36:39], v[192:195], v[100:103], v[36:39]\n\t"
      "ds_read_b64_tr_b16 v[192:193], v236 offset:58368\n\t"
      "ds_read_b64_tr_b16 v[194:195], v236 offset:62464\n\t"
      "v_mfma_f32_16x16x32_bf16 v[40:43], v[196:199], v[96:99], v[40:43]\n\t"
      "v_exp_f32 v93, v93\n\t"
      "v_mfma_f32_16x16x32_bf16 v[44:47], v[196:199], v[100:103], v[44:47]\n\t"
      "ds_read_b64_tr_b16 v[196:197], v237 offset:58368\n\t"
      "v_cvt_pk_bf16_f32 v110, v92, v93\n\t"
      "v_mfma_f32_16x16x32_bf16 v[48:51], v[200:203], v[96:99], v[48:51]\n\t"
      "s_waitcnt lgkmcnt(15)\n\t"
      "v_exp_f32 v94, v94\n\t"
      "v_mfma_f32_16x16x32_bf16 v[52:55], v[200:203], v[100:103], v[52:55]\n\t"
      "ds_read_b64_tr_b16 v[198:199], v237 offset:62464\n\t"
      "ds_read_b64_tr_b16 v[200:201], v236 offset:58880\n\t"
      "v_mfma_f32_16x16x32_bf16 v[56:59], v[204:207], v[96:99], v[56:59]\n\t"
      "v_exp_f32 v95, v95\n\t"
      "v_mfma_f32_16x16x32_bf16 v[60:63], v[204:207], v[100:103], v[60:63]\n\t"
      "ds_read_b64_tr_b16 v[202:203], v236 offset:62976\n\t"
      "v_cvt_pk_bf16_f32 v111, v94, v95\n\t"
      "v_mfma_f32_16x16x32_bf16 v[216:219], v[224:227], v[96:99], v[216:219]\n\t"
      "s_barrier\n\t"
      "v_mfma_f32_16x16x32_bf16 v[220:223], v[224:227], v[100:103], v[220:223]\n\t"
      "v_mfma_f32_16x16x32_bf16 v[80:83], v[144:147], v[112:115], v[208:211]\n\t"
      "v_exp_f32 v64, v64\n\t"
      "v_mfma_f32_16x16x32_bf16 v[84:87], v[144:147], v[128:131], v[212:215]\n\t"
      "ds_read_b128 v[144:147], v228 offset:0\n\t"
      "ds_read_b64_tr_b16 v[204:205], v237 offset:58880\n\t"
      "v_mfma_f32_16x16x32_bf16 v[88:91], v[160:163], v[112:115], v[208:211]\n\t"
      "v_exp_f32 v65, v65\n\t"
      "v_mfma_f32_16x16x32_bf16 v[92:95], v[160:163], v[128:131], v[212:215]\n\t"
      "ds_read_b128 v[160:163], v228 offset:4096\n\t"
      "v_cvt_pk_bf16_f32 v96, v64, v65\n\t"
      "v_mfma_f32_16x16x32_bf16 v[80:83], v[148:151], v[116:119], v[80:83]\n\t"
      "s_waitcnt lgkmcnt(15)\n\t"
      "v_exp_f32 v66, v66\n\t"
      "v_mfma_f32_16x16x32_bf16 v[84:87], v[148:151], v[132:135], v[84:87]\n\t"
      "ds_read_b128 v[148:151], v229 offset:0\n\t"
      "ds_read_b64_tr_b16 v[206:207], v237 offset:62976\n\t"
      "v_mfma_f32_16x16x32_bf16 v[88:91], v[164:167], v[116:119], v[88:91]\n\t"
      "v_exp_f32 v67, v67\n\t"
      "v_mfma_f32_16x16x32_bf16 v[92:95], v[164:167], v[132:135], v[92:95]\n\t"
      "ds_read_b128 v[164:167], v229 offset:4096\n\t"
      "v_cvt_pk_bf16_f32 v97, v66, v67\n\t"
      "v_mfma_f32_16x16x32_bf16 v[0:3], v[176:179], v[104:107], v[0:3]\n\t"
      "v_exp_f32 v72, v72\n\t"
      "v_mfma_f32_16x16x32_bf16 v[4:7], v[176:179], v[108:111], v[4:7]\n\t"
      "ds_read_b64_tr_b16 v[176:177], v238 offset:0\n\t"
      "ds_read_b64_tr_b16 v[178:179], v238 offset:4096\n\t"
      "v_mfma_f32_16x16x32_bf16 v[8:11], v[180:183], v[104:107], v[8:11]\n\t"
      "v_exp_f32 v73, v73\n\t"
      "v_mfma_f32_16x16x32_bf16 v[12:15], v[180:183], v[108:111], v[12:15]\n\t"
      "ds_read_b64_tr_b16 v[180:181], v239 offset:0\n\t"
      "v_cvt_pk_bf16_f32 v98, v72, v73\n\t"
      "v_mfma_f32_16x16x32_bf16 v[16:19], v[184:187], v[104:107], v[16:19]\n\t"
      "s_waitcnt lgkmcnt(15)\n\t"
      "v_exp_f32 v74, v74\n\t"
      "v_mfma_f32_16x16x32_bf16 v[20:23], v[184:187], v[108:111], v[20:23]\n\t"
      "ds_read_b64_tr_b16 v[182:183], v239 offset:4096\n\t"
      "ds_read_b64_tr_b16 v[184:185], v238 offset:512\n\t"
      "v_mfma_f32_16x16x32_bf16 v[24:27], v[188:191], v[104:107], v[24:27]\n\t"
      "v_exp_f32 v75, v75\n\t"
      "v_mfma_f32_16x16x32_bf16 v[28:31], v[188:191], v[108:111], v[28:31]\n\t"
      "ds_read_b64_tr_b16 v[186:187], v238 offset:4608\n\t"
      "v_cvt_pk_bf16_f32 v99, v74, v75\n\t"
      "v_mfma_f32_16x16x32_bf16 v[80:83], v[152:155], v[120:123], v[80:83]\n\t"
      "v_exp_f32 v68, v68\n\t"
      "v_mfma_f32_16x16x32_bf16 v[84:87], v[152:155], v[136:139], v[84:87]\n\t"
      "ds_read_b128 v[152:155], v230 offset:0\n\t"
      "ds_read_b64_tr_b16 v[188:189], v239 offset:512\n\t"
      "v_mfma_f32_16x16x32_bf16 v[88:91], v[168:171], v[120:123], v[88:91]\n\t"
      "v_exp_f32 v69, v69\n\t"
      "v_mfma_f32_16x16x32_bf16 v[92:95], v[168:171], v[136:139], v[92:95]\n\t"
      "ds_read_b128 v[168:171], v230 offset:4096\n\t"
      "v_cvt_pk_bf16_f32 v100, v68, v69\n\t"
      "v_mfma_f32_16x16x32_bf16 v[80:83], v[156:159], v[124:127], v[80:83]\n\t"
      "s_waitcnt lgkmcnt(15)\n\t"
      "v_exp_f32 v70, v70\n\t"
      "v_mfma_f32_16x16x32_bf16 v[84:87], v[156:159], v[140:143], v[84:87]\n\t"
      "ds_read_b128 v[156:159], v231 offset:0\n\t"
      "ds_read_b64_tr_b16 v[190:191], v239 offset:4608\n\t"
      "v_mfma_f32_16x16x32_bf16 v[88:91], v[172:175], v[124:127], v[88:91]\n\t"
      "v_exp_f32 v71, v71\n\t"
      "v_mfma_f32_16x16x32_bf16 v[92:95], v[172:175], v[140:143], v[92:95]\n\t"
      "ds_read_b128 v[172:175], v231 offset:4096\n\t"
      "v_cvt_pk_bf16_f32 v101, v70, v71\n\t"
      "v_mfma_f32_16x16x32_bf16 v[32:35], v[192:195], v[104:107], v[32:35]\n\t"
      "v_exp_f32 v76, v76\n\t"
      "v_mfma_f32_16x16x32_bf16 v[36:39], v[192:195], v[108:111], v[36:39]\n\t"
      "ds_read_b64_tr_b16 v[192:193], v238 offset:1024\n\t"
      "ds_read_b64_tr_b16 v[194:195], v238 offset:5120\n\t"
      "v_mfma_f32_16x16x32_bf16 v[40:43], v[196:199], v[104:107], v[40:43]\n\t"
      "v_exp_f32 v77, v77\n\t"
      "v_mfma_f32_16x16x32_bf16 v[44:47], v[196:199], v[108:111], v[44:47]\n\t"
      "ds_read_b64_tr_b16 v[196:197], v239 offset:1024\n\t"
      "v_cvt_pk_bf16_f32 v102, v76, v77\n\t"
      "v_mfma_f32_16x16x32_bf16 v[48:51], v[200:203], v[104:107], v[48:51]\n\t"
      "s_waitcnt lgkmcnt(15)\n\t"
      "v_exp_f32 v78, v78\n\t"
      "v_mfma_f32_16x16x32_bf16 v[52:55], v[200:203], v[108:111], v[52:55]\n\t"
      "ds_read_b64_tr_b16 v[198:199], v239 offset:5120\n\t"
      "ds_read_b64_tr_b16 v[200:201], v238 offset:1536\n\t"
      "v_mfma_f32_16x16x32_bf16 v[56:59], v[204:207], v[104:107], v[56:59]\n\t"
      "v_exp_f32 v79, v79\n\t"
      "v_mfma_f32_16x16x32_bf16 v[60:63], v[204:207], v[108:111], v[60:63]\n\t"
      "ds_read_b64_tr_b16 v[202:203], v238 offset:5632\n\t"
      "v_cvt_pk_bf16_f32 v103, v78, v79\n\t"
      "v_mfma_f32_16x16x32_bf16 v[216:219], v[224:227], v[104:107], v[216:219]\n\t"
      "v_mfma_f32_16x16x32_bf16 v[220:223], v[224:227], v[108:111], v[220:223]\n\t"
      "v_mfma_f32_16x16x32_bf16 v[64:67], v[144:147], v[112:115], v[208:211]\n\t"
      "v_exp_f32 v80, v80\n\t"
      "v_mfma_f32_16x16x32_bf16 v[68:71], v[144:147], v[128:131], v[212:215]\n\t"
      "ds_read_b128 v[144:147], v228 offset:8192\n\t"
      "ds_read_b64_tr_b16 v[204:205], v239 offset:1536\n\t"
      "v_mfma_f32_16x16x32_bf16 v[72:75], v[160:163], v[112:115], v[208:211]\n\t"
      "v_exp_f32 v81, v81\n\t"
      "v_mfma_f32_16x16x32_bf16 v[76:79], v[160:163], v[128:131], v[212:215]\n\t"
      "ds_read_b128 v[160:163], v228 offset:12288\n\t"
      "v_cvt_pk_bf16_f32 v104, v80, v81\n\t"
      "v_mfma_f32_16x16x32_bf16 v[64:67], v[148:151], v[116:119], v[64:67]\n\t"
      "s_waitcnt lgkmcnt(15)\n\t"
      "v_exp_f32 v82, v82\n\t"
      "v_mfma_f32_16x16x32_bf16 v[68:71], v[148:151], v[132:135], v[68:71]\n\t"
      "ds_read_b128 v[148:151], v229 offset:8192\n\t"
      "ds_read_b64_tr_b16 v[206:207], v239 offset:5632\n\t"
      "v_mfma_f32_16x16x32_bf16 v[72:75], v[164:167], v[116:119], v[72:75]\n\t"
      "v_exp_f32 v83, v83\n\t"
      "v_mfma_f32_16x16x32_bf16 v[76:79], v[164:167], v[132:135], v[76:79]\n\t"
      "ds_read_b128 v[164:167], v229 offset:12288\n\t"
      "v_cvt_pk_bf16_f32 v105, v82, v83\n\t"
      "v_mfma_f32_16x16x32_bf16 v[0:3], v[176:179], v[96:99], v[0:3]\n\t"
      "v_exp_f32 v88, v88\n\t"
      "v_mfma_f32_16x16x32_bf16 v[4:7], v[176:179], v[100:103], v[4:7]\n\t"
      "ds_read_b64_tr_b16 v[176:177], v238 offset:8192\n\t"
      "ds_read_b64_tr_b16 v[178:179], v238 offset:12288\n\t"
      "v_mfma_f32_16x16x32_bf16 v[8:11], v[180:183], v[96:99], v[8:11]\n\t"
      "v_exp_f32 v89, v89\n\t"
      "v_mfma_f32_16x16x32_bf16 v[12:15], v[180:183], v[100:103], v[12:15]\n\t"
      "ds_read_b64_tr_b16 v[180:181], v239 offset:8192\n\t"
      "v_cvt_pk_bf16_f32 v106, v88, v89\n\t"
      "v_mfma_f32_16x16x32_bf16 v[16:19], v[184:187], v[96:99], v[16:19]\n\t"
      "s_waitcnt lgkmcnt(15)\n\t"
      "v_exp_f32 v90, v90\n\t"
      "v_mfma_f32_16x16x32_bf16 v[20:23], v[184:187], v[100:103], v[20:23]\n\t"
      "ds_read_b64_tr_b16 v[182:183], v239 offset:12288\n\t"
      "ds_read_b64_tr_b16 v[184:185], v238 offset:8704\n\t"
      "v_mfma_f32_16x16x32_bf16 v[24:27], v[188:191], v[96:99], v[24:27]\n\t"
      "v_exp_f32 v91, v91\n\t"
      "v_mfma_f32_16x16x32_bf16 v[28:31], v[188:191], v[100:103], v[28:31]\n\t"
      "ds_read_b64_tr_b16 v[186:187], v238 offset:12800\n\t"
      "v_cvt_pk_bf16_f32 v107, v90, v91\n\t"
      "v_mfma_f32_16x16x32_bf16 v[64:67], v[152:155], v[120:123], v[64:67]\n\t"
      "v_exp_f32 v84, v84\n\t"
      "v_mfma_f32_16x16x32_bf16 v[68:71], v[152:155], v[136:139], v[68:71]\n\t"
      "ds_read_b128 v[152:155], v230 offset:8192\n\t"
      "ds_read_b64_tr_b16 v[188:189], v239 offset:8704\n\t"
      "v_mfma_f32_16x16x32_bf16 v[72:75], v[168:171], v[120:123], v[72:75]\n\t"
      "v_exp_f32 v85, v85\n\t"
      "v_mfma_f32_16x16x32_bf16 v[76:79], v[168:171], v[136:139], v[76:79]\n\t"
      "ds_read_b128 v[168:171], v230 offset:12288\n\t"
      "v_cvt_pk_bf16_f32 v108, v84, v85\n\t"
      "v_mfma_f32_16x16x32_bf16 v[64:67], v[156:159], v[124:127], v[64:67]\n\t"
      "s_waitcnt lgkmcnt(15)\n\t"
      "v_exp_f32 v86, v86\n\t"
      "v_mfma_f32_16x16x32_bf16 v[68:71], v[156:159], v[140:143], v[68:71]\n\t"
      "ds_read_b128 v[156:159], v231 offset:8192\n\t"
      "ds_read_b64_tr_b16 v[190:191], v239 offset:12800\n\t"
      "v_mfma_f32_16x16x32_bf16 v[72:75], v[172:175], v[124:127], v[72:75]\n\t"
      "v_exp_f32 v87, v87\n\t"
      "v_mfma_f32_16x16x32_bf16 v[76:79], v[172:175], v[140:143], v[76:79]\n\t"
      "ds_read_b128 v[172:175], v231 offset:12288\n\t"
      "v_cvt_pk_bf16_f32 v109, v86, v87\n\t"
      "v_mfma_f32_16x16x32_bf16 v[32:35], v[192:195], v[96:99], v[32:35]\n\t"
      "v_exp_f32 v92, v92\n\t"
      "v_mfma_f32_16x16x32_bf16 v[36:39], v[192:195], v[100:103], v[36:39]\n\t"
      "ds_read_b64_tr_b16 v[192:193], v238 offset:9216\n\t"
      "ds_read_b64_tr_b16 v[194:195], v238 offset:13312\n\t"
      "v_mfma_f32_16x16x32_bf16 v[40:43], v[196:199], v[96:99], v[40:43]\n\t"
      "v_exp_f32 v93, v93\n\t"
      "v_mfma_f32_16x16x32_bf16 v[44:47], v[196:199], v[100:103], v[44:47]\n\t"
      "ds_read_b64_tr_b16 v[196:197], v239 offset:9216\n\t"
      "v_cvt_pk_bf16_f32 v110, v92, v93\n\t"
      "v_mfma_f32_16x16x32_bf16 v[48:51], v[200:203], v[96:99], v[48:51]\n\t"
      "s_waitcnt lgkmcnt(15)\n\t"
      "v_exp_f32 v94, v94\n\t"
      "v_mfma_f32_16x16x32_bf16 v[52:55], v[200:203], v[100:103], v[52:55]\n\t"
      "ds_read_b64_tr_b16 v[198:199], v239 offset:13312\n\t"
      "ds_read_b64_tr_b16 v[200:201], v238 offset:9728\n\t"
      "v_mfma_f32_16x16x32_bf16 v[56:59], v[204:207], v[96:99], v[56:59]\n\t"
      "v_exp_f32 v95, v95\n\t"
      "v_mfma_f32_16x16x32_bf16 v[60:63], v[204:207], v[100:103], v[60:63]\n\t"
      "ds_read_b64_tr_b16 v[202:203], v238 offset:13824\n\t"
      "v_cvt_pk_bf16_f32 v111, v94, v95\n\t"
      "v_mfma_f32_16x16x32_bf16 v[216:219], v[224:227], v[96:99], v[216:219]\n\t"
      "s_barrier\n\t"
      "v_mfma_f32_16x16x32_bf16 v[220:223], v[224:227], v[100:103], v[220:223]\n\t"
      "v_mfma_f32_16x16x32_bf16 v[80:83], v[144:147], v[112:115], v[208:211]\n\t"
      "v_exp_f32 v64, v64\n\t"
      "v_mfma_f32_16x16x32_bf16 v[84:87], v[144:147], v[128:131], v[212:215]\n\t"
      "ds_read_b64_tr_b16 v[204:205], v239 offset:9728\n\t"
      "v_mfma_f32_16x16x32_bf16 v[88:91], v[160:163], v[112:115], v[208:211]\n\t"
      "v_exp_f32 v65, v65\n\t"
      "v_mfma_f32_16x16x32_bf16 v[92:95], v[160:163], v[128:131], v[212:215]\n\t"
      "v_cvt_pk_bf16_f32 v96, v64, v65\n\t"
      "v_mfma_f32_16x16x32_bf16 v[80:83], v[148:151], v[116:119], v[80:83]\n\t"
      "s_waitcnt lgkmcnt(15)\n\t"
      "v_exp_f32 v66, v66\n\t"
      "v_mfma_f32_16x16x32_bf16 v[84:87], v[148:151], v[132:135], v[84:87]\n\t"
      "ds_read_b64_tr_b16 v[206:207], v239 offset:13824\n\t"
      "v_mfma_f32_16x16x32_bf16 v[88:91], v[164:167], v[116:119], v[88:91]\n\t"
      "v_exp_f32 v67, v67\n\t"
      "v_mfma_f32_16x16x32_bf16 v[92:95], v[164:167], v[132:135], v[92:95]\n\t"
      "v_cvt_pk_bf16_f32 v97, v66, v67\n\t"
      "v_mfma_f32_16x16x32_bf16 v[0:3], v[176:179], v[104:107], v[0:3]\n\t"
      "s_waitcnt lgkmcnt(14)\n\t"
      "v_exp_f32 v72, v72\n\t"
      "v_mfma_f32_16x16x32_bf16 v[4:7], v[176:179], v[108:111], v[4:7]\n\t"
      "ds_read_b64_tr_b16 v[176:177], v236 offset:0\n\t"
      "ds_read_b64_tr_b16 v[178:179], v236 offset:4096\n\t"
      "v_mfma_f32_16x16x32_bf16 v[8:11], v[180:183], v[104:107], v[8:11]\n\t"
      "v_exp_f32 v73, v73\n\t"
      "v_mfma_f32_16x16x32_bf16 v[12:15], v[180:183], v[108:111], v[12:15]\n\t"
      "ds_read_b64_tr_b16 v[180:181], v237 offset:0\n\t"
      "v_cvt_pk_bf16_f32 v98, v72, v73\n\t"
      "v_mfma_f32_16x16x32_bf16 v[16:19], v[184:187], v[104:107], v[16:19]\n\t"
      "s_waitcnt lgkmcnt(12)\n\t"
      "v_exp_f32 v74, v74\n\t"
      "v_mfma_f32_16x16x32_bf16 v[20:23], v[184:187], v[108:111], v[20:23]\n\t"
      "ds_read_b64_tr_b16 v[182:183], v237 offset:4096\n\t"
      "ds_read_b64_tr_b16 v[184:185], v236 offset:512\n\t"
      "v_mfma_f32_16x16x32_bf16 v[24:27], v[188:191], v[104:107], v[24:27]\n\t"
      "v_exp_f32 v75, v75\n\t"
      "v_mfma_f32_16x16x32_bf16 v[28:31], v[188:191], v[108:111], v[28:31]\n\t"
      "ds_read_b64_tr_b16 v[186:187], v236 offset:4608\n\t"
      "v_cvt_pk_bf16_f32 v99, v74, v75\n\t"
      "v_mfma_f32_16x16x32_bf16 v[80:83], v[152:155], v[120:123], v[80:83]\n\t"
      "v_exp_f32 v68, v68\n\t"
      "v_mfma_f32_16x16x32_bf16 v[84:87], v[152:155], v[136:139], v[84:87]\n\t"
      "ds_read_b64_tr_b16 v[188:189], v237 offset:512\n\t"
      "v_mfma_f32_16x16x32_bf16 v[88:91], v[168:171], v[120:123], v[88:91]\n\t"
      "v_exp_f32 v69, v69\n\t"
      "v_mfma_f32_16x16x32_bf16 v[92:95], v[168:171], v[136:139], v[92:95]\n\t"
      "v_cvt_pk_bf16_f32 v100, v68, v69\n\t"
      "v_mfma_f32_16x16x32_bf16 v[80:83], v[156:159], v[124:127], v[80:83]\n\t"
      "s_waitcnt lgkmcnt(13)\n\t"
      "v_exp_f32 v70, v70\n\t"
      "v_mfma_f32_16x16x32_bf16 v[84:87], v[156:159], v[140:143], v[84:87]\n\t"
      "ds_read_b64_tr_b16 v[190:191], v237 offset:4608\n\t"
      "v_mfma_f32_16x16x32_bf16 v[88:91], v[172:175], v[124:127], v[88:91]\n\t"
      "v_exp_f32 v71, v71\n\t"
      "v_mfma_f32_16x16x32_bf16 v[92:95], v[172:175], v[140:143], v[92:95]\n\t"
      "v_cvt_pk_bf16_f32 v101, v70, v71\n\t"
      "v_mfma_f32_16x16x32_bf16 v[32:35], v[192:195], v[104:107], v[32:35]\n\t"
      "s_waitcnt lgkmcnt(10)\n\t"
      "v_exp_f32 v76, v76\n\t"
      "v_mfma_f32_16x16x32_bf16 v[36:39], v[192:195], v[108:111], v[36:39]\n\t"
      "ds_read_b64_tr_b16 v[192:193], v236 offset:1024\n\t"
      "ds_read_b64_tr_b16 v[194:195], v236 offset:5120\n\t"
      "v_mfma_f32_16x16x32_bf16 v[40:43], v[196:199], v[104:107], v[40:43]\n\t"
      "v_exp_f32 v77, v77\n\t"
      "v_mfma_f32_16x16x32_bf16 v[44:47], v[196:199], v[108:111], v[44:47]\n\t"
      "ds_read_b64_tr_b16 v[196:197], v237 offset:1024\n\t"
      "v_cvt_pk_bf16_f32 v102, v76, v77\n\t"
      "v_mfma_f32_16x16x32_bf16 v[48:51], v[200:203], v[104:107], v[48:51]\n\t"
      "s_waitcnt lgkmcnt(11)\n\t"
      "v_exp_f32 v78, v78\n\t"
      "v_mfma_f32_16x16x32_bf16 v[52:55], v[200:203], v[108:111], v[52:55]\n\t"
      "ds_read_b64_tr_b16 v[198:199], v237 offset:5120\n\t"
      "ds_read_b64_tr_b16 v[200:201], v236 offset:1536\n\t"
      "v_mfma_f32_16x16x32_bf16 v[56:59], v[204:207], v[104:107], v[56:59]\n\t"
      "v_exp_f32 v79, v79\n\t"
      "v_mfma_f32_16x16x32_bf16 v[60:63], v[204:207], v[108:111], v[60:63]\n\t"
      "ds_read_b64_tr_b16 v[202:203], v236 offset:5632\n\t"
      "v_cvt_pk_bf16_f32 v103, v78, v79\n\t"
      "v_mfma_f32_16x16x32_bf16 v[216:219], v[224:227], v[104:107], v[216:219]\n\t"
      "v_mfma_f32_16x16x32_bf16 v[220:223], v[224:227], v[108:111], v[220:223]\n\t"
      "v_exp_f32 v80, v80\n\t"
      "ds_read_b64_tr_b16 v[204:205], v237 offset:1536\n\t"
      "v_exp_f32 v81, v81\n\t"
      "s_nop 0\n\t"
      "v_cvt_pk_bf16_f32 v104, v80, v81\n\t"
      "s_waitcnt lgkmcnt(13)\n\t"
      "v_exp_f32 v82, v82\n\t"
      "ds_read_b64_tr_b16 v[206:207], v237 offset:5632\n\t"
      "v_exp_f32 v83, v83\n\t"
      "s_nop 0\n\t"
      "v_cvt_pk_bf16_f32 v105, v82, v83\n\t"
      "v_mfma_f32_16x16x32_bf16 v[0:3], v[176:179], v[96:99], v[0:3]\n\t"
      "s_waitcnt lgkmcnt(10)\n\t"
      "v_exp_f32 v88, v88\n\t"
      "v_mfma_f32_16x16x32_bf16 v[4:7], v[176:179], v[100:103], v[4:7]\n\t"
      "ds_read_b64_tr_b16 v[176:177], v236 offset:8192\n\t"
      "ds_read_b64_tr_b16 v[178:179], v236 offset:12288\n\t"
      "v_mfma_f32_16x16x32_bf16 v[8:11], v[180:183], v[96:99], v[8:11]\n\t"
      "v_exp_f32 v89, v89\n\t"
      "v_mfma_f32_16x16x32_bf16 v[12:15], v[180:183], v[100:103], v[12:15]\n\t"
      "ds_read_b64_tr_b16 v[180:181], v237 offset:8192\n\t"
      "v_cvt_pk_bf16_f32 v106, v88, v89\n\t"
      "v_mfma_f32_16x16x32_bf16 v[16:19], v[184:187], v[96:99], v[16:19]\n\t"
      "s_waitcnt lgkmcnt(11)\n\t"
      "v_exp_f32 v90, v90\n\t"
      "v_mfma_f32_16x16x32_bf16 v[20:23], v[184:187], v[100:103], v[20:23]\n\t"
      "ds_read_b64_tr_b16 v[182:183], v237 offset:12288\n\t"
      "ds_read_b64_tr_b16 v[184:185], v236 offset:8704\n\t"
      "v_mfma_f32_16x16x32_bf16 v[24:27], v[188:191], v[96:99], v[24:27]\n\t"
      "v_exp_f32 v91, v91\n\t"
      "v_mfma_f32_16x16x32_bf16 v[28:31], v[188:191], v[100:103], v[28:31]\n\t"
      "ds_read_b64_tr_b16 v[186:187], v236 offset:12800\n\t"
      "v_cvt_pk_bf16_f32 v107, v90, v91\n\t"
      "v_exp_f32 v84, v84\n\t"
      "ds_read_b64_tr_b16 v[188:189], v237 offset:8704\n\t"
      "v_exp_f32 v85, v85\n\t"
      "s_nop 0\n\t"
      "v_cvt_pk_bf16_f32 v108, v84, v85\n\t"
      "s_waitcnt lgkmcnt(13)\n\t"
      "v_exp_f32 v86, v86\n\t"
      "ds_read_b64_tr_b16 v[190:191], v237 offset:12800\n\t"
      "v_exp_f32 v87, v87\n\t"
      "s_nop 0\n\t"
      "v_cvt_pk_bf16_f32 v109, v86, v87\n\t"
      "v_mfma_f32_16x16x32_bf16 v[32:35], v[192:195], v[96:99], v[32:35]\n\t"
      "s_waitcnt lgkmcnt(10)\n\t"
      "v_exp_f32 v92, v92\n\t"
      "v_mfma_f32_16x16x32_bf16 v[36:39], v[192:195], v[100:103], v[36:39]\n\t"
      "ds_read_b64_tr_b16 v[192:193], v236 offset:9216\n\t"
      "ds_read_b64_tr_b16 v[194:195], v236 offset:13312\n\t"
      "v_mfma_f32_16x16x32_bf16 v[40:43], v[196:199], v[96:99], v[40:43]\n\t"
      "v_exp_f32 v93, v93\n\t"
      "v_mfma_f32_16x16x32_bf16 v[44:47], v[196:199], v[100:103], v[44:47]\n\t"
      "ds_read_b64_tr_b16 v[196:197], v237 offset:9216\n\t"
      "v_cvt_pk_bf16_f32 v110, v92, v93\n\t"
      "v_mfma_f32_16x16x32_bf16 v[48:51], v[200:203], v[96:99], v[48:51]\n\t"
      "s_waitcnt lgkmcnt(11)\n\t"
      "v_exp_f32 v94, v94\n\t"
      "v_mfma_f32_16x16x32_bf16 v[52:55], v[200:203], v[100:103], v[52:55]\n\t"
      "ds_read_b64_tr_b16 v[198:199], v237 offset:13312\n\t"
      "ds_read_b64_tr_b16 v[200:201], v236 offset:9728\n\t"
      "v_mfma_f32_16x16x32_bf16 v[56:59], v[204:207], v[96:99], v[56:59]\n\t"
      "v_exp_f32 v95, v95\n\t"
      "v_mfma_f32_16x16x32_bf16 v[60:63], v[204:207], v[100:103], v[60:63]\n\t"
      "ds_read_b64_tr_b16 v[202:203], v236 offset:13824\n\t"
      "v_cvt_pk_bf16_f32 v111, v94, v95\n\t"
      "v_mfma_f32_16x16x32_bf16 v[216:219], v[224:227], v[96:99], v[216:219]\n\t"
      "s_barrier\n\t"
      "v_mfma_f32_16x16x32_bf16 v[220:223], v[224:227], v[100:103], v[220:223]\n\t"
      "ds_read_b64_tr_b16 v[204:205], v237 offset:9728\n\t"
      "ds_read_b64_tr_b16 v[206:207], v237 offset:13824\n\t"
      "s_mov_b32 s43, 0x5d800000\n\t"
      "v_mfma_f32_16x16x32_bf16 v[216:219], v[224:227], v[104:107], v[216:219]\n\t"
      "v_mfma_f32_16x16x32_bf16 v[220:223], v[224:227], v[108:111], v[220:223]\n\t"
      "s_waitcnt lgkmcnt(10)\n\t"
      "v_mfma_f32_16x16x32_bf16 v[0:3], v[176:179], v[104:107], v[0:3]\n\t"
      "v_mfma_f32_16x16x32_bf16 v[8:11], v[180:183], v[104:107], v[8:11]\n\t"
      "v_mfma_f32_16x16x32_bf16 v[16:19], v[184:187], v[104:107], v[16:19]\n\t"
      "s_waitcnt lgkmcnt(4)\n\t"
      "v_mfma_f32_16x16x32_bf16 v[24:27], v[188:191], v[104:107], v[24:27]\n\t"
      "v_mfma_f32_16x16x32_bf16 v[32:35], v[192:195], v[104:107], v[32:35]\n\t"
      "v_mfma_f32_16x16x32_bf16 v[40:43], v[196:199], v[104:107], v[40:43]\n\t"
      "s_waitcnt lgkmcnt(0)\n\t"
      "v_mfma_f32_16x16x32_bf16 v[48:51], v[200:203], v[104:107], v[48:51]\n\t"
      "v_mov_b32 v240, v216\n\t"
      "v_mov_b32 v241, v220\n\t"
      "v_mfma_f32_16x16x32_bf16 v[56:59], v[204:207], v[104:107], v[56:59]\n\t"
      "s_nop 0\n\t"
      "v_permlane32_swap_b32 v216, v240\n\t"
      "v_permlane32_swap_b32 v220, v241\n\t"
      "s_nop 0\n\t"
      "v_mov_b32 v240, v216\n\t"
      "v_mov_b32 v241, v220\n\t"
      "s_nop 0\n\t"
      "v_permlane16_swap_b32 v216, v240\n\t"
      "v_permlane16_swap_b32 v220, v241\n\t"
      "s_nop 0\n\t"
      "v_cmp_nge_f32 vcc, s43, v216\n\t"
      "v_cmp_nge_f32 s[46:47], s43, v220\n\t"
      "v_rcp_f32 v216, v216\n\t"
      "v_rcp_f32 v220, v220\n\t"
      "s_nop 0\n\t"
      "v_mfma_f32_16x16x32_bf16 v[4:7], v[176:179], v[108:111], v[4:7]\n\t"
      "v_mul_f32 v0, v0, v216\n\t"
      "v_mul_f32 v1, v1, v216\n\t"
      "v_mul_f32 v2, v2, v216\n\t"
      "v_mul_f32 v3, v3, v216\n\t"
      "v_mfma_f32_16x16x32_bf16 v[12:15], v[180:183], v[108:111], v[12:15]\n\t"
      "v_mul_f32 v8, v8, v216\n\t"
      "v_mul_f32 v9, v9, v216\n\t"
      "v_mul_f32 v10, v10, v216\n\t"
      "v_mul_f32 v11, v11, v216\n\t"
      "v_cvt_pk_bf16_f32 v0, v0, v1\n\t"
      "v_cvt_pk_bf16_f32 v1, v2, v3\n\t"
      "v_mfma_f32_16x16x32_bf16 v[20:23], v[184:187], v[108:111], v[20:23]\n\t"
      "v_mul_f32 v16, v16, v216\n\t"
      "v_mul_f32 v17, v17, v216\n\t"
      "v_mul_f32 v18, v18, v216\n\t"
      "v_mul_f32 v19, v19, v216\n\t"
      "v_cvt_pk_bf16_f32 v8, v8, v9\n\t"
      "v_cvt_pk_bf16_f32 v9, v10, v11\n\t"
      "ds_write_b64 v248, v[0:1] offset:0\n\t"
      "v_mfma_f32_16x16x32_bf16 v[28:31], v[188:191], v[108:111], v[28:31]\n\t"
      "v_mul_f32 v24, v24, v216\n\t"
      "v_mul_f32 v25, v25, v216\n\t"
      "v_mul_f32 v26, v26, v216\n\t"
      "v_mul_f32 v27, v27, v216\n\t"
      "v_cvt_pk_bf16_f32 v16, v16, v17\n\t"
      "v_cvt_pk_bf16_f32 v17, v18, v19\n\t"
      "ds_write_b64 v248, v[8:9] offset:544\n\t"
      "v_mfma_f32_16x16x32_bf16 v[36:39], v[192:195], v[108:111], v[36:39]\n\t"
      "v_mul_f32 v32, v32, v216\n\t"
      "v_mul_f32 v33, v33, v216\n\t"
      "v_mul_f32 v34, v34, v216\n\t"
      "v_mul_f32 v35, v35, v216\n\t"
      "v_cvt_pk_bf16_f32 v24, v24, v25\n\t"
      "v_cvt_pk_bf16_f32 v25, v26, v27\n\t"
      "ds_write_b64 v248, v[16:17] offset:1088\n\t"
      "v_mfma_f32_16x16x32_bf16 v[44:47], v[196:199], v[108:111], v[44:47]\n\t"
      "v_mul_f32 v40, v40, v216\n\t"
      "v_mul_f32 v41, v41, v216\n\t"
      "v_mul_f32 v42, v42, v216\n\t"
      "v_mul_f32 v43, v43, v216\n\t"
      "v_cvt_pk_bf16_f32 v32, v32, v33\n\t"
      "v_cvt_pk_bf16_f32 v33, v34, v35\n\t"
      "ds_write_b64 v248, v[24:25] offset:1632\n\t"
      "v_mfma_f32_16x16x32_bf16 v[52:55], v[200:203], v[108:111], v[52:55]\n\t"
      "v_mul_f32 v48, v48, v216\n\t"
      "v_mul_f32 v49, v49, v216\n\t"
      "v_mul_f32 v50, v50, v216\n\t"
      "v_mul_f32 v51, v51, v216\n\t"
      "v_cvt_pk_bf16_f32 v40, v40, v41\n\t"
      "v_cvt_pk_bf16_f32 v41, v42, v43\n\t"
      "ds_write_b64 v248, v[32:33] offset:2176\n\t"
      "v_mfma_f32_16x16x32_bf16 v[60:63], v[204:207], v[108:111], v[60:63]\n\t"
      "v_mul_f32 v56, v56, v216\n\t"
      "v_mul_f32 v57, v57, v216\n\t"
      "v_mul_f32 v58, v58, v216\n\t"
      "v_mul_f32 v59, v59, v216\n\t"
      "v_cvt_pk_bf16_f32 v48, v48, v49\n\t"
      "v_cvt_pk_bf16_f32 v49, v50, v51\n\t"
      "ds_write_b64 v248, v[40:41] offset:2720\n\t"
      "v_cvt_pk_bf16_f32 v56, v56, v57\n\t"
      "v_cvt_pk_bf16_f32 v57, v58, v59\n\t"
      "ds_write_b64 v248, v[48:49] offset:3264\n\t"
      "ds_write_b64 v248, v[56:57] offset:3808\n\t"
      "v_mul_f32 v4, v4, v220\n\t"
      "v_mul_f32 v5, v5, v220\n\t"
      "v_mul_f32 v6, v6, v220\n\t"
      "v_mul_f32 v7, v7, v220\n\t"
      "v_mul_f32 v12, v12, v220\n\t"
      "v_mul_f32 v13, v13, v220\n\t"
      "v_mul_f32 v14, v14, v220\n\t"
      "v_mul_f32 v15, v15, v220\n\t"
      "v_cvt_pk_bf16_f32 v4, v4, v5\n\t"
      "v_cvt_pk_bf16_f32 v5, v6, v7\n\t"
      "s_waitcnt lgkmcnt(0)\n\t"
      "ds_read_b128 v[176:179], v249 offset:0\n\t"
      "ds_read_b128 v[180:183], v250 offset:128\n\t"
      "ds_read_b128 v[184:187], v249 offset:256\n\t"
      "ds_read_b128 v[188:191], v250 offset:384\n\t"
      "v_mul_f32 v20, v20, v220\n\t"
      "v_mul_f32 v21, v21, v220\n\t"
      "v_mul_f32 v22, v22, v220\n\t"
      "v_mul_f32 v23, v23, v220\n\t"
      "v_cvt_pk_bf16_f32 v12, v12, v13\n\t"
      "v_cvt_pk_bf16_f32 v13, v14, v15\n\t"
      "ds_write_b64 v248, v[4:5] offset:4352\n\t"
      "v_mul_f32 v28, v28, v220\n\t"
      "v_mul_f32 v29, v29, v220\n\t"
      "v_mul_f32 v30, v30, v220\n\t"
      "v_mul_f32 v31, v31, v220\n\t"
      "v_cvt_pk_bf16_f32 v20, v20, v21\n\t"
      "v_cvt_pk_bf16_f32 v21, v22, v23\n\t"
      "ds_write_b64 v248, v[12:13] offset:4896\n\t"
      "v_mul_f32 v36, v36, v220\n\t"
      "v_mul_f32 v37, v37, v220\n\t"
      "v_mul_f32 v38, v38, v220\n\t"
      "v_mul_f32 v39, v39, v220\n\t"
      "v_cvt_pk_bf16_f32 v28, v28, v29\n\t"
      "v_cvt_pk_bf16_f32 v29, v30, v31\n\t"
      "ds_write_b64 v248, v[20:21] offset:5440\n\t"
      "v_mul_f32 v44, v44, v220\n\t"
      "v_mul_f32 v45, v45, v220\n\t"
      "v_mul_f32 v46, v46, v220\n\t"
      "v_mul_f32 v47, v47, v220\n\t"
      "v_cvt_pk_bf16_f32 v36, v36, v37\n\t"
      "v_cvt_pk_bf16_f32 v37, v38, v39\n\t"
      "ds_write_b64 v248, v[28:29] offset:5984\n\t"
      "s_waitcnt lgkmcnt(4)\n\t"
      "s_add_u32 s42, s40, 0\n\t"
      "buffer_store_dwordx4 v[176:179], v251, %[so], s42 offen sc1 nt\n\t"
      "v_mul_f32 v52, v52, v220\n\t"
      "v_mul_f32 v53, v53, v220\n\t"
      "v_mul_f32 v54, v54, v220\n\t"
      "v_mul_f32 v55, v55, v220\n\t"
      "v_cvt_pk_bf16_f32 v44, v44, v45\n\t"
      "v_cvt_pk_bf16_f32 v45, v46, v47\n\t"
      "ds_write_b64 v248, v[36:37] offset:6528\n\t"
      "s_add_u32 s42, s40, 1024\n\t"
      "buffer_store_dwordx4 v[180:183], v251, %[so], s42 offen sc1 nt\n\t"
      "v_mul_f32 v60, v60, v220\n\t"
      "v_mul_f32 v61, v61, v220\n\t"
      "v_mul_f32 v62, v62, v220\n\t"
      "v_mul_f32 v63, v63, v220\n\t"
      "v_cvt_pk_bf16_f32 v52, v52, v53\n\t"
      "v_cvt_pk_bf16_f32 v53, v54, v55\n\t"
      "ds_write_b64 v248, v[44:45] offset:7072\n\t"
      "s_add_u32 s42, s40, 2048\n\t"
      "buffer_store_dwordx4 v[184:187], v251, %[so], s42 offen sc1 nt\n\t"
      "v_cvt_pk_bf16_f32 v60, v60, v61\n\t"
      "v_cvt_pk_bf16_f32 v61, v62, v63\n\t"
      "ds_write_b64 v248, v[52:53] offset:7616\n\t"
      "s_add_u32 s42, s40, 3072\n\t"
      "buffer_store_dwordx4 v[188:191], v251, %[so], s42 offen sc1 nt\n\t"
      "ds_write_b64 v248, v[60:61] offset:8160\n\t"
      "s_waitcnt lgkmcnt(0)\n\t"
      "ds_read_b128 v[192:195], v249 offset:4352\n\t"
      "ds_read_b128 v[196:199], v250 offset:4480\n\t"
      "ds_read_b128 v[200:203], v249 offset:4608\n\t"
      "ds_read_b128 v[204:207], v250 offset:4736\n\t"
      "s_waitcnt lgkmcnt(3)\n\t"
      "s_add_u32 s42, s40, 4096\n\t"
      "buffer_store_dwordx4 v[192:195], v251, %[so], s42 offen sc1 nt\n\t"
      "s_waitcnt lgkmcnt(2)\n\t"
      "s_add_u32 s42, s40, 5120\n\t"
      "buffer_store_dwordx4 v[196:199], v251, %[so], s42 offen sc1 nt\n\t"
      "s_waitcnt lgkmcnt(1)\n\t"
      "s_add_u32 s42, s40, 6144\n\t"
      "buffer_store_dwordx4 v[200:203], v251, %[so], s42 offen sc1 nt\n\t"
      "s_waitcnt lgkmcnt(0)\n\t"
      "s_add_u32 s42, s40, 7168\n\t"
      "buffer_store_dwordx4 v[204:207], v251, %[so], s42 offen sc1 nt\n\t"
      "s_or_b64 vcc, vcc, s[46:47]\n\t"
      "s_or_b32 %[flag], vcc_lo, vcc_hi\n\t"
      "s_branch Lend%=\n\t"
      "Lc1%=:\n\t"
      "v_lshl_add_u32 v243, v240, 8, v242\n\t"
      "v_add_u32 v228, %[lds], v243\n\t"
      "v_xor_b32 v229, 64, v228\n\t"
      "v_xor_b32 v230, 128, v228\n\t"
      "v_xor_b32 v231, 192, v228\n\t"
      "v_add_u32 v232, 0x10000, v228\n\t"
      "v_add_u32 v233, 0x10000, v229\n\t"
      "v_add_u32 v234, 0x10000, v230\n\t"
      "v_add_u32 v235, 0x10000, v231\n\t"
      "v_lshrrev_b32 v243, 1, v241\n\t"
      "v_lshlrev_b32 v243, 11, v243\n\t"
      "v_and_b32 v244, 1, v241\n\t"
      "v_lshrrev_b32 v245, 2, v240\n\t"
      "v_lshl_add_u32 v245, v244, 2, v245\n\t"
      "v_lshl_add_u32 v243, v245, 6, v243\n\t"
      "v_and_b32 v245, 3, v247\n\t"
      "v_lshl_add_u32 v243, v245, 3, v243\n\t"
      "v_lshl_add_u32 v243, v244, 5, v243\n\t"
      "s_add_u32 s42, %[lds], 81920\n\t"
      "v_add_u32 v236, s42, v243\n\t"
      "v_xor_b32 v237, 32, v236\n\t"
      "v_add_u32 v238, 0x10000, v236\n\t"
      "v_add_u32 v239, 0x10000, v237\n\t"
      "s_add_u32 s42, s40, 131072\n\t"
      "v_cmp_eq_u32 vcc, 0, v240\n\t"
      "v_add_u32 v242, s42, v228\n\t"
      "v_add_u32 v243, s42, v229\n\t"
      "v_add_u32 v244, s42, v230\n\t"
      "v_add_u32 v245, s42, v231\n\t"
      "v_mov_b32 v224, 0x3f803f80\n\t"
      "v_cndmask_b32 v224, 0, v224, vcc\n\t"
      "v_mov_b32 v225, v224\n\t"
      "v_mov_b32 v226, v224\n\t"
      "v_mov_b32 v227, v224\n\t"
      "s_mul_i32 s42, %[w], 8704\n\t"
      "s_add_u32 s42, s42, %[lds]\n\t"
      "s_add_u32 s42, s42, 16384\n\t"
      "v_lshrrev_b32 v248, 1, v240\n\t"
      "v_and_b32 v248, 2, v248\n\t"
      "v_xor_b32 v248, v248, v241\n\t"
      "v_lshlrev_b32 v248, 3, v248\n\t"
      "v_lshl_add_u32 v248, v240, 5, v248\n\t"
      "v_add_u32 v248, s42, v248\n\t"
      "v_lshrrev_b32 v249, 1, v240\n\t"
      "v_mul_u32_u24 v249, 544, v249\n\t"
      "v_lshl_add_u32 v249, v241, 5, v249\n\t"
      "v_and_b32 v250, 1, v240\n\t"
      "v_lshl_add_u32 v249, v250, 4, v249\n\t"
      "v_add_u32 v249, s42, v249\n\t"
      "v_xor_b32 v250, 16, v249\n\t"
      "v_lshlrev_b32 v251, 4, v247\n\t"
      "s_mov_b32 s44, 0x3e0293ee\n\t"
      "s_mov_b32 s45, 0x3e0293ee\n\t"
      "s_mov_b32 s46, 0xffff0000\n\t"
      "s_waitcnt vmcnt(0)\n\t"
      "ds_read_b128 v[112:115], v242 offset:0\n\t"
      "ds_read_b128 v[116:119], v243 offset:0\n\t"
      "ds_read_b128 v[120:123], v244 offset:0\n\t"
      "ds_read_b128 v[124:127], v245 offset:0\n\t"
      "s_waitcnt lgkmcnt(3)\n\t"
      "v_lshlrev_b32 v80, 16, v112\n\t"
      "v_and_b32 v81, s46, v112\n\t"
      "v_lshlrev_b32 v82, 16, v113\n\t"
      "v_and_b32 v83, s46, v113\n\t"
      "v_lshlrev_b32 v84, 16, v114\n\t"
      "v_and_b32 v85, s46, v114\n\t"
      "v_lshlrev_b32 v86, 16, v115\n\t"
      "v_and_b32 v87, s46, v115\n\t"
      "v_pk_mul_f32 v[80:81], v[80:81], s[44:45]\n\t"
      "v_pk_mul_f32 v[82:83], v[82:83], s[44:45]\n\t"
      "v_pk_mul_f32 v[84:85], v[84:85], s[44:45]\n\t"
      "v_pk_mul_f32 v[86:87], v[86:87], s[44:45]\n\t"
      "v_cvt_pk_bf16_f32 v112, v80, v81\n\t"
      "v_cvt_pk_bf16_f32 v113, v82, v83\n\t"
      "v_cvt_pk_bf16_f32 v114, v84, v85\n\t"
      "v_cvt_pk_bf16_f32 v115, v86, v87\n\t"
      "s_waitcnt lgkmcnt(2)\n\t"
      "v_lshlrev_b32 v80, 16, v116\n\t"
      "v_and_b32 v81, s46, v116\n\t"
      "v_lshlrev_b32 v82, 16, v117\n\t"
      "v_and_b32 v83, s46, v117\n\t"
      "v_lshlrev_b32 v84, 16, v118\n\t"
      "v_and_b32 v85, s46, v118\n\t"
      "v_lshlrev_b32 v86, 16, v119\n\t"
      "v_and_b32 v87, s46, v119\n\t"
      "v_pk_mul_f32 v[80:81], v[80:81], s[44:45]\n\t"
      "v_pk_mul_f32 v[82:83], v[82:83], s[44:45]\n\t"
      "v_pk_mul_f32 v[84:85], v[84:85], s[44:45]\n\t"
      "v_pk_mul_f32 v[86:87], v[86:87], s[44:45]\n\t"
      "v_cvt_pk_bf16_f32 v116, v80, v81\n\t"
      "v_cvt_pk_bf16_f32 v117, v82, v83\n\t"
      "v_cvt_pk_bf16_f32 v118, v84, v85\n\t"
      "v_cvt_pk_bf16_f32 v119, v86, v87\n\t"
      "s_waitcnt lgkmcnt(1)\n\t"
      "v_lshlrev_b32 v80, 16, v120\n\t"
      "v_and_b32 v81, s46, v120\n\t"
      "v_lshlrev_b32 v82, 16, v121\n\t"
      "v_and_b32 v83, s46, v121\n\t"
      "v_lshlrev_b32 v84, 16, v122\n\t"
      "v_and_b32 v85, s46, v122\n\t"
      "v_lshlrev_b32 v86, 16, v123\n\t"
      "v_and_b32 v87, s46, v123\n\t"
      "v_pk_mul_f32 v[80:81], v[80:81], s[44:45]\n\t"
      "v_pk_mul_f32 v[82:83], v[82:83], s[44:45]\n\t"
      "v_pk_mul_f32 v[84:85], v[84:85], s[44:45]\n\t"
      "v_pk_mul_f32 v[86:87], v[86:87], s[44:45]\n\t"
      "v_cvt_pk_bf16_f32 v120, v80, v81\n\t"
      "v_cvt_pk_bf16_f32 v121, v82, v83\n\t"
      "v_cvt_pk_bf16_f32 v122, v84, v85\n\t"
      "v_cvt_pk_bf16_f32 v123, v86, v87\n\t"
      "s_waitcnt lgkmcnt(0)\n\t"
      "v_lshlrev_b32 v80, 16, v124\n\t"
      "v_and_b32 v81, s46, v124\n\t"
      "v_lshlrev_b32 v82, 16, v125\n\t"
      "v_and_b32 v83, s46, v125\n\t"
      "v_lshlrev_b32 v84, 16, v126\n\t"
      "v_and_b32 v85, s46, v126\n\t"
      "v_lshlrev_b32 v86, 16, v127\n\t"
      "v_and_b32 v87, s46, v127\n\t"
      "v_pk_mul_f32 v[80:81], v[80:81], s[44:45]\n\t"
      "v_pk_mul_f32 v[82:83], v[82:83], s[44:45]\n\t"
      "v_pk_mul_f32 v[84:85], v[84:85], s[44:45]\n\t"
      "v_pk_mul_f32 v[86:87], v[86:87], s[44:45]\n\t"
      "v_cvt_pk_bf16_f32 v124, v80, v81\n\t"
      "v_cvt_pk_bf16_f32 v125, v82, v83\n\t"
      "v_cvt_pk_bf16_f32 v126, v84, v85\n\t"
      "v_cvt_pk_bf16_f32 v127, v86, v87\n\t"
      "s_waitcnt lgkmcnt(0)\n\t"
      "s_barrier\n\t"
      "ds_read_b128 v[144:147], v228 offset:0\n\t"
      "ds_read_b128 v[148:151], v229 offset:0\n\t"
      "ds_read_b128 v[152:155], v230 offset:0\n\t"
      "ds_read_b128 v[156:159], v231 offset:0\n\t"
      "s_waitcnt lgkmcnt(3)\n\t"
      "v_mfma_f32_16x16x32_bf16 v[64:67], v[144:147], v[112:115], 0\n\t"
      "s_waitcnt lgkmcnt(2)\n\t"
      "v_mfma_f32_16x16x32_bf16 v[64:67], v[148:151], v[116:119], v[64:67]\n\t"
      "s_waitcnt lgkmcnt(1)\n\t"
      "v_mfma_f32_16x16x32_bf16 v[64:67], v[152:155], v[120:123], v[64:67]\n\t"
      "s_waitcnt lgkmcnt(0)\n\t"
      "v_mfma_f32_16x16x32_bf16 v[64:67], v[156:159], v[124:127], v[64:67]\n\t"
      "s_nop 7\n\t"
      "v_max3_f32 v240, v64, v65, v66\n\t"
      "v_max_f32 v240, v240, v67\n\t"
      "v_mov_b32 v241, v240\n\t"
      "s_nop 1\n\t"
      "v_permlane16_swap_b32 v240, v241\n\t"
      "s_nop 0\n\t"
      "v_max_f32 v240, v240, v241\n\t"
      "v_mov_b32 v241, v240\n\t"
      "s_nop 1\n\t"
      "v_permlane32_swap_b32 v240, v241\n\t"
      "s_nop 0\n\t"
      "v_max_f32 v240, v240, v241\n\t"
      "v_sub_f32 v208, 0, v240\n\t"
      "v_sub_f32 v209, 0, v240\n\t"
      "v_sub_f32 v210, 0, v240\n\t"
      "v_sub_f32 v211, 0, v240\n\t"
      "v_add_f32 v64, v64, v208\n\t"
      "v_add_f32 v65, v65, v208\n\t"
      "v_add_f32 v66, v66, v208\n\t"
      "v_add_f32 v67, v67, v208\n\t"
      "s_barrier\n\t"
      "ds_read_b128 v[160:163], v228 offset:4096\n\t"
      "ds_read_b128 v[164:167], v229 offset:4096\n\t"
      "ds_read_b128 v[168:171], v230 offset:4096\n\t"
      "ds_read_b128 v[172:175], v231 offset:4096\n\t"
      "ds_read_b128 v[144:147], v228 offset:8192\n\t"
      "ds_read_b128 v[148:151], v229 offset:8192\n\t"
      "ds_read_b128 v[152:155], v230 offset:8192\n\t"
      "ds_read_b128 v[156:159], v231 offset:8192\n\t"
      "s_waitcnt lgkmcnt(7)\n\t"
      "v_mfma_f32_16x16x32_bf16 v[72:75], v[160:163], v[112:115], v[208:211]\n\t"
      "ds_read_b128 v[160:163], v228 offset:12288\n\t"
      "s_waitcnt lgkmcnt(7)\n\t"
      "v_mfma_f32_16x16x32_bf16 v[72:75], v[164:167], v[116:119], v[72:75]\n\t"
      "ds_read_b128 v[164:167], v229 offset:12288\n\t"
      "s_waitcnt lgkmcnt(7)\n\t"
      "v_mfma_f32_16x16x32_bf16 v[72:75], v[168:171], v[120:123], v[72:75]\n\t"
      "ds_read_b128 v[168:171], v230 offset:12288\n\t"
      "s_waitcnt lgkmcnt(7)\n\t"
      "v_mfma_f32_16x16x32_bf16 v[72:75], v[172:175], v[124:127], v[72:75]\n\t"
      "ds_read_b128 v[172:175], v231 offset:12288\n\t"
      "s_barrier\n\t"
      "s_waitcnt lgkmcnt(7)\n\t"
      "v_mfma_f32_16x16x32_bf16 v[80:83], v[144:147], v[112:115], v[208:211]\n\t"
      "v_exp_f32 v64, v64\n\t"
      "ds_read_b128 v[144:147], v228 offset:16384\n\t"
      "ds_read_b64_tr_b16 v[176:177], v236 offset:0\n\t"
      "s_waitcnt lgkmcnt(8)\n\t"
      "v_mfma_f32_16x16x32_bf16 v[80:83], v[148:151], v[116:119], v[80:83]\n\t"
      "v_exp_f32 v65, v65\n\t"
      "ds_read_b128 v[148:151], v229 offset:16384\n\t"
      "ds_read_b64_tr_b16 v[180:181], v237 offset:0\n\t"
      "ds_read_b64_tr_b16 v[178:179], v236 offset:4096\n\t"
      "v_cvt_pk_bf16_f32 v96, v64, v65\n\t"
      "s_waitcnt lgkmcnt(10)\n\t"
      "v_mfma_f32_16x16x32_bf16 v[80:83], v[152:155], v[120:123], v[80:83]\n\t"
      "v_exp_f32 v66, v66\n\t"
      "ds_read_b128 v[152:155], v230 offset:16384\n\t"
      "ds_read_b64_tr_b16 v[184:185], v236 offset:512\n\t"
      "ds_read_b64_tr_b16 v[182:183], v237 offset:4096\n\t"
      "s_waitcnt lgkmcnt(12)\n\t"
      "v_mfma_f32_16x16x32_bf16 v[80:83], v[156:159], v[124:127], v[80:83]\n\t"
      "v_exp_f32 v67, v67\n\t"
      "ds_read_b128 v[156:159], v231 offset:16384\n\t"
      "ds_read_b64_tr_b16 v[188:189], v237 offset:512\n\t"
      "ds_read_b64_tr_b16 v[186:187], v236 offset:4608\n\t"
      "v_cvt_pk_bf16_f32 v97, v66, v67\n\t"
      "s_waitcnt lgkmcnt(14)\n\t"
      "v_mfma_f32_16x16x32_bf16 v[88:91], v[160:163], v[112:115], v[208:211]\n\t"
      "v_exp_f32 v72, v72\n\t"
      "ds_read_b128 v[160:163], v228 offset:20480\n\t"
      "ds_read_b64_tr_b16 v[192:193], v236 offset:1024\n\t"
      "ds_read_b64_tr_b16 v[190:191], v237 offset:4608\n\t"
      "s_waitcnt lgkmcnt(15)\n\t"
      "v_mfma_f32_16x16x32_bf16 v[88:91], v[164:167], v[116:119], v[88:91]\n\t"
      "v_exp_f32 v73, v73\n\t"
      "ds_read_b128 v[164:167], v229 offset:20480\n\t"
      "ds_read_b64_tr_b16 v[196:197], v237 offset:1024\n\t"
      "ds_read_b64_tr_b16 v[194:195], v236 offset:5120\n\t"
      "v_cvt_pk_bf16_f32 v98, v72, v73\n\t"
      "v_mfma_f32_16x16x32_bf16 v[88:91], v[168:171], v[120:123], v[88:91]\n\t"
      "v_exp_f32 v74, v74\n\t"
      "ds_read_b128 v[168:171], v230 offset:20480\n\t"
      "ds_read_b64_tr_b16 v[200:201], v236 offset:1536\n\t"
      "ds_read_b64_tr_b16 v[198:199], v237 offset:5120\n\t"
      "s_waitcnt lgkmcnt(15)\n\t"
      "v_mfma_f32_16x16x32_bf16 v[88:91], v[172:175], v[124:127], v[88:91]\n\t"
      "v_exp_f32 v75, v75\n\t"
      "ds_read_b128 v[172:175], v231 offset:20480\n\t"
      "ds_read_b64_tr_b16 v[204:205], v237 offset:1536\n\t"
      "ds_read_b64_tr_b16 v[202:203], v236 offset:5632\n\t"
      "v_cvt_pk_bf16_f32 v99, v74, v75\n\t"
      "s_nop 0\n\t"
      "s_barrier\n\t"
      "v_mfma_f32_16x16x32_bf16 v[64:67], v[144:147], v[112:115], v[208:211]\n\t"
      "s_waitcnt lgkmcnt(8)\n\t"
      "v_exp_f32 v80, v80\n\t"
      "ds_read_b128 v[144:147], v228 offset:24576\n\t"
      "ds_read_b64_tr_b16 v[206:207], v237 offset:5632\n\t"
      "v_mfma_f32_16x16x32_bf16 v[0:3], v[176:179], v[96:99], 0\n\t"
      "ds_read_b64_tr_b16 v[176:177], v236 offset:8192\n\t"
      "v_mfma_f32_16x16x32_bf16 v[72:75], v[160:163], v[112:115], v[208:211]\n\t"
      "v_exp_f32 v81, v81\n\t"
      "ds_read_b128 v[160:163], v228 offset:28672\n\t"
      "ds_read_b64_tr_b16 v[178:179], v236 offset:12288\n\t"
      "v_mfma_f32_16x16x32_bf16 v[8:11], v[180:183], v[96:99], 0\n\t"
      "ds_read_b64_tr_b16 v[180:181], v237 offset:8192\n\t"
      "v_cvt_pk_bf16_f32 v104, v80, v81\n\t"
      "v_mfma_f32_16x16x32_bf16 v[64:67], v[148:151], v[116:119], v[64:67]\n\t"
      "v_exp_f32 v82, v82\n\t"
      "ds_read_b128 v[148:151], v229 offset:24576\n\t"
      "ds_read_b64_tr_b16 v[182:183], v237 offset:12288\n\t"
      "v_mfma_f32_16x16x32_bf16 v[16:19], v[184:187], v[96:99], 0\n\t"
      "ds_read_b64_tr_b16 v[184:185], v236 offset:8704\n\t"
      "v_mfma_f32_16x16x32_bf16 v[72:75], v[164:167], v[116:119], v[72:75]\n\t"
      "s_waitcnt lgkmcnt(12)\n\t"
      "v_exp_f32 v83, v83\n\t"
      "ds_read_b128 v[164:167], v229 offset:28672\n\t"
      "ds_read_b64_tr_b16 v[186:187], v236 offset:12800\n\t"
      "v_mfma_f32_16x16x32_bf16 v[24:27], v[188:191], v[96:99], 0\n\t"
      "ds_read_b64_tr_b16 v[188:189], v237 offset:8704\n\t"
      "v_cvt_pk_bf16_f32 v105, v82, v83\n\t"
      "v_mfma_f32_16x16x32_bf16 v[64:67], v[152:155], v[120:123], v[64:67]\n\t"
      "v_exp_f32 v88, v88\n\t"
      "ds_read_b128 v[152:155], v230 offset:24576\n\t"
      "ds_read_b64_tr_b16 v[190:191], v237 offset:12800\n\t"
      "v_mfma_f32_16x16x32_bf16 v[32:35], v[192:195], v[96:99], 0\n\t"
      "ds_read_b64_tr_b16 v[192:193], v236 offset:9216\n\t"
      "v_mfma_f32_16x16x32_bf16 v[72:75], v[168:171], v[120:123], v[72:75]\n\t"
      "v_exp_f32 v89, v89\n\t"
      "ds_read_b128 v[168:171], v230 offset:28672\n\t"
      "ds_read_b64_tr_b16 v[194:195], v236 offset:13312\n\t"
      "v_mfma_f32_16x16x32_bf16 v[40:43], v[196:199], v[96:99], 0\n\t"
      "ds_read_b64_tr_b16 v[196:197], v237 offset:9216\n\t"
      "v_cvt_pk_bf16_f32 v106, v88, v89\n\t"
      "v_mfma_f32_16x16x32_bf16 v[64:67], v[156:159], v[124:127], v[64:67]\n\t"
      "s_waitcnt lgkmcnt(15)\n\t"
      "v_exp_f32 v90, v90\n\t"
      "ds_read_b128 v[156:159], v231 offset:24576\n\t"
      "ds_read_b64_tr_b16 v[198:199], v237 offset:13312\n\t"
      "v_mfma_f32_16x16x32_bf16 v[48:51], v[200:203], v[96:99], 0\n\t"
      "ds_read_b64_tr_b16 v[200:201], v236 offset:9728\n\t"
      "v_mfma_f32_16x16x32_bf16 v[72:75], v[172:175], v[124:127], v[72:75]\n\t"
      "v_exp_f32 v91, v91\n\t"
      "ds_read_b128 v[172:175], v231 offset:28672\n\t"
      "ds_read_b64_tr_b16 v[202:203], v236 offset:13824\n\t"
      "v_mfma_f32_16x16x32_bf16 v[56:59], v[204:207], v[96:99], 0\n\t"
      "ds_read_b64_tr_b16 v[204:205], v237 offset:9728\n\t"
      "v_cvt_pk_bf16_f32 v107, v90, v91\n\t"
      "s_nop 0\n\t"
      "v_mfma_f32_16x16x32_bf16 v[216:219], v[224:227], v[96:99], 0\n\t"
      "s_barrier\n\t"
      "v_mfma_f32_16x16x32_bf16 v[80:83], v[144:147], v[112:115], v[208:211]\n\t"
      "s_waitcnt lgkmcnt(13)\n\t"
      "v_exp_f32 v64, v64\n\t"
      "ds_read_b128 v[144:147], v228 offset:32768\n\t"
      "ds_read_b64_tr_b16 v[206:207], v237 offset:13824\n\t"
      "v_mfma_f32_16x16x32_bf16 v[0:3], v[176:179], v[104:107], v[0:3]\n\t"
      "ds_read_b64_tr_b16 v[176:177], v236 offset:16384\n\t"
      "v_mfma_f32_16x16x32_bf16 v[88:91], v[160:163], v[112:115], v[208:211]\n\t"
      "v_exp_f32 v65, v65\n\t"
      "ds_read_b128 v[160:163], v228 offset:36864\n\t"
      "ds_read_b64_tr_b16 v[178:179], v236 offset:20480\n\t"
      "v_mfma_f32_16x16x32_bf16 v[8:11], v[180:183], v[104:107], v[8:11]\n\t"
      "ds_read_b64_tr_b16 v[180:181], v237 offset:16384\n\t"
      "v_cvt_pk_bf16_f32 v96, v64, v65\n\t"
      "v_mfma_f32_16x16x32_bf16 v[80:83], v[148:151], v[116:119], v[80:83]\n\t"
      "v_exp_f32 v66, v66\n\t"
      "ds_read_b128 v[148:151], v229 offset:32768\n\t"
      "ds_read_b64_tr_b16 v[182:183], v237 offset:20480\n\t"
      "v_mfma_f32_16x16x32_bf16 v[16:19], v[184:187], v[104:107], v[16:19]\n\t"
      "ds_read_b64_tr_b16 v[184:185], v236 offset:16896\n\t"
      "v_mfma_f32_16x16x32_bf16 v[88:91], v[164:167], v[116:119], v[88:91]\n\t"
      "s_waitcnt lgkmcnt(13)\n\t"
      "v_exp_f32 v67, v67\n\t"
      "ds_read_b128 v[164:167], v229 offset:36864\n\t"
      "ds_read_b64_tr_b16 v[186:187], v236 offset:20992\n\t"
      "v_mfma_f32_16x16x32_bf16 v[24:27], v[188:191], v[104:107], v[24:27]\n\t"
      "ds_read_b64_tr_b16 v[188:189], v237 offset:16896\n\t"
      "v_cvt_pk_bf16_f32 v97, v66, v67\n\t"
      "v_mfma_f32_16x16x32_bf16 v[80:83], v[152:155], v[120:123], v[80:83]\n\t"
      "v_exp_f32 v72, v72\n\t"
      "ds_read_b128 v[152:155], v230 offset:32768\n\t"
      "ds_read_b64_tr_b16 v[190:191], v237 offset:20992\n\t"
      "v_mfma_f32_16x16x32_bf16 v[32:35], v[192:195], v[104:107], v[32:35]\n\t"
      "ds_read_b64_tr_b16 v[192:193], v236 offset:17408\n\t"
      "v_mfma_f32_16x16x32_bf16 v[88:91], v[168:171], v[120:123], v[88:91]\n\t"
      "v_exp_f32 v73, v73\n\t"
      "ds_read_b128 v[168:171], v230 offset:36864\n\t"
      "ds_read_b64_tr_b16 v[194:195], v236 offset:21504\n\t"
      "v_mfma_f32_16x16x32_bf16 v[40:43], v[196:199], v[104:107], v[40:43]\n\t"
      "ds_read_b64_tr_b16 v[196:197], v237 offset:17408\n\t"
      "v_cvt_pk_bf16_f32 v98, v72, v73\n\t"
      "v_mfma_f32_16x16x32_bf16 v[80:83], v[156:159], v[124:127], v[80:83]\n\t"
      "s_waitcnt lgkmcnt(15)\n\t"
      "v_exp_f32 v74, v74\n\t"
      "ds_read_b128 v[156:159], v231 offset:32768\n\t"
      "ds_read_b64_tr_b16 v[198:199], v237 offset:21504\n\t"
      "v_mfma_f32_16x16x32_bf16 v[48:51], v[200:203], v[104:107], v[48:51]\n\t"
      "ds_read_b64_tr_b16 v[200:201], v236 offset:17920\n\t"
      "v_mfma_f32_16x16x32_bf16 v[88:91], v[172:175], v[124:127], v[88:91]\n\t"
      "v_exp_f32 v75, v75\n\t"
      "ds_read_b128 v[172:175], v231 offset:36864\n\t"
      "ds_read_b64_tr_b16 v[202:203], v236 offset:22016\n\t"
      "v_mfma_f32_16x16x32_bf16 v[56:59], v[204:207], v[104:107], v[56:59]\n\t"
      "ds_read_b64_tr_b16 v[204:205], v237 offset:17920\n\t"
      "v_cvt_pk_bf16_f32 v99, v74, v75\n\t"
      "s_nop 0\n\t"
      "v_mfma_f32_16x16x32_bf16 v[216:219], v[224:227], v[104:107], v[216:219]\n\t"
      "s_barrier\n\t"
      "v_mfma_f32_16x16x32_bf16 v[64:67], v[144:147], v[112:115], v[208:211]\n\t"
      "s_waitcnt lgkmcnt(13)\n\t"
      "v_exp_f32 v80, v80\n\t"
      "ds_read_b128 v[144:147], v228 offset:40960\n\t"
      "ds_read_b64_tr_b16 v[206:207], v237 offset:22016\n\t"
      "v_mfma_f32_16x16x32_bf16 v[0:3], v[176:179], v[96:99], v[0:3]\n\t"
      "ds_read_b64_tr_b16 v[176:177], v236 offset:24576\n\t"
      "v_mfma_f32_16x16x32_bf16 v[72:75], v[160:163], v[112:115], v[208:211]\n\t"
      "v_exp_f32 v81, v81\n\t"
      "ds_read_b128 v[160:163], v228 offset:45056\n\t"
      "ds_read_b64_tr_b16 v[178:179], v236 offset:28672\n\t"
      "v_mfma_f32_16x16x32_bf16 v[8:11], v[180:183], v[96:99], v[8:11]\n\t"
      "ds_read_b64_tr_b16 v[180:181], v237 offset:24576\n\t"
      "v_cvt_pk_bf16_f32 v104, v80, v81\n\t"
      "v_mfma_f32_16x16x32_bf16 v[64:67], v[148:151], v[116:119], v[64:67]\n\t"
      "v_exp_f32 v82, v82\n\t"
      "ds_read_b128 v[148:151], v229 offset:40960\n\t"
      "ds_read_b64_tr_b16 v[182:183], v237 offset:28672\n\t"
      "v_mfma_f32_16x16x32_bf16 v[16:19], v[184:187], v[96:99], v[16:19]\n\t"
      "ds_read_b64_tr_b16 v[184:185], v236 offset:25088\n\t"
      "v_mfma_f32_16x16x32_bf16 v[72:75], v[164:167], v[116:119], v[72:75]\n\t"
      "s_waitcnt lgkmcnt(13)\n\t"
      "v_exp_f32 v83, v83\n\t"
      "ds_read_b128 v[164:167], v229 offset:45056\n\t"
      "ds_read_b64_tr_b16 v[186:187], v236 offset:29184\n\t"
      "v_mfma_f32_16x16x32_bf16 v[24:27], v[188:191], v[96:99], v[24:27]\n\t"
      "ds_read_b64_tr_b16 v[188:189], v237 offset:25088\n\t"
      "v_cvt_pk_bf16_f32 v105, v82, v83\n\t"
      "v_mfma_f32_16x16x32_bf16 v[64:67], v[152:155], v[120:123], v[64:67]\n\t"
      "v_exp_f32 v88, v88\n\t"
      "ds_read_b128 v[152:155], v230 offset:40960\n\t"
      "ds_read_b64_tr_b16 v[190:191], v237 offset:29184\n\t"
      "v_mfma_f32_16x16x32_bf16 v[32:35], v[192:195], v[96:99], v[32:35]\n\t"
      "ds_read_b64_tr_b16 v[192:193], v236 offset:25600\n\t"
      "v_mfma_f32_16x16x32_bf16 v[72:75], v[168:171], v[120:123], v[72:75]\n\t"
      "v_exp_f32 v89, v89\n\t"
      "ds_read_b128 v[168:171], v230 offset:45056\n\t"
      "ds_read_b64_tr_b16 v[194:195], v236 offset:29696\n\t"
      "v_mfma_f32_16x16x32_bf16 v[40:43], v[196:199], v[96:99], v[40:43]\n\t"
      "ds_read_b64_tr_b16 v[196:197], v237 offset:25600\n\t"
      "v_cvt_pk_bf16_f32 v106, v88, v89\n\t"
      "v_mfma_f32_16x16x32_bf16 v[64:67], v[156:159], v[124:127], v[64:67]\n\t"
      "s_waitcnt lgkmcnt(15)\n\t"
      "v_exp_f32 v90, v90\n\t"
      "ds_read_b128 v[156:159], v231 offset:40960\n\t"
      "ds_read_b64_tr_b16 v[198:199], v237 offset:29696\n\t"
      "v_mfma_f32_16x16x32_bf16 v[48:51], v[200:203], v[96:99], v[48:51]\n\t"
      "ds_read_b64_tr_b16 v[200:201], v236 offset:26112\n\t"
      "v_mfma_f32_16x16x32_bf16 v[72:75], v[172:175], v[124:127], v[72:75]\n\t"
      "v_exp_f32 v91, v91\n\t"
      "ds_read_b128 v[172:175], v231 offset:45056\n\t"
      "ds_read_b64_tr_b16 v[202:203], v236 offset:30208\n\t"
      "v_mfma_f32_16x16x32_bf16 v[56:59], v[204:207], v[96:99], v[56:59]\n\t"
      "ds_read_b64_tr_b16 v[204:205], v237 offset:26112\n\t"
      "v_cvt_pk_bf16_f32 v107, v90, v91\n\t"
      "s_nop 0\n\t"
      "v_mfma_f32_16x16x32_bf16 v[216:219], v[224:227], v[96:99], v[216:219]\n\t"
      "s_barrier\n\t"
      "v_mfma_f32_16x16x32_bf16 v[80:83], v[144:147], v[112:115], v[208:211]\n\t"
      "s_waitcnt lgkmcnt(13)\n\t"
      "v_exp_f32 v64, v64\n\t"
      "ds_read_b128 v[144:147], v228 offset:49152\n\t"
      "ds_read_b64_tr_b16 v[206:207], v237 offset:30208\n\t"
      "v_mfma_f32_16x16x32_bf16 v[0:3], v[176:179], v[104:107], v[0:3]\n\t"
      "ds_read_b64_tr_b16 v[176:177], v236 offset:32768\n\t"
      "v_mfma_f32_16x16x32_bf16 v[88:91], v[160:163], v[112:115], v[208:211]\n\t"
      "v_exp_f32 v65, v65\n\t"
      "ds_read_b128 v[160:163], v228 offset:53248\n\t"
      "ds_read_b64_tr_b16 v[178:179], v236 offset:36864\n\t"
      "v_mfma_f32_16x16x32_bf16 v[8:11], v[180:183], v[104:107], v[8:11]\n\t"
      "ds_read_b64_tr_b16 v[180:181], v237 offset:32768\n\t"
      "v_cvt_pk_bf16_f32 v96, v64, v65\n\t"
      "v_mfma_f32_16x16x32_bf16 v[80:83], v[148:151], v[116:119], v[80:83]\n\t"
      "v_exp_f32 v66, v66\n\t"
      "ds_read_b128 v[148:151], v229 offset:49152\n\t"
      "ds_read_b64_tr_b16 v[182:183], v237 offset:36864\n\t"
      "v_mfma_f32_16x16x32_bf16 v[16:19], v[184:187], v[104:107], v[16:19]\n\t"
      "ds_read_b64_tr_b16 v[184:185], v236 offset:33280\n\t"
      "v_mfma_f32_16x16x32_bf16 v[88:91], v[164:167], v[116:119], v[88:91]\n\t"
      "s_waitcnt lgkmcnt(13)\n\t"
      "v_exp_f32 v67, v67\n\t"
      "ds_read_b128 v[164:167], v229 offset:53248\n\t"
      "ds_read_b64_tr_b16 v[186:187], v236 offset:37376\n\t"
      "v_mfma_f32_16x16x32_bf16 v[24:27], v[188:191], v[104:107], v[24:27]\n\t"
      "ds_read_b64_tr_b16 v[188:189], v237 offset:33280\n\t"
      "v_cvt_pk_bf16_f32 v97, v66, v67\n\t"
      "v_mfma_f32_16x16x32_bf16 v[80:83], v[152:155], v[120:123], v[80:83]\n\t"
      "v_exp_f32 v72, v72\n\t"
      "ds_read_b128 v[152:155], v230 offset:49152\n\t"
      "ds_read_b64_tr_b16 v[190:191], v237 offset:37376\n\t"
      "v_mfma_f32_16x16x32_bf16 v[32:35], v[192:195], v[104:107], v[32:35]\n\t"
      "ds_read_b64_tr_b16 v[192:193], v236 offset:33792\n\t"
      "v_mfma_f32_16x16x32_bf16 v[88:91], v[168:171], v[120:123], v[88:91]\n\t"
      "v_exp_f32 v73, v73\n\t"
      "ds_read_b128 v[168:171], v230 offset:53248\n\t"
      "ds_read_b64_tr_b16 v[194:195], v236 offset:37888\n\t"
      "v_mfma_f32_16x16x32_bf16 v[40:43], v[196:199], v[104:107], v[40:43]\n\t"
      "ds_read_b64_tr_b16 v[196:197], v237 offset:33792\n\t"
      "v_cvt_pk_bf16_f32 v98, v72, v73\n\t"
      "v_mfma_f32_16x16x32_bf16 v[80:83], v[156:159], v[124:127], v[80:83]\n\t"
      "s_waitcnt lgkmcnt(15)\n\t"
      "v_exp_f32 v74, v74\n\t"
      "ds_read_b128 v[156:159], v231 offset:49152\n\t"
      "ds_read_b64_tr_b16 v[198:199], v237 offset:37888\n\t"
      "v_mfma_f32_16x16x32_bf16 v[48:51], v[200:203], v[104:107], v[48:51]\n\t"
      "ds_read_b64_tr_b16 v[200:201], v236 offset:34304\n\t"
      "v_mfma_f32_16x16x32_bf16 v[88:91], v[172:175], v[124:127], v[88:91]\n\t"
      "v_exp_f32 v75, v75\n\t"
      "ds_read_b128 v[172:175], v231 offset:53248\n\t"
      "ds_read_b64_tr_b16 v[202:203], v236 offset:38400\n\t"
      "v_mfma_f32_16x16x32_bf16 v[56:59], v[204:207], v[104:107], v[56:59]\n\t"
      "ds_read_b64_tr_b16 v[204:205], v237 offset:34304\n\t"
      "v_cvt_pk_bf16_f32 v99, v74, v75\n\t"
      "s_nop 0\n\t"
      "v_mfma_f32_16x16x32_bf16 v[216:219], v[224:227], v[104:107], v[216:219]\n\t"
      "v_mfma_f32_16x16x32_bf16 v[64:67], v[144:147], v[112:115], v[208:211]\n\t"
      "s_waitcnt lgkmcnt(13)\n\t"
      "v_exp_f32 v80, v80\n\t"
      "ds_read_b128 v[144:147], v228 offset:57344\n\t"
      "ds_read_b64_tr_b16 v[206:207], v237 offset:38400\n\t"
      "v_mfma_f32_16x16x32_bf16 v[0:3], v[176:179], v[96:99], v[0:3]\n\t"
      "ds_read_b64_tr_b16 v[176:177], v236 offset:40960\n\t"
      "v_mfma_f32_16x16x32_bf16 v[72:75], v[160:163], v[112:115], v[208:211]\n\t"
      "v_exp_f32 v81, v81\n\t"
      "ds_read_b128 v[160:163], v228 offset:61440\n\t"
      "ds_read_b64_tr_b16 v[178:179], v236 offset:45056\n\t"
      "v_mfma_f32_16x16x32_bf16 v[8:11], v[180:183], v[96:99], v[8:11]\n\t"
      "ds_read_b64_tr_b16 v[180:181], v237 offset:40960\n\t"
      "v_cvt_pk_bf16_f32 v104, v80, v81\n\t"
      "v_mfma_f32_16x16x32_bf16 v[64:67], v[148:151], v[116:119], v[64:67]\n\t"
      "v_exp_f32 v82, v82\n\t"
      "ds_read_b128 v[148:151], v229 offset:57344\n\t"
      "ds_read_b64_tr_b16 v[182:183], v237 offset:45056\n\t"
      "v_mfma_f32_16x16x32_bf16 v[16:19], v[184:187], v[96:99], v[16:19]\n\t"
      "ds_read_b64_tr_b16 v[184:185], v236 offset:41472\n\t"
      "v_mfma_f32_16x16x32_bf16 v[72:75], v[164:167], v[116:119], v[72:75]\n\t"
      "s_waitcnt lgkmcnt(13)\n\t"
      "v_exp_f32 v83, v83\n\t"
      "ds_read_b128 v[164:167], v229 offset:61440\n\t"
      "ds_read_b64_tr_b16 v[186:187], v236 offset:45568\n\t"
      "v_mfma_f32_16x16x32_bf16 v[24:27], v[188:191], v[96:99], v[24:27]\n\t"
      "ds_read_b64_tr_b16 v[188:189], v237 offset:41472\n\t"
      "v_cvt_pk_bf16_f32 v105, v82, v83\n\t"
      "v_mfma_f32_16x16x32_bf16 v[64:67], v[152:155], v[120:123], v[64:67]\n\t"
      "v_exp_f32 v88, v88\n\t"
      "ds_read_b128 v[152:155], v230 offset:57344\n\t"
      "ds_read_b64_tr_b16 v[190:191], v237 offset:45568\n\t"
      "v_mfma_f32_16x16x32_bf16 v[32:35], v[192:195], v[96:99], v[32:35]\n\t"
      "ds_read_b64_tr_b16 v[192:193], v236 offset:41984\n\t"
      "v_mfma_f32_16x16x32_bf16 v[72:75], v[168:171], v[120:123], v[72:75]\n\t"
      "v_exp_f32 v89, v89\n\t"
      "ds_read_b128 v[168:171], v230 offset:61440\n\t"
      "ds_read_b64_tr_b16 v[194:195], v236 offset:46080\n\t"
      "v_mfma_f32_16x16x32_bf16 v[40:43], v[196:199], v[96:99], v[40:43]\n\t"
      "ds_read_b64_tr_b16 v[196:197], v237 offset:41984\n\t"
      "v_cvt_pk_bf16_f32 v106, v88, v89\n\t"
      "v_mfma_f32_16x16x32_bf16 v[64:67], v[156:159], v[124:127], v[64:67]\n\t"
      "s_waitcnt lgkmcnt(15)\n\t"
      "v_exp_f32 v90, v90\n\t"
      "ds_read_b128 v[156:159], v231 offset:57344\n\t"
      "ds_read_b64_tr_b16 v[198:199], v237 offset:46080\n\t"
      "v_mfma_f32_16x16x32_bf16 v[48:51], v[200:203], v[96:99], v[48:51]\n\t"
      "ds_read_b64_tr_b16 v[200:201], v236 offset:42496\n\t"
      "v_mfma_f32_16x16x32_bf16 v[72:75], v[172:175], v[124:127], v[72:75]\n\t"
      "v_exp_f32 v91, v91\n\t"
      "ds_read_b128 v[172:175], v231 offset:61440\n\t"
      "ds_read_b64_tr_b16 v[202:203], v236 offset:46592\n\t"
      "v_mfma_f32_16x16x32_bf16 v[56:59], v[204:207], v[96:99], v[56:59]\n\t"
      "ds_read_b64_tr_b16 v[204:205], v237 offset:42496\n\t"
      "v_cvt_pk_bf16_f32 v107, v90, v91\n\t"
      "s_nop 0\n\t"
      "v_mfma_f32_16x16x32_bf16 v[216:219], v[224:227], v[96:99], v[216:219]\n\t"
      "s_barrier\n\t"
      "v_mfma_f32_16x16x32_bf16 v[80:83], v[144:147], v[112:115], v[208:211]\n\t"
      "s_waitcnt lgkmcnt(13)\n\t"
      "v_exp_f32 v64, v64\n\t"
      "ds_read_b128 v[144:147], v232 offset:0\n\t"
      "ds_read_b64_tr_b16 v[206:207], v237 offset:46592\n\t"
      "v_mfma_f32_16x16x32_bf16 v[0:3], v[176:179], v[104:107], v[0:3]\n\t"
      "ds_read_b64_tr_b16 v[176:177], v236 offset:49152\n\t"
      "v_mfma_f32_16x16x32_bf16 v[88:91], v[160:163], v[112:115], v[208:211]\n\t"
      "v_exp_f32 v65, v65\n\t"
      "ds_read_b128 v[160:163], v232 offset:4096\n\t"
      "ds_read_b64_tr_b16 v[178:179], v236 offset:53248\n\t"
      "v_mfma_f32_16x16x32_bf16 v[8:11], v[180:183], v[104:107], v[8:11]\n\t"
      "ds_read_b64_tr_b16 v[180:181], v237 offset:49152\n\t"
      "v_cvt_pk_bf16_f32 v96, v64, v65\n\t"
      "v_mfma_f32_16x16x32_bf16 v[80:83], v[148:151], v[116:119], v[80:83]\n\t"
      "v_exp_f32 v66, v66\n\t"
      "ds_read_b128 v[148:151], v233 offset:0\n\t"
      "ds_read_b64_tr_b16 v[182:183], v237 offset:53248\n\t"
      "v_mfma_f32_16x16x32_bf16 v[16:19], v[184:187], v[104:107], v[16:19]\n\t"
      "ds_read_b64_tr_b16 v[184:185], v236 offset:49664\n\t"
      "v_mfma_f32_16x16x32_bf16 v[88:91], v[164:167], v[116:119], v[88:91]\n\t"
      "s_waitcnt lgkmcnt(13)\n\t"
      "v_exp_f32 v67, v67\n\t"
      "ds_read_b128 v[164:167], v233 offset:4096\n\t"
      "ds_read_b64_tr_b16 v[186:187], v236 offset:53760\n\t"
      "v_mfma_f32_16x16x32_bf16 v[24:27], v[188:191], v[104:107], v[24:27]\n\t"
      "ds_read_b64_tr_b16 v[188:189], v237 offset:49664\n\t"
      "v_cvt_pk_bf16_f32 v97, v66, v67\n\t"
      "v_mfma_f32_16x16x32_bf16 v[80:83], v[152:155], v[120:123], v[80:83]\n\t"
      "v_exp_f32 v72, v72\n\t"
      "ds_read_b128 v[152:155], v234 offset:0\n\t"
      "ds_read_b64_tr_b16 v[190:191], v237 offset:53760\n\t"
      "v_mfma_f32_16x16x32_bf16 v[32:35], v[192:195], v[104:107], v[32:35]\n\t"
      "ds_read_b64_tr_b16 v[192:193], v236 offset:50176\n\t"
      "v_mfma_f32_16x16x32_bf16 v[88:91], v[168:171], v[120:123], v[88:91]\n\t"
      "v_exp_f32 v73, v73\n\t"
      "ds_read_b128 v[168:171], v234 offset:4096\n\t"
      "ds_read_b64_tr_b16 v[194:195], v236 offset:54272\n\t"
      "v_mfma_f32_16x16x32_bf16 v[40:43], v[196:199], v[104:107], v[40:43]\n\t"
      "ds_read_b64_tr_b16 v[196:197], v237 offset:50176\n\t"
      "v_cvt_pk_bf16_f32 v98, v72, v73\n\t"
      "v_mfma_f32_16x16x32_bf16 v[80:83], v[156:159], v[124:127], v[80:83]\n\t"
      "s_waitcnt lgkmcnt(15)\n\t"
      "v_exp_f32 v74, v74\n\t"
      "ds_read_b128 v[156:159], v235 offset:0\n\t"
      "ds_read_b64_tr_b16 v[198:199], v237 offset:54272\n\t"
      "v_mfma_f32_16x16x32_bf16 v[48:51], v[200:203], v[104:107], v[48:51]\n\t"
      "ds_read_b64_tr_b16 v[200:201], v236 offset:50688\n\t"
      "v_mfma_f32_16x16x32_bf16 v[88:91], v[172:175], v[124:127], v[88:91]\n\t"
      "v_exp_f32 v75, v75\n\t"
      "ds_read_b128 v[172:175], v235 offset:4096\n\t"
      "ds_read_b64_tr_b16 v[202:203], v236 offset:54784\n\t"
      "v_mfma_f32_16x16x32_bf16 v[56:59], v[204:207], v[104:107], v[56:59]\n\t"
      "ds_read_b64_tr_b16 v[204:205], v237 offset:50688\n\t"
      "v_cvt_pk_bf16_f32 v99, v74, v75\n\t"
      "s_nop 0\n\t"
      "v_mfma_f32_16x16x32_bf16 v[216:219], v[224:227], v[104:107], v[216:219]\n\t"
      "v_mfma_f32_16x16x32_bf16 v[64:67], v[144:147], v[112:115], v[208:211]\n\t"
      "s_waitcnt lgkmcnt(13)\n\t"
      "v_exp_f32 v80, v80\n\t"
      "ds_read_b128 v[144:147], v232 offset:8192\n\t"
      "ds_read_b64_tr_b16 v[206:207], v237 offset:54784\n\t"
      "v_mfma_f32_16x16x32_bf16 v[0:3], v[176:179], v[96:99], v[0:3]\n\t"
      "ds_read_b64_tr_b16 v[176:177], v236 offset:57344\n\t"
      "v_mfma_f32_16x16x32_bf16 v[72:75], v[160:163], v[112:115], v[208:211]\n\t"
      "v_exp_f32 v81, v81\n\t"
      "ds_read_b128 v[160:163], v232 offset:12288\n\t"
      "ds_read_b64_tr_b16 v[178:179], v236 offset:61440\n\t"
      "v_mfma_f32_16x16x32_bf16 v[8:11], v[180:183], v[96:99], v[8:11]\n\t"
      "ds_read_b64_tr_b16 v[180:181], v237 offset:57344\n\t"
      "v_cvt_pk_bf16_f32 v104, v80, v81\n\t"
      "v_mfma_f32_16x16x32_bf16 v[64:67], v[148:151], v[116:119], v[64:67]\n\t"
      "v_exp_f32 v82, v82\n\t"
      "ds_read_b128 v[148:151], v233 offset:8192\n\t"
      "ds_read_b64_tr_b16 v[182:183], v237 offset:61440\n\t"
      "v_mfma_f32_16x16x32_bf16 v[16:19], v[184:187], v[96:99], v[16:19]\n\t"
      "ds_read_b64_tr_b16 v[184:185], v236 offset:57856\n\t"
      "v_mfma_f32_16x16x32_bf16 v[72:75], v[164:167], v[116:119], v[72:75]\n\t"
      "s_waitcnt lgkmcnt(13)\n\t"
      "v_exp_f32 v83, v83\n\t"
      "ds_read_b128 v[164:167], v233 offset:12288\n\t"
      "ds_read_b64_tr_b16 v[186:187], v236 offset:61952\n\t"
      "v_mfma_f32_16x16x32_bf16 v[24:27], v[188:191], v[96:99], v[24:27]\n\t"
      "ds_read_b64_tr_b16 v[188:189], v237 offset:57856\n\t"
      "v_cvt_pk_bf16_f32 v105, v82, v83\n\t"
      "v_mfma_f32_16x16x32_bf16 v[64:67], v[152:155], v[120:123], v[64:67]\n\t"
      "v_exp_f32 v88, v88\n\t"
      "ds_read_b128 v[152:155], v234 offset:8192\n\t"
      "ds_read_b64_tr_b16 v[190:191], v237 offset:61952\n\t"
      "v_mfma_f32_16x16x32_bf16 v[32:35], v[192:195], v[96:99], v[32:35]\n\t"
      "ds_read_b64_tr_b16 v[192:193], v236 offset:58368\n\t"
      "v_mfma_f32_16x16x32_bf16 v[72:75], v[168:171], v[120:123], v[72:75]\n\t"
      "v_exp_f32 v89, v89\n\t"
      "ds_read_b128 v[168:171], v234 offset:12288\n\t"
      "ds_read_b64_tr_b16 v[194:195], v236 offset:62464\n\t"
      "v_mfma_f32_16x16x32_bf16 v[40:43], v[196:199], v[96:99], v[40:43]\n\t"
      "ds_read_b64_tr_b16 v[196:197], v237 offset:58368\n\t"
      "v_cvt_pk_bf16_f32 v106, v88, v89\n\t"
      "v_mfma_f32_16x16x32_bf16 v[64:67], v[156:159], v[124:127], v[64:67]\n\t"
      "s_waitcnt lgkmcnt(15)\n\t"
      "v_exp_f32 v90, v90\n\t"
      "ds_read_b128 v[156:159], v235 offset:8192\n\t"
      "ds_read_b64_tr_b16 v[198:199], v237 offset:62464\n\t"
      "v_mfma_f32_16x16x32_bf16 v[48:51], v[200:203], v[96:99], v[48:51]\n\t"
      "ds_read_b64_tr_b16 v[200:201], v236 offset:58880\n\t"
      "v_mfma_f32_16x16x32_bf16 v[72:75], v[172:175], v[124:127], v[72:75]\n\t"
      "v_exp_f32 v91, v91\n\t"
      "ds_read_b128 v[172:175], v235 offset:12288\n\t"
      "ds_read_b64_tr_b16 v[202:203], v236 offset:62976\n\t"
      "v_mfma_f32_16x16x32_bf16 v[56:59], v[204:207], v[96:99], v[56:59]\n\t"
      "ds_read_b64_tr_b16 v[204:205], v237 offset:58880\n\t"
      "v_cvt_pk_bf16_f32 v107, v90, v91\n\t"
      "s_nop 0\n\t"
      "v_mfma_f32_16x16x32_bf16 v[216:219], v[224:227], v[96:99], v[216:219]\n\t"
      "s_barrier\n\t"
      "v_mfma_f32_16x16x32_bf16 v[80:83], v[144:147], v[112:115], v[208:211]\n\t"
      "s_waitcnt lgkmcnt(13)\n\t"
      "v_exp_f32 v64, v64\n\t"
      "ds_read_b128 v[144:147], v228 offset:0\n\t"
      "ds_read_b64_tr_b16 v[206:207], v237 offset:62976\n\t"
      "v_mfma_f32_16x16x32_bf16 v[0:3], v[176:179], v[104:107], v[0:3]\n\t"
      "ds_read_b64_tr_b16 v[176:177], v238 offset:0\n\t"
      "v_mfma_f32_16x16x32_bf16 v[88:91], v[160:163], v[112:115], v[208:211]\n\t"
      "v_exp_f32 v65, v65\n\t"
      "ds_read_b128 v[160:163], v228 offset:4096\n\t"
      "ds_read_b64_tr_b16 v[178:179], v238 offset:4096\n\t"
      "v_mfma_f32_16x16x32_bf16 v[8:11], v[180:183], v[104:107], v[8:11]\n\t"
      "ds_read_b64_tr_b16 v[180:181], v239 offset:0\n\t"
      "v_cvt_pk_bf16_f32 v96, v64, v65\n\t"
      "v_mfma_f32_16x16x32_bf16 v[80:83], v[148:151], v[116:119], v[80:83]\n\t"
      "v_exp_f32 v66, v66\n\t"
      "ds_read_b128 v[148:151], v229 offset:0\n\t"
      "ds_read_b64_tr_b16 v[182:183], v239 offset:4096\n\t"
      "v_mfma_f32_16x16x32_bf16 v[16:19], v[184:187], v[104:107], v[16:19]\n\t"
      "ds_read_b64_tr_b16 v[184:185], v238 offset:512\n\t"
      "v_mfma_f32_16x16x32_bf16 v[88:91], v[164:167], v[116:119], v[88:91]\n\t"
      "s_waitcnt lgkmcnt(13)\n\t"
      "v_exp_f32 v67, v67\n\t"
      "ds_read_b128 v[164:167], v229 offset:4096\n\t"
      "ds_read_b64_tr_b16 v[186:187], v238 offset:4608\n\t"
      "v_mfma_f32_16x16x32_bf16 v[24:27], v[188:191], v[104:107], v[24:27]\n\t"
      "ds_read_b64_tr_b16 v[188:189], v239 offset:512\n\t"
      "v_cvt_pk_bf16_f32 v97, v66, v67\n\t"
      "v_mfma_f32_16x16x32_bf16 v[80:83], v[152:155], v[120:123], v[80:83]\n\t"
      "v_exp_f32 v72, v72\n\t"
      "ds_read_b128 v[152:155], v230 offset:0\n\t"
      "ds_read_b64_tr_b16 v[190:191], v239 offset:4608\n\t"
      "v_mfma_f32_16x16x32_bf16 v[32:35], v[192:195], v[104:107], v[32:35]\n\t"
      "ds_read_b64_tr_b16 v[192:193], v238 offset:1024\n\t"
      "v_mfma_f32_16x16x32_bf16 v[88:91], v[168:171], v[120:123], v[88:91]\n\t"
      "v_exp_f32 v73, v73\n\t"
      "ds_read_b128 v[168:171], v230 offset:4096\n\t"
      "ds_read_b64_tr_b16 v[194:195], v238 offset:5120\n\t"
      "v_mfma_f32_16x16x32_bf16 v[40:43], v[196:199], v[104:107], v[40:43]\n\t"
      "ds_read_b64_tr_b16 v[196:197], v239 offset:1024\n\t"
      "v_cvt_pk_bf16_f32 v98, v72, v73\n\t"
      "v_mfma_f32_16x16x32_bf16 v[80:83], v[156:159], v[124:127], v[80:83]\n\t"
      "s_waitcnt lgkmcnt(15)\n\t"
      "v_exp_f32 v74, v74\n\t"
      "ds_read_b128 v[156:159], v231 offset:0\n\t"
      "ds_read_b64_tr_b16 v[198:199], v239 offset:5120\n\t"
      "v_mfma_f32_16x16x32_bf16 v[48:51], v[200:203], v[104:107], v[48:51]\n\t"
      "ds_read_b64_tr_b16 v[200:201], v238 offset:1536\n\t"
      "v_mfma_f32_16x16x32_bf16 v[88:91], v[172:175], v[124:127], v[88:91]\n\t"
      "v_exp_f32 v75, v75\n\t"
      "ds_read_b128 v[172:175], v231 offset:4096\n\t"
      "ds_read_b64_tr_b16 v[202:203], v238 offset:5632\n\t"
      "v_mfma_f32_16x16x32_bf16 v[56:59], v[204:207], v[104:107], v[56:59]\n\t"
      "ds_read_b64_tr_b16 v[204:205], v239 offset:1536\n\t"
      "v_cvt_pk_bf16_f32 v99, v74, v75\n\t"
      "s_nop 0\n\t"
      "v_mfma_f32_16x16x32_bf16 v[216:219], v[224:227], v[104:107], v[216:219]\n\t"
      "v_mfma_f32_16x16x32_bf16 v[64:67], v[144:147], v[112:115], v[208:211]\n\t"
      "s_waitcnt lgkmcnt(13)\n\t"
      "v_exp_f32 v80, v80\n\t"
      "ds_read_b128 v[144:147], v228 offset:8192\n\t"
      "ds_read_b64_tr_b16 v[206:207], v239 offset:5632\n\t"
      "v_mfma_f32_16x16x32_bf16 v[0:3], v[176:179], v[96:99], v[0:3]\n\t"
      "ds_read_b64_tr_b16 v[176:177], v238 offset:8192\n\t"
      "v_mfma_f32_16x16x32_bf16 v[72:75], v[160:163], v[112:115], v[208:211]\n\t"
      "v_exp_f32 v81, v81\n\t"
      "ds_read_b128 v[160:163], v228 offset:12288\n\t"
      "ds_read_b64_tr_b16 v[178:179], v238 offset:12288\n\t"
      "v_mfma_f32_16x16x32_bf16 v[8:11], v[180:183], v[96:99], v[8:11]\n\t"
      "ds_read_b64_tr_b16 v[180:181], v239 offset:8192\n\t"
      "v_cvt_pk_bf16_f32 v104, v80, v81\n\t"
      "v_mfma_f32_16x16x32_bf16 v[64:67], v[148:151], v[116:119], v[64:67]\n\t"
      "v_exp_f32 v82, v82\n\t"
      "ds_read_b128 v[148:151], v229 offset:8192\n\t"
      "ds_read_b64_tr_b16 v[182:183], v239 offset:12288\n\t"
      "v_mfma_f32_16x16x32_bf16 v[16:19], v[184:187], v[96:99], v[16:19]\n\t"
      "ds_read_b64_tr_b16 v[184:185], v238 offset:8704\n\t"
      "v_mfma_f32_16x16x32_bf16 v[72:75], v[164:167], v[116:119], v[72:75]\n\t"
      "s_waitcnt lgkmcnt(13)\n\t"
      "v_exp_f32 v83, v83\n\t"
      "ds_read_b128 v[164:167], v229 offset:12288\n\t"
      "ds_read_b64_tr_b16 v[186:187], v238 offset:12800\n\t"
      "v_mfma_f32_16x16x32_bf16 v[24:27], v[188:191], v[96:99], v[24:27]\n\t"
      "ds_read_b64_tr_b16 v[188:189], v239 offset:8704\n\t"
      "v_cvt_pk_bf16_f32 v105, v82, v83\n\t"
      "v_mfma_f32_16x16x32_bf16 v[64:67], v[152:155], v[120:123], v[64:67]\n\t"
      "v_exp_f32 v88, v88\n\t"
      "ds_read_b128 v[152:155], v230 offset:8192\n\t"
      "ds_read_b64_tr_b16 v[190:191], v239 offset:12800\n\t"
      "v_mfma_f32_16x16x32_bf16 v[32:35], v[192:195], v[96:99], v[32:35]\n\t"
      "ds_read_b64_tr_b16 v[192:193], v238 offset:9216\n\t"
      "v_mfma_f32_16x16x32_bf16 v[72:75], v[168:171], v[120:123], v[72:75]\n\t"
      "v_exp_f32 v89, v89\n\t"
      "ds_read_b128 v[168:171], v230 offset:12288\n\t"
      "ds_read_b64_tr_b16 v[194:195], v238 offset:13312\n\t"
      "v_mfma_f32_16x16x32_bf16 v[40:43], v[196:199], v[96:99], v[40:43]\n\t"
      "ds_read_b64_tr_b16 v[196:197], v239 offset:9216\n\t"
      "v_cvt_pk_bf16_f32 v106, v88, v89\n\t"
      "v_mfma_f32_16x16x32_bf16 v[64:67], v[156:159], v[124:127], v[64:67]\n\t"
      "s_waitcnt lgkmcnt(15)\n\t"
      "v_exp_f32 v90, v90\n\t"
      "ds_read_b128 v[156:159], v231 offset:8192\n\t"
      "ds_read_b64_tr_b16 v[198:199], v239 offset:13312\n\t"
      "v_mfma_f32_16x16x32_bf16 v[48:51], v[200:203], v[96:99], v[48:51]\n\t"
      "ds_read_b64_tr_b16 v[200:201], v238 offset:9728\n\t"
      "v_mfma_f32_16x16x32_bf16 v[72:75], v[172:175], v[124:127], v[72:75]\n\t"
      "v_exp_f32 v91, v91\n\t"
      "ds_read_b128 v[172:175], v231 offset:12288\n\t"
      "ds_read_b64_tr_b16 v[202:203], v238 offset:13824\n\t"
      "v_mfma_f32_16x16x32_bf16 v[56:59], v[204:207], v[96:99], v[56:59]\n\t"
      "ds_read_b64_tr_b16 v[204:205], v239 offset:9728\n\t"
      "v_cvt_pk_bf16_f32 v107, v90, v91\n\t"
      "s_nop 0\n\t"
      "v_mfma_f32_16x16x32_bf16 v[216:219], v[224:227], v[96:99], v[216:219]\n\t"
      "s_barrier\n\t"
      "v_mfma_f32_16x16x32_bf16 v[80:83], v[144:147], v[112:115], v[208:211]\n\t"
      "s_waitcnt lgkmcnt(13)\n\t"
      "v_exp_f32 v64, v64\n\t"
      "ds_read_b128 v[144:147], v228 offset:16384\n\t"
      "ds_read_b64_tr_b16 v[206:207], v239 offset:13824\n\t"
      "v_mfma_f32_16x16x32_bf16 v[0:3], v[176:179], v[104:107], v[0:3]\n\t"
      "ds_read_b64_tr_b16 v[176:177], v236 offset:0\n\t"
      "v_mfma_f32_16x16x32_bf16 v[88:91], v[160:163], v[112:115], v[208:211]\n\t"
      "v_exp_f32 v65, v65\n\t"
      "ds_read_b128 v[160:163], v228 offset:20480\n\t"
      "ds_read_b64_tr_b16 v[178:179], v236 offset:4096\n\t"
      "v_mfma_f32_16x16x32_bf16 v[8:11], v[180:183], v[104:107], v[8:11]\n\t"
      "ds_read_b64_tr_b16 v[180:181], v237 offset:0\n\t"
      "v_cvt_pk_bf16_f32 v96, v64, v65\n\t"
      "v_mfma_f32_16x16x32_bf16 v[80:83], v[148:151], v[116:119], v[80:83]\n\t"
      "v_exp_f32 v66, v66\n\t"
      "ds_read_b128 v[148:151], v229 offset:16384\n\t"
      "ds_read_b64_tr_b16 v[182:183], v237 offset:4096\n\t"
      "v_mfma_f32_16x16x32_bf16 v[16:19], v[184:187], v[104:107], v[16:19]\n\t"
      "ds_read_b64_tr_b16 v[184:185], v236 offset:512\n\t"
      "v_mfma_f32_16x16x32_bf16 v[88:91], v[164:167], v[116:119], v[88:91]\n\t"
      "s_waitcnt lgkmcnt(13)\n\t"
      "v_exp_f32 v67, v67\n\t"
      "ds_read_b128 v[164:167], v229 offset:20480\n\t"
      "ds_read_b64_tr_b16 v[186:187], v236 offset:4608\n\t"
      "v_mfma_f32_16x16x32_bf16 v[24:27], v[188:191], v[104:107], v[24:27]\n\t"
      "ds_read_b64_tr_b16 v[188:189], v237 offset:512\n\t"
      "v_cvt_pk_bf16_f32 v97, v66, v67\n\t"
      "v_mfma_f32_16x16x32_bf16 v[80:83], v[152:155], v[120:123], v[80:83]\n\t"
      "v_exp_f32 v72, v72\n\t"
      "ds_read_b128 v[152:155], v230 offset:16384\n\t"
      "ds_read_b64_tr_b16 v[190:191], v237 offset:4608\n\t"
      "v_mfma_f32_16x16x32_bf16 v[32:35], v[192:195], v[104:107], v[32:35]\n\t"
      "ds_read_b64_tr_b16 v[192:193], v236 offset:1024\n\t"
      "v_mfma_f32_16x16x32_bf16 v[88:91], v[168:171], v[120:123], v[88:91]\n\t"
      "v_exp_f32 v73, v73\n\t"
      "ds_read_b128 v[168:171], v230 offset:20480\n\t"
      "ds_read_b64_tr_b16 v[194:195], v236 offset:5120\n\t"
      "v_mfma_f32_16x16x32_bf16 v[40:43], v[196:199], v[104:107], v[40:43]\n\t"
      "ds_read_b64_tr_b16 v[196:197], v237 offset:1024\n\t"
      "v_cvt_pk_bf16_f32 v98, v72, v73\n\t"
      "v_mfma_f32_16x16x32_bf16 v[80:83], v[156:159], v[124:127], v[80:83]\n\t"
      "s_waitcnt lgkmcnt(15)\n\t"
      "v_exp_f32 v74, v74\n\t"
      "ds_read_b128 v[156:159], v231 offset:16384\n\t"
      "ds_read_b64_tr_b16 v[198:199], v237 offset:5120\n\t"
      "v_mfma_f32_16x16x32_bf16 v[48:51], v[200:203], v[104:107], v[48:51]\n\t"
      "ds_read_b64_tr_b16 v[200:201], v236 offset:1536\n\t"
      "v_mfma_f32_16x16x32_bf16 v[88:91], v[172:175], v[124:127], v[88:91]\n\t"
      "v_exp_f32 v75, v75\n\t"
      "ds_read_b128 v[172:175], v231 offset:20480\n\t"
      "ds_read_b64_tr_b16 v[202:203], v236 offset:5632\n\t"
      "v_mfma_f32_16x16x32_bf16 v[56:59], v[204:207], v[104:107], v[56:59]\n\t"
      "ds_read_b64_tr_b16 v[204:205], v237 offset:1536\n\t"
      "v_cvt_pk_bf16_f32 v99, v74, v75\n\t"
      "s_nop 0\n\t"
      "v_mfma_f32_16x16x32_bf16 v[216:219], v[224:227], v[104:107], v[216:219]\n\t"
      "v_mfma_f32_16x16x32_bf16 v[64:67], v[144:147], v[112:115], v[208:211]\n\t"
      "s_waitcnt lgkmcnt(13)\n\t"
      "v_exp_f32 v80, v80\n\t"
      "ds_read_b128 v[144:147], v228 offset:24576\n\t"
      "ds_read_b64_tr_b16 v[206:207], v237 offset:5632\n\t"
      "v_mfma_f32_16x16x32_bf16 v[0:3], v[176:179], v[96:99], v[0:3]\n\t"
      "ds_read_b64_tr_b16 v[176:177], v236 offset:8192\n\t"
      "v_mfma_f32_16x16x32_bf16 v[72:75], v[160:163], v[112:115], v[208:211]\n\t"
      "v_exp_f32 v81, v81\n\t"
      "ds_read_b128 v[160:163], v228 offset:28672\n\t"
      "ds_read_b64_tr_b16 v[178:179], v236 offset:12288\n\t"
      "v_mfma_f32_16x16x32_bf16 v[8:11], v[180:183], v[96:99], v[8:11]\n\t"
      "ds_read_b64_tr_b16 v[180:181], v237 offset:8192\n\t"
      "v_cvt_pk_bf16_f32 v104, v80, v81\n\t"
      "v_mfma_f32_16x16x32_bf16 v[64:67], v[148:151], v[116:119], v[64:67]\n\t"
      "v_exp_f32 v82, v82\n\t"
      "ds_read_b128 v[148:151], v229 offset:24576\n\t"
      "ds_read_b64_tr_b16 v[182:183], v237 offset:12288\n\t"
      "v_mfma_f32_16x16x32_bf16 v[16:19], v[184:187], v[96:99], v[16:19]\n\t"
      "ds_read_b64_tr_b16 v[184:185], v236 offset:8704\n\t"
      "v_mfma_f32_16x16x32_bf16 v[72:75], v[164:167], v[116:119], v[72:75]\n\t"
      "s_waitcnt lgkmcnt(13)\n\t"
      "v_exp_f32 v83, v83\n\t"
      "ds_read_b128 v[164:167], v229 offset:28672\n\t"
      "ds_read_b64_tr_b16 v[186:187], v236 offset:12800\n\t"
      "v_mfma_f32_16x16x32_bf16 v[24:27], v[188:191], v[96:99], v[24:27]\n\t"
      "ds_read_b64_tr_b16 v[188:189], v237 offset:8704\n\t"
      "v_cvt_pk_bf16_f32 v105, v82, v83\n\t"
      "v_mfma_f32_16x16x32_bf16 v[64:67], v[152:155], v[120:123], v[64:67]\n\t"
      "v_exp_f32 v88, v88\n\t"
      "ds_read_b128 v[152:155], v230 offset:24576\n\t"
      "ds_read_b64_tr_b16 v[190:191], v237 offset:12800\n\t"
      "v_mfma_f32_16x16x32_bf16 v[32:35], v[192:195], v[96:99], v[32:35]\n\t"
      "ds_read_b64_tr_b16 v[192:193], v236 offset:9216\n\t"
      "v_mfma_f32_16x16x32_bf16 v[72:75], v[168:171], v[120:123], v[72:75]\n\t"
      "v_exp_f32 v89, v89\n\t"
      "ds_read_b128 v[168:171], v230 offset:28672\n\t"
      "ds_read_b64_tr_b16 v[194:195], v236 offset:13312\n\t"
      "v_mfma_f32_16x16x32_bf16 v[40:43], v[196:199], v[96:99], v[40:43]\n\t"
      "ds_read_b64_tr_b16 v[196:197], v237 offset:9216\n\t"
      "v_cvt_pk_bf16_f32 v106, v88, v89\n\t"
      "v_mfma_f32_16x16x32_bf16 v[64:67], v[156:159], v[124:127], v[64:67]\n\t"
      "s_waitcnt lgkmcnt(15)\n\t"
      "v_exp_f32 v90, v90\n\t"
      "ds_read_b128 v[156:159], v231 offset:24576\n\t"
      "ds_read_b64_tr_b16 v[198:199], v237 offset:13312\n\t"
      "v_mfma_f32_16x16x32_bf16 v[48:51], v[200:203], v[96:99], v[48:51]\n\t"
      "ds_read_b64_tr_b16 v[200:201], v236 offset:9728\n\t"
      "v_mfma_f32_16x16x32_bf16 v[72:75], v[172:175], v[124:127], v[72:75]\n\t"
      "v_exp_f32 v91, v91\n\t"
      "ds_read_b128 v[172:175], v231 offset:28672\n\t"
      "ds_read_b64_tr_b16 v[202:203], v236 offset:13824\n\t"
      "v_mfma_f32_16x16x32_bf16 v[56:59], v[204:207], v[96:99], v[56:59]\n\t"
      "ds_read_b64_tr_b16 v[204:205], v237 offset:9728\n\t"
      "v_cvt_pk_bf16_f32 v107, v90, v91\n\t"
      "s_nop 0\n\t"
      "v_mfma_f32_16x16x32_bf16 v[216:219], v[224:227], v[96:99], v[216:219]\n\t"
      "s_barrier\n\t"
      "v_mfma_f32_16x16x32_bf16 v[80:83], v[144:147], v[112:115], v[208:211]\n\t"
      "s_waitcnt lgkmcnt(13)\n\t"
      "v_exp_f32 v64, v64\n\t"
      "ds_read_b128 v[144:147], v228 offset:32768\n\t"
      "ds_read_b64_tr_b16 v[206:207], v237 offset:13824\n\t"
      "v_mfma_f32_16x16x32_bf16 v[0:3], v[176:179], v[104:107], v[0:3]\n\t"
      "ds_read_b64_tr_b16 v[176:177], v236 offset:16384\n\t"
      "v_mfma_f32_16x16x32_bf16 v[88:91], v[160:163], v[112:115], v[208:211]\n\t"
      "v_exp_f32 v65, v65\n\t"
      "ds_read_b128 v[160:163], v228 offset:36864\n\t"
      "ds_read_b64_tr_b16 v[178:179], v236 offset:20480\n\t"
      "v_mfma_f32_16x16x32_bf16 v[8:11], v[180:183], v[104:107], v[8:11]\n\t"
      "ds_read_b64_tr_b16 v[180:181], v237 offset:16384\n\t"
      "v_cvt_pk_bf16_f32 v96, v64, v65\n\t"
      "v_mfma_f32_16x16x32_bf16 v[80:83], v[148:151], v[116:119], v[80:83]\n\t"
      "v_exp_f32 v66, v66\n\t"
      "ds_read_b128 v[148:151], v229 offset:32768\n\t"
      "ds_read_b64_tr_b16 v[182:183], v237 offset:20480\n\t"
      "v_mfma_f32_16x16x32_bf16 v[16:19], v[184:187], v[104:107], v[16:19]\n\t"
      "ds_read_b64_tr_b16 v[184:185], v236 offset:16896\n\t"
      "v_mfma_f32_16x16x32_bf16 v[88:91], v[164:167], v[116:119], v[88:91]\n\t"
      "s_waitcnt lgkmcnt(13)\n\t"
      "v_exp_f32 v67, v67\n\t"
      "ds_read_b128 v[164:167], v229 offset:36864\n\t"
      "ds_read_b64_tr_b16 v[186:187], v236 offset:20992\n\t"
      "v_mfma_f32_16x16x32_bf16 v[24:27], v[188:191], v[104:107], v[24:27]\n\t"
      "ds_read_b64_tr_b16 v[188:189], v237 offset:16896\n\t"
      "v_cvt_pk_bf16_f32 v97, v66, v67\n\t"
      "v_mfma_f32_16x16x32_bf16 v[80:83], v[152:155], v[120:123], v[80:83]\n\t"
      "v_exp_f32 v72, v72\n\t"
      "ds_read_b128 v[152:155], v230 offset:32768\n\t"
      "ds_read_b64_tr_b16 v[190:191], v237 offset:20992\n\t"
      "v_mfma_f32_16x16x32_bf16 v[32:35], v[192:195], v[104:107], v[32:35]\n\t"
      "ds_read_b64_tr_b16 v[192:193], v236 offset:17408\n\t"
      "v_mfma_f32_16x16x32_bf16 v[88:91], v[168:171], v[120:123], v[88:91]\n\t"
      "v_exp_f32 v73, v73\n\t"
      "ds_read_b128 v[168:171], v230 offset:36864\n\t"
      "ds_read_b64_tr_b16 v[194:195], v236 offset:21504\n\t"
      "v_mfma_f32_16x16x32_bf16 v[40:43], v[196:199], v[104:107], v[40:43]\n\t"
      "ds_read_b64_tr_b16 v[196:197], v237 offset:17408\n\t"
      "v_cvt_pk_bf16_f32 v98, v72, v73\n\t"
      "v_mfma_f32_16x16x32_bf16 v[80:83], v[156:159], v[124:127], v[80:83]\n\t"
      "s_waitcnt lgkmcnt(15)\n\t"
      "v_exp_f32 v74, v74\n\t"
      "ds_read_b128 v[156:159], v231 offset:32768\n\t"
      "ds_read_b64_tr_b16 v[198:199], v237 offset:21504\n\t"
      "v_mfma_f32_16x16x32_bf16 v[48:51], v[200:203], v[104:107], v[48:51]\n\t"
      "ds_read_b64_tr_b16 v[200:201], v236 offset:17920\n\t"
      "v_mfma_f32_16x16x32_bf16 v[88:91], v[172:175], v[124:127], v[88:91]\n\t"
      "v_exp_f32 v75, v75\n\t"
      "ds_read_b128 v[172:175], v231 offset:36864\n\t"
      "ds_read_b64_tr_b16 v[202:203], v236 offset:22016\n\t"
      "v_mfma_f32_16x16x32_bf16 v[56:59], v[204:207], v[104:107], v[56:59]\n\t"
      "ds_read_b64_tr_b16 v[204:205], v237 offset:17920\n\t"
      "v_cvt_pk_bf16_f32 v99, v74, v75\n\t"
      "s_nop 0\n\t"
      "v_mfma_f32_16x16x32_bf16 v[216:219], v[224:227], v[104:107], v[216:219]\n\t"
      "v_mfma_f32_16x16x32_bf16 v[64:67], v[144:147], v[112:115], v[208:211]\n\t"
      "s_waitcnt lgkmcnt(13)\n\t"
      "v_exp_f32 v80, v80\n\t"
      "ds_read_b128 v[144:147], v228 offset:40960\n\t"
      "ds_read_b64_tr_b16 v[206:207], v237 offset:22016\n\t"
      "v_mfma_f32_16x16x32_bf16 v[0:3], v[176:179], v[96:99], v[0:3]\n\t"
      "ds_read_b64_tr_b16 v[176:177], v236 offset:24576\n\t"
      "v_mfma_f32_16x16x32_bf16 v[72:75], v[160:163], v[112:115], v[208:211]\n\t"
      "v_exp_f32 v81, v81\n\t"
      "ds_read_b128 v[160:163], v228 offset:45056\n\t"
      "ds_read_b64_tr_b16 v[178:179], v236 offset:28672\n\t"
      "v_mfma_f32_16x16x32_bf16 v[8:11], v[180:183], v[96:99], v[8:11]\n\t"
      "ds_read_b64_tr_b16 v[180:181], v237 offset:24576\n\t"
      "v_cvt_pk_bf16_f32 v104, v80, v81\n\t"
      "v_mfma_f32_16x16x32_bf16 v[64:67], v[148:151], v[116:119], v[64:67]\n\t"
      "v_exp_f32 v82, v82\n\t"
      "ds_read_b128 v[148:151], v229 offset:40960\n\t"
      "ds_read_b64_tr_b16 v[182:183], v237 offset:28672\n\t"
      "v_mfma_f32_16x16x32_bf16 v[16:19], v[184:187], v[96:99], v[16:19]\n\t"
      "ds_read_b64_tr_b16 v[184:185], v236 offset:25088\n\t"
      "v_mfma_f32_16x16x32_bf16 v[72:75], v[164:167], v[116:119], v[72:75]\n\t"
      "s_waitcnt lgkmcnt(13)\n\t"
      "v_exp_f32 v83, v83\n\t"
      "ds_read_b128 v[164:167], v229 offset:45056\n\t"
      "ds_read_b64_tr_b16 v[186:187], v236 offset:29184\n\t"
      "v_mfma_f32_16x16x32_bf16 v[24:27], v[188:191], v[96:99], v[24:27]\n\t"
      "ds_read_b64_tr_b16 v[188:189], v237 offset:25088\n\t"
      "v_cvt_pk_bf16_f32 v105, v82, v83\n\t"
      "v_mfma_f32_16x16x32_bf16 v[64:67], v[152:155], v[120:123], v[64:67]\n\t"
      "v_exp_f32 v88, v88\n\t"
      "ds_read_b128 v[152:155], v230 offset:40960\n\t"
      "ds_read_b64_tr_b16 v[190:191], v237 offset:29184\n\t"
      "v_mfma_f32_16x16x32_bf16 v[32:35], v[192:195], v[96:99], v[32:35]\n\t"
      "ds_read_b64_tr_b16 v[192:193], v236 offset:25600\n\t"
      "v_mfma_f32_16x16x32_bf16 v[72:75], v[168:171], v[120:123], v[72:75]\n\t"
      "v_exp_f32 v89, v89\n\t"
      "ds_read_b128 v[168:171], v230 offset:45056\n\t"
      "ds_read_b64_tr_b16 v[194:195], v236 offset:29696\n\t"
      "v_mfma_f32_16x16x32_bf16 v[40:43], v[196:199], v[96:99], v[40:43]\n\t"
      "ds_read_b64_tr_b16 v[196:197], v237 offset:25600\n\t"
      "v_cvt_pk_bf16_f32 v106, v88, v89\n\t"
      "v_mfma_f32_16x16x32_bf16 v[64:67], v[156:159], v[124:127], v[64:67]\n\t"
      "s_waitcnt lgkmcnt(15)\n\t"
      "v_exp_f32 v90, v90\n\t"
      "ds_read_b128 v[156:159], v231 offset:40960\n\t"
      "ds_read_b64_tr_b16 v[198:199], v237 offset:29696\n\t"
      "v_mfma_f32_16x16x32_bf16 v[48:51], v[200:203], v[96:99], v[48:51]\n\t"
      "ds_read_b64_tr_b16 v[200:201], v236 offset:26112\n\t"
      "v_mfma_f32_16x16x32_bf16 v[72:75], v[172:175], v[124:127], v[72:75]\n\t"
      "v_exp_f32 v91, v91\n\t"
      "ds_read_b128 v[172:175], v231 offset:45056\n\t"
      "ds_read_b64_tr_b16 v[202:203], v236 offset:30208\n\t"
      "v_mfma_f32_16x16x32_bf16 v[56:59], v[204:207], v[96:99], v[56:59]\n\t"
      "ds_read_b64_tr_b16 v[204:205], v237 offset:26112\n\t"
      "v_cvt_pk_bf16_f32 v107, v90, v91\n\t"
      "s_nop 0\n\t"
      "v_mfma_f32_16x16x32_bf16 v[216:219], v[224:227], v[96:99], v[216:219]\n\t"
      "s_barrier\n\t"
      "v_mfma_f32_16x16x32_bf16 v[80:83], v[144:147], v[112:115], v[208:211]\n\t"
      "s_waitcnt lgkmcnt(13)\n\t"
      "v_exp_f32 v64, v64\n\t"
      "ds_read_b128 v[144:147], v228 offset:49152\n\t"
      "ds_read_b64_tr_b16 v[206:207], v237 offset:30208\n\t"
      "v_mfma_f32_16x16x32_bf16 v[0:3], v[176:179], v[104:107], v[0:3]\n\t"
      "ds_read_b64_tr_b16 v[176:177], v236 offset:32768\n\t"
      "v_mfma_f32_16x16x32_bf16 v[88:91], v[160:163], v[112:115], v[208:211]\n\t"
      "v_exp_f32 v65, v65\n\t"
      "ds_read_b128 v[160:163], v228 offset:53248\n\t"
      "ds_read_b64_tr_b16 v[178:179], v236 offset:36864\n\t"
      "v_mfma_f32_16x16x32_bf16 v[8:11], v[180:183], v[104:107], v[8:11]\n\t"
      "ds_read_b64_tr_b16 v[180:181], v237 offset:32768\n\t"
      "v_cvt_pk_bf16_f32 v96, v64, v65\n\t"
      "v_mfma_f32_16x16x32_bf16 v[80:83], v[148:151], v[116:119], v[80:83]\n\t"
      "v_exp_f32 v66, v66\n\t"
      "ds_read_b128 v[148:151], v229 offset:49152\n\t"
      "ds_read_b64_tr_b16 v[182:183], v237 offset:36864\n\t"
      "v_mfma_f32_16x16x32_bf16 v[16:19], v[184:187], v[104:107], v[16:19]\n\t"
      "ds_read_b64_tr_b16 v[184:185], v236 offset:33280\n\t"
      "v_mfma_f32_16x16x32_bf16 v[88:91], v[164:167], v[116:119], v[88:91]\n\t"
      "s_waitcnt lgkmcnt(13)\n\t"
      "v_exp_f32 v67, v67\n\t"
      "ds_read_b128 v[164:167], v229 offset:53248\n\t"
      "ds_read_b64_tr_b16 v[186:187], v236 offset:37376\n\t"
      "v_mfma_f32_16x16x32_bf16 v[24:27], v[188:191], v[104:107], v[24:27]\n\t"
      "ds_read_b64_tr_b16 v[188:189], v237 offset:33280\n\t"
      "v_cvt_pk_bf16_f32 v97, v66, v67\n\t"
      "v_mfma_f32_16x16x32_bf16 v[80:83], v[152:155], v[120:123], v[80:83]\n\t"
      "v_exp_f32 v72, v72\n\t"
      "ds_read_b128 v[152:155], v230 offset:49152\n\t"
      "ds_read_b64_tr_b16 v[190:191], v237 offset:37376\n\t"
      "v_mfma_f32_16x16x32_bf16 v[32:35], v[192:195], v[104:107], v[32:35]\n\t"
      "ds_read_b64_tr_b16 v[192:193], v236 offset:33792\n\t"
      "v_mfma_f32_16x16x32_bf16 v[88:91], v[168:171], v[120:123], v[88:91]\n\t"
      "v_exp_f32 v73, v73\n\t"
      "ds_read_b128 v[168:171], v230 offset:53248\n\t"
      "ds_read_b64_tr_b16 v[194:195], v236 offset:37888\n\t"
      "v_mfma_f32_16x16x32_bf16 v[40:43], v[196:199], v[104:107], v[40:43]\n\t"
      "ds_read_b64_tr_b16 v[196:197], v237 offset:33792\n\t"
      "v_cvt_pk_bf16_f32 v98, v72, v73\n\t"
      "v_mfma_f32_16x16x32_bf16 v[80:83], v[156:159], v[124:127], v[80:83]\n\t"
      "s_waitcnt lgkmcnt(15)\n\t"
      "v_exp_f32 v74, v74\n\t"
      "ds_read_b128 v[156:159], v231 offset:49152\n\t"
      "ds_read_b64_tr_b16 v[198:199], v237 offset:37888\n\t"
      "v_mfma_f32_16x16x32_bf16 v[48:51], v[200:203], v[104:107], v[48:51]\n\t"
      "ds_read_b64_tr_b16 v[200:201], v236 offset:34304\n\t"
      "v_mfma_f32_16x16x32_bf16 v[88:91], v[172:175], v[124:127], v[88:91]\n\t"
      "v_exp_f32 v75, v75\n\t"
      "ds_read_b128 v[172:175], v231 offset:53248\n\t"
      "ds_read_b64_tr_b16 v[202:203], v236 offset:38400\n\t"
      "v_mfma_f32_16x16x32_bf16 v[56:59], v[204:207], v[104:107], v[56:59]\n\t"
      "ds_read_b64_tr_b16 v[204:205], v237 offset:34304\n\t"
      "v_cvt_pk_bf16_f32 v99, v74, v75\n\t"
      "s_nop 0\n\t"
      "v_mfma_f32_16x16x32_bf16 v[216:219], v[224:227], v[104:107], v[216:219]\n\t"
      "v_mfma_f32_16x16x32_bf16 v[64:67], v[144:147], v[112:115], v[208:211]\n\t"
      "s_waitcnt lgkmcnt(13)\n\t"
      "v_exp_f32 v80, v80\n\t"
      "ds_read_b128 v[144:147], v228 offset:57344\n\t"
      "ds_read_b64_tr_b16 v[206:207], v237 offset:38400\n\t"
      "v_mfma_f32_16x16x32_bf16 v[0:3], v[176:179], v[96:99], v[0:3]\n\t"
      "ds_read_b64_tr_b16 v[176:177], v236 offset:40960\n\t"
      "v_mfma_f32_16x16x32_bf16 v[72:75], v[160:163], v[112:115], v[208:211]\n\t"
      "v_exp_f32 v81, v81\n\t"
      "ds_read_b128 v[160:163], v228 offset:61440\n\t"
      "ds_read_b64_tr_b16 v[178:179], v236 offset:45056\n\t"
      "v_mfma_f32_16x16x32_bf16 v[8:11], v[180:183], v[96:99], v[8:11]\n\t"
      "ds_read_b64_tr_b16 v[180:181], v237 offset:40960\n\t"
      "v_cvt_pk_bf16_f32 v104, v80, v81\n\t"
      "v_mfma_f32_16x16x32_bf16 v[64:67], v[148:151], v[116:119], v[64:67]\n\t"
      "v_exp_f32 v82, v82\n\t"
      "ds_read_b128 v[148:151], v229 offset:57344\n\t"
      "ds_read_b64_tr_b16 v[182:183], v237 offset:45056\n\t"
      "v_mfma_f32_16x16x32_bf16 v[16:19], v[184:187], v[96:99], v[16:19]\n\t"
      "ds_read_b64_tr_b16 v[184:185], v236 offset:41472\n\t"
      "v_mfma_f32_16x16x32_bf16 v[72:75], v[164:167], v[116:119], v[72:75]\n\t"
      "s_waitcnt lgkmcnt(13)\n\t"
      "v_exp_f32 v83, v83\n\t"
      "ds_read_b128 v[164:167], v229 offset:61440\n\t"
      "ds_read_b64_tr_b16 v[186:187], v236 offset:45568\n\t"
      "v_mfma_f32_16x16x32_bf16 v[24:27], v[188:191], v[96:99], v[24:27]\n\t"
      "ds_read_b64_tr_b16 v[188:189], v237 offset:41472\n\t"
      "v_cvt_pk_bf16_f32 v105, v82, v83\n\t"
      "v_mfma_f32_16x16x32_bf16 v[64:67], v[152:155], v[120:123], v[64:67]\n\t"
      "v_exp_f32 v88, v88\n\t"
      "ds_read_b128 v[152:155], v230 offset:57344\n\t"
      "ds_read_b64_tr_b16 v[190:191], v237 offset:45568\n\t"
      "v_mfma_f32_16x16x32_bf16 v[32:35], v[192:195], v[96:99], v[32:35]\n\t"
      "ds_read_b64_tr_b16 v[192:193], v236 offset:41984\n\t"
      "v_mfma_f32_16x16x32_bf16 v[72:75], v[168:171], v[120:123], v[72:75]\n\t"
      "v_exp_f32 v89, v89\n\t"
      "ds_read_b128 v[168:171], v230 offset:61440\n\t"
      "ds_read_b64_tr_b16 v[194:195], v236 offset:46080\n\t"
      "v_mfma_f32_16x16x32_bf16 v[40:43], v[196:199], v[96:99], v[40:43]\n\t"
      "ds_read_b64_tr_b16 v[196:197], v237 offset:41984\n\t"
      "v_cvt_pk_bf16_f32 v106, v88, v89\n\t"
      "v_mfma_f32_16x16x32_bf16 v[64:67], v[156:159], v[124:127], v[64:67]\n\t"
      "s_waitcnt lgkmcnt(15)\n\t"
      "v_exp_f32 v90, v90\n\t"
      "ds_read_b128 v[156:159], v231 offset:57344\n\t"
      "ds_read_b64_tr_b16 v[198:199], v237 offset:46080\n\t"
      "v_mfma_f32_16x16x32_bf16 v[48:51], v[200:203], v[96:99], v[48:51]\n\t"
      "ds_read_b64_tr_b16 v[200:201], v236 offset:42496\n\t"
      "v_mfma_f32_16x16x32_bf16 v[72:75], v[172:175], v[124:127], v[72:75]\n\t"
      "v_exp_f32 v91, v91\n\t"
      "ds_read_b128 v[172:175], v231 offset:61440\n\t"
      "ds_read_b64_tr_b16 v[202:203], v236 offset:46592\n\t"
      "v_mfma_f32_16x16x32_bf16 v[56:59], v[204:207], v[96:99], v[56:59]\n\t"
      "ds_read_b64_tr_b16 v[204:205], v237 offset:42496\n\t"
      "v_cvt_pk_bf16_f32 v107, v90, v91\n\t"
      "s_nop 0\n\t"
      "v_mfma_f32_16x16x32_bf16 v[216:219], v[224:227], v[96:99], v[216:219]\n\t"
      "s_barrier\n\t"
      "v_mfma_f32_16x16x32_bf16 v[80:83], v[144:147], v[112:115], v[208:211]\n\t"
      "s_waitcnt lgkmcnt(13)\n\t"
      "v_exp_f32 v64, v64\n\t"
      "ds_read_b128 v[144:147], v232 offset:0\n\t"
      "ds_read_b64_tr_b16 v[206:207], v237 offset:46592\n\t"
      "v_mfma_f32_16x16x32_bf16 v[0:3], v[176:179], v[104:107], v[0:3]\n\t"
      "ds_read_b64_tr_b16 v[176:177], v236 offset:49152\n\t"
      "v_mfma_f32_16x16x32_bf16 v[88:91], v[160:163], v[112:115], v[208:211]\n\t"
      "v_exp_f32 v65, v65\n\t"
      "ds_read_b128 v[160:163], v232 offset:4096\n\t"
      "ds_read_b64_tr_b16 v[178:179], v236 offset:53248\n\t"
      "v_mfma_f32_16x16x32_bf16 v[8:11], v[180:183], v[104:107], v[8:11]\n\t"
      "ds_read_b64_tr_b16 v[180:181], v237 offset:49152\n\t"
      "v_cvt_pk_bf16_f32 v96, v64, v65\n\t"
      "v_mfma_f32_16x16x32_bf16 v[80:83], v[148:151], v[116:119], v[80:83]\n\t"
      "v_exp_f32 v66, v66\n\t"
      "ds_read_b128 v[148:151], v233 offset:0\n\t"
      "ds_read_b64_tr_b16 v[182:183], v237 offset:53248\n\t"
      "v_mfma_f32_16x16x32_bf16 v[16:19], v[184:187], v[104:107], v[16:19]\n\t"
      "ds_read_b64_tr_b16 v[184:185], v236 offset:49664\n\t"
      "v_mfma_f32_16x16x32_bf16 v[88:91], v[164:167], v[116:119], v[88:91]\n\t"
      "s_waitcnt lgkmcnt(13)\n\t"
      "v_exp_f32 v67, v67\n\t"
      "ds_read_b128 v[164:167], v233 offset:4096\n\t"
      "ds_read_b64_tr_b16 v[186:187], v236 offset:53760\n\t"
      "v_mfma_f32_16x16x32_bf16 v[24:27], v[188:191], v[104:107], v[24:27]\n\t"
      "ds_read_b64_tr_b16 v[188:189], v237 offset:49664\n\t"
      "v_cvt_pk_bf16_f32 v97, v66, v67\n\t"
      "v_mfma_f32_16x16x32_bf16 v[80:83], v[152:155], v[120:123], v[80:83]\n\t"
      "v_exp_f32 v72, v72\n\t"
      "ds_read_b128 v[152:155], v234 offset:0\n\t"
      "ds_read_b64_tr_b16 v[190:191], v237 offset:53760\n\t"
      "v_mfma_f32_16x16x32_bf16 v[32:35], v[192:195], v[104:107], v[32:35]\n\t"
      "ds_read_b64_tr_b16 v[192:193], v236 offset:50176\n\t"
      "v_mfma_f32_16x16x32_bf16 v[88:91], v[168:171], v[120:123], v[88:91]\n\t"
      "v_exp_f32 v73, v73\n\t"
      "ds_read_b128 v[168:171], v234 offset:4096\n\t"
      "ds_read_b64_tr_b16 v[194:195], v236 offset:54272\n\t"
      "v_mfma_f32_16x16x32_bf16 v[40:43], v[196:199], v[104:107], v[40:43]\n\t"
      "ds_read_b64_tr_b16 v[196:197], v237 offset:50176\n\t"
      "v_cvt_pk_bf16_f32 v98, v72, v73\n\t"
      "v_mfma_f32_16x16x32_bf16 v[80:83], v[156:159], v[124:127], v[80:83]\n\t"
      "s_waitcnt lgkmcnt(15)\n\t"
      "v_exp_f32 v74, v74\n\t"
      "ds_read_b128 v[156:159], v235 offset:0\n\t"
      "ds_read_b64_tr_b16 v[198:199], v237 offset:54272\n\t"
      "v_mfma_f32_16x16x32_bf16 v[48:51], v[200:203], v[104:107], v[48:51]\n\t"
      "ds_read_b64_tr_b16 v[200:201], v236 offset:50688\n\t"
      "v_mfma_f32_16x16x32_bf16 v[88:91], v[172:175], v[124:127], v[88:91]\n\t"
      "v_exp_f32 v75, v75\n\t"
      "ds_read_b128 v[172:175], v235 offset:4096\n\t"
      "ds_read_b64_tr_b16 v[202:203], v236 offset:54784\n\t"
      "v_mfma_f32_16x16x32_bf16 v[56:59], v[204:207], v[104:107], v[56:59]\n\t"
      "ds_read_b64_tr_b16 v[204:205], v237 offset:50688\n\t"
      "v_cvt_pk_bf16_f32 v99, v74, v75\n\t"
      "s_nop 0\n\t"
      "v_mfma_f32_16x16x32_bf16 v[216:219], v[224:227], v[104:107], v[216:219]\n\t"
      "v_mfma_f32_16x16x32_bf16 v[64:67], v[144:147], v[112:115], v[208:211]\n\t"
      "s_waitcnt lgkmcnt(13)\n\t"
      "v_exp_f32 v80, v80\n\t"
      "ds_read_b128 v[144:147], v232 offset:8192\n\t"
      "ds_read_b64_tr_b16 v[206:207], v237 offset:54784\n\t"
      "v_mfma_f32_16x16x32_bf16 v[0:3], v[176:179], v[96:99], v[0:3]\n\t"
      "ds_read_b64_tr_b16 v[176:177], v236 offset:57344\n\t"
      "v_mfma_f32_16x16x32_bf16 v[72:75], v[160:163], v[112:115], v[208:211]\n\t"
      "v_exp_f32 v81, v81\n\t"
      "ds_read_b128 v[160:163], v232 offset:12288\n\t"
      "ds_read_b64_tr_b16 v[178:179], v236 offset:61440\n\t"
      "v_mfma_f32_16x16x32_bf16 v[8:11], v[180:183], v[96:99], v[8:11]\n\t"
      "ds_read_b64_tr_b16 v[180:181], v237 offset:57344\n\t"
      "v_cvt_pk_bf16_f32 v104, v80, v81\n\t"
      "v_mfma_f32_16x16x32_bf16 v[64:67], v[148:151], v[116:119], v[64:67]\n\t"
      "v_exp_f32 v82, v82\n\t"
      "ds_read_b128 v[148:151], v233 offset:8192\n\t"
      "ds_read_b64_tr_b16 v[182:183], v237 offset:61440\n\t"
      "v_mfma_f32_16x16x32_bf16 v[16:19], v[184:187], v[96:99], v[16:19]\n\t"
      "ds_read_b64_tr_b16 v[184:185], v236 offset:57856\n\t"
      "v_mfma_f32_16x16x32_bf16 v[72:75], v[164:167], v[116:119], v[72:75]\n\t"
      "s_waitcnt lgkmcnt(13)\n\t"
      "v_exp_f32 v83, v83\n\t"
      "ds_read_b128 v[164:167], v233 offset:12288\n\t"
      "ds_read_b64_tr_b16 v[186:187], v236 offset:61952\n\t"
      "v_mfma_f32_16x16x32_bf16 v[24:27], v[188:191], v[96:99], v[24:27]\n\t"
      "ds_read_b64_tr_b16 v[188:189], v237 offset:57856\n\t"
      "v_cvt_pk_bf16_f32 v105, v82, v83\n\t"
      "v_mfma_f32_16x16x32_bf16 v[64:67], v[152:155], v[120:123], v[64:67]\n\t"
      "v_exp_f32 v88, v88\n\t"
      "ds_read_b128 v[152:155], v234 offset:8192\n\t"
      "ds_read_b64_tr_b16 v[190:191], v237 offset:61952\n\t"
      "v_mfma_f32_16x16x32_bf16 v[32:35], v[192:195], v[96:99], v[32:35]\n\t"
      "ds_read_b64_tr_b16 v[192:193], v236 offset:58368\n\t"
      "v_mfma_f32_16x16x32_bf16 v[72:75], v[168:171], v[120:123], v[72:75]\n\t"
      "v_exp_f32 v89, v89\n\t"
      "ds_read_b128 v[168:171], v234 offset:12288\n\t"
      "ds_read_b64_tr_b16 v[194:195], v236 offset:62464\n\t"
      "v_mfma_f32_16x16x32_bf16 v[40:43], v[196:199], v[96:99], v[40:43]\n\t"
      "ds_read_b64_tr_b16 v[196:197], v237 offset:58368\n\t"
      "v_cvt_pk_bf16_f32 v106, v88, v89\n\t"
      "v_mfma_f32_16x16x32_bf16 v[64:67], v[156:159], v[124:127], v[64:67]\n\t"
      "s_waitcnt lgkmcnt(15)\n\t"
      "v_exp_f32 v90, v90\n\t"
      "ds_read_b128 v[156:159], v235 offset:8192\n\t"
      "ds_read_b64_tr_b16 v[198:199], v237 offset:62464\n\t"
      "v_mfma_f32_16x16x32_bf16 v[48:51], v[200:203], v[96:99], v[48:51]\n\t"
      "ds_read_b64_tr_b16 v[200:201], v236 offset:58880\n\t"
      "v_mfma_f32_16x16x32_bf16 v[72:75], v[172:175], v[124:127], v[72:75]\n\t"
      "v_exp_f32 v91, v91\n\t"
      "ds_read_b128 v[172:175], v235 offset:12288\n\t"
      "ds_read_b64_tr_b16 v[202:203], v236 offset:62976\n\t"
      "v_mfma_f32_16x16x32_bf16 v[56:59], v[204:207], v[96:99], v[56:59]\n\t"
      "ds_read_b64_tr_b16 v[204:205], v237 offset:58880\n\t"
      "v_cvt_pk_bf16_f32 v107, v90, v91\n\t"
      "s_nop 0\n\t"
      "v_mfma_f32_16x16x32_bf16 v[216:219], v[224:227], v[96:99], v[216:219]\n\t"
      "s_barrier\n\t"
      "v_mfma_f32_16x16x32_bf16 v[80:83], v[144:147], v[112:115], v[208:211]\n\t"
      "s_waitcnt lgkmcnt(13)\n\t"
      "v_exp_f32 v64, v64\n\t"
      "ds_read_b128 v[144:147], v228 offset:0\n\t"
      "ds_read_b64_tr_b16 v[206:207], v237 offset:62976\n\t"
      "v_mfma_f32_16x16x32_bf16 v[0:3], v[176:179], v[104:107], v[0:3]\n\t"
      "ds_read_b64_tr_b16 v[176:177], v238 offset:0\n\t"
      "v_mfma_f32_16x16x32_bf16 v[88:91], v[160:163], v[112:115], v[208:211]\n\t"
      "v_exp_f32 v65, v65\n\t"
      "ds_read_b128 v[160:163], v228 offset:4096\n\t"
      "ds_read_b64_tr_b16 v[178:179], v238 offset:4096\n\t"
      "v_mfma_f32_16x16x32_bf16 v[8:11], v[180:183], v[104:107], v[8:11]\n\t"
      "ds_read_b64_tr_b16 v[180:181], v239 offset:0\n\t"
      "v_cvt_pk_bf16_f32 v96, v64, v65\n\t"
      "v_mfma_f32_16x16x32_bf16 v[80:83], v[148:151], v[116:119], v[80:83]\n\t"
      "v_exp_f32 v66, v66\n\t"
      "ds_read_b128 v[148:151], v229 offset:0\n\t"
      "ds_read_b64_tr_b16 v[182:183], v239 offset:4096\n\t"
      "v_mfma_f32_16x16x32_bf16 v[16:19], v[184:187], v[104:107], v[16:19]\n\t"
      "ds_read_b64_tr_b16 v[184:185], v238 offset:512\n\t"
      "v_mfma_f32_16x16x32_bf16 v[88:91], v[164:167], v[116:119], v[88:91]\n\t"
      "s_waitcnt lgkmcnt(13)\n\t"
      "v_exp_f32 v67, v67\n\t"
      "ds_read_b128 v[164:167], v229 offset:4096\n\t"
      "ds_read_b64_tr_b16 v[186:187], v238 offset:4608\n\t"
      "v_mfma_f32_16x16x32_bf16 v[24:27], v[188:191], v[104:107], v[24:27]\n\t"
      "ds_read_b64_tr_b16 v[188:189], v239 offset:512\n\t"
      "v_cvt_pk_bf16_f32 v97, v66, v67\n\t"
      "v_mfma_f32_16x16x32_bf16 v[80:83], v[152:155], v[120:123], v[80:83]\n\t"
      "v_exp_f32 v72, v72\n\t"
      "ds_read_b128 v[152:155], v230 offset:0\n\t"
      "ds_read_b64_tr_b16 v[190:191], v239 offset:4608\n\t"
      "v_mfma_f32_16x16x32_bf16 v[32:35], v[192:195], v[104:107], v[32:35]\n\t"
      "ds_read_b64_tr_b16 v[192:193], v238 offset:1024\n\t"
      "v_mfma_f32_16x16x32_bf16 v[88:91], v[168:171], v[120:123], v[88:91]\n\t"
      "v_exp_f32 v73, v73\n\t"
      "ds_read_b128 v[168:171], v230 offset:4096\n\t"
      "ds_read_b64_tr_b16 v[194:195], v238 offset:5120\n\t"
      "v_mfma_f32_16x16x32_bf16 v[40:43], v[196:199], v[104:107], v[40:43]\n\t"
      "ds_read_b64_tr_b16 v[196:197], v239 offset:1024\n\t"
      "v_cvt_pk_bf16_f32 v98, v72, v73\n\t"
      "v_mfma_f32_16x16x32_bf16 v[80:83], v[156:159], v[124:127], v[80:83]\n\t"
      "s_waitcnt lgkmcnt(15)\n\t"
      "v_exp_f32 v74, v74\n\t"
      "ds_read_b128 v[156:159], v231 offset:0\n\t"
      "ds_read_b64_tr_b16 v[198:199], v239 offset:5120\n\t"
      "v_mfma_f32_16x16x32_bf16 v[48:51], v[200:203], v[104:107], v[48:51]\n\t"
      "ds_read_b64_tr_b16 v[200:201], v238 offset:1536\n\t"
      "v_mfma_f32_16x16x32_bf16 v[88:91], v[172:175], v[124:127], v[88:91]\n\t"
      "v_exp_f32 v75, v75\n\t"
      "ds_read_b128 v[172:175], v231 offset:4096\n\t"
      "ds_read_b64_tr_b16 v[202:203], v238 offset:5632\n\t"
      "v_mfma_f32_16x16x32_bf16 v[56:59], v[204:207], v[104:107], v[56:59]\n\t"
      "ds_read_b64_tr_b16 v[204:205], v239 offset:1536\n\t"
      "v_cvt_pk_bf16_f32 v99, v74, v75\n\t"
      "s_nop 0\n\t"
      "v_mfma_f32_16x16x32_bf16 v[216:219], v[224:227], v[104:107], v[216:219]\n\t"
      "v_mfma_f32_16x16x32_bf16 v[64:67], v[144:147], v[112:115], v[208:211]\n\t"
      "s_waitcnt lgkmcnt(13)\n\t"
      "v_exp_f32 v80, v80\n\t"
      "ds_read_b128 v[144:147], v228 offset:8192\n\t"
      "ds_read_b64_tr_b16 v[206:207], v239 offset:5632\n\t"
      "v_mfma_f32_16x16x32_bf16 v[0:3], v[176:179], v[96:99], v[0:3]\n\t"
      "ds_read_b64_tr_b16 v[176:177], v238 offset:8192\n\t"
      "v_mfma_f32_16x16x32_bf16 v[72:75], v[160:163], v[112:115], v[208:211]\n\t"
      "v_exp_f32 v81, v81\n\t"
      "ds_read_b128 v[160:163], v228 offset:12288\n\t"
      "ds_read_b64_tr_b16 v[178:179], v238 offset:12288\n\t"
      "v_mfma_f32_16x16x32_bf16 v[8:11], v[180:183], v[96:99], v[8:11]\n\t"
      "ds_read_b64_tr_b16 v[180:181], v239 offset:8192\n\t"
      "v_cvt_pk_bf16_f32 v104, v80, v81\n\t"
      "v_mfma_f32_16x16x32_bf16 v[64:67], v[148:151], v[116:119], v[64:67]\n\t"
      "v_exp_f32 v82, v82\n\t"
      "ds_read_b128 v[148:151], v229 offset:8192\n\t"
      "ds_read_b64_tr_b16 v[182:183], v239 offset:12288\n\t"
      "v_mfma_f32_16x16x32_bf16 v[16:19], v[184:187], v[96:99], v[16:19]\n\t"
      "ds_read_b64_tr_b16 v[184:185], v238 offset:8704\n\t"
      "v_mfma_f32_16x16x32_bf16 v[72:75], v[164:167], v[116:119], v[72:75]\n\t"
      "s_waitcnt lgkmcnt(13)\n\t"
      "v_exp_f32 v83, v83\n\t"
      "ds_read_b128 v[164:167], v229 offset:12288\n\t"
      "ds_read_b64_tr_b16 v[186:187], v238 offset:12800\n\t"
      "v_mfma_f32_16x16x32_bf16 v[24:27], v[188:191], v[96:99], v[24:27]\n\t"
      "ds_read_b64_tr_b16 v[188:189], v239 offset:8704\n\t"
      "v_cvt_pk_bf16_f32 v105, v82, v83\n\t"
      "v_mfma_f32_16x16x32_bf16 v[64:67], v[152:155], v[120:123], v[64:67]\n\t"
      "v_exp_f32 v88, v88\n\t"
      "ds_read_b128 v[152:155], v230 offset:8192\n\t"
      "ds_read_b64_tr_b16 v[190:191], v239 offset:12800\n\t"
      "v_mfma_f32_16x16x32_bf16 v[32:35], v[192:195], v[96:99], v[32:35]\n\t"
      "ds_read_b64_tr_b16 v[192:193], v238 offset:9216\n\t"
      "v_mfma_f32_16x16x32_bf16 v[72:75], v[168:171], v[120:123], v[72:75]\n\t"
      "v_exp_f32 v89, v89\n\t"
      "ds_read_b128 v[168:171], v230 offset:12288\n\t"
      "ds_read_b64_tr_b16 v[194:195], v238 offset:13312\n\t"
      "v_mfma_f32_16x16x32_bf16 v[40:43], v[196:199], v[96:99], v[40:43]\n\t"
      "ds_read_b64_tr_b16 v[196:197], v239 offset:9216\n\t"
      "v_cvt_pk_bf16_f32 v106, v88, v89\n\t"
      "v_mfma_f32_16x16x32_bf16 v[64:67], v[156:159], v[124:127], v[64:67]\n\t"
      "s_waitcnt lgkmcnt(15)\n\t"
      "v_exp_f32 v90, v90\n\t"
      "ds_read_b128 v[156:159], v231 offset:8192\n\t"
      "ds_read_b64_tr_b16 v[198:199], v239 offset:13312\n\t"
      "v_mfma_f32_16x16x32_bf16 v[48:51], v[200:203], v[96:99], v[48:51]\n\t"
      "ds_read_b64_tr_b16 v[200:201], v238 offset:9728\n\t"
      "v_mfma_f32_16x16x32_bf16 v[72:75], v[172:175], v[124:127], v[72:75]\n\t"
      "v_exp_f32 v91, v91\n\t"
      "ds_read_b128 v[172:175], v231 offset:12288\n\t"
      "ds_read_b64_tr_b16 v[202:203], v238 offset:13824\n\t"
      "v_mfma_f32_16x16x32_bf16 v[56:59], v[204:207], v[96:99], v[56:59]\n\t"
      "ds_read_b64_tr_b16 v[204:205], v239 offset:9728\n\t"
      "v_cvt_pk_bf16_f32 v107, v90, v91\n\t"
      "s_nop 0\n\t"
      "v_mfma_f32_16x16x32_bf16 v[216:219], v[224:227], v[96:99], v[216:219]\n\t"
      "s_barrier\n\t"
      "v_mfma_f32_16x16x32_bf16 v[80:83], v[144:147], v[112:115], v[208:211]\n\t"
      "s_waitcnt lgkmcnt(13)\n\t"
      "v_exp_f32 v64, v64\n\t"
      "ds_read_b128 v[144:147], v228 offset:16384\n\t"
      "ds_read_b64_tr_b16 v[206:207], v239 offset:13824\n\t"
      "v_mfma_f32_16x16x32_bf16 v[0:3], v[176:179], v[104:107], v[0:3]\n\t"
      "ds_read_b64_tr_b16 v[176:177], v236 offset:0\n\t"
      "v_mfma_f32_16x16x32_bf16 v[88:91], v[160:163], v[112:115], v[208:211]\n\t"
      "v_exp_f32 v65, v65\n\t"
      "ds_read_b128 v[160:163], v228 offset:20480\n\t"
      "ds_read_b64_tr_b16 v[178:179], v236 offset:4096\n\t"
      "v_mfma_f32_16x16x32_bf16 v[8:11], v[180:183], v[104:107], v[8:11]\n\t"
      "ds_read_b64_tr_b16 v[180:181], v237 offset:0\n\t"
      "v_cvt_pk_bf16_f32 v96, v64, v65\n\t"
      "v_mfma_f32_16x16x32_bf16 v[80:83], v[148:151], v[116:119], v[80:83]\n\t"
      "v_exp_f32 v66, v66\n\t"
      "ds_read_b128 v[148:151], v229 offset:16384\n\t"
      "ds_read_b64_tr_b16 v[182:183], v237 offset:4096\n\t"
      "v_mfma_f32_16x16x32_bf16 v[16:19], v[184:187], v[104:107], v[16:19]\n\t"
      "ds_read_b64_tr_b16 v[184:185], v236 offset:512\n\t"
      "v_mfma_f32_16x16x32_bf16 v[88:91], v[164:167], v[116:119], v[88:91]\n\t"
      "s_waitcnt lgkmcnt(13)\n\t"
      "v_exp_f32 v67, v67\n\t"
      "ds_read_b128 v[164:167], v229 offset:20480\n\t"
      "ds_read_b64_tr_b16 v[186:187], v236 offset:4608\n\t"
      "v_mfma_f32_16x16x32_bf16 v[24:27], v[188:191], v[104:107], v[24:27]\n\t"
      "ds_read_b64_tr_b16 v[188:189], v237 offset:512\n\t"
      "v_cvt_pk_bf16_f32 v97, v66, v67\n\t"
      "v_mfma_f32_16x16x32_bf16 v[80:83], v[152:155], v[120:123], v[80:83]\n\t"
      "v_exp_f32 v72, v72\n\t"
      "ds_read_b128 v[152:155], v230 offset:16384\n\t"
      "ds_read_b64_tr_b16 v[190:191], v237 offset:4608\n\t"
      "v_mfma_f32_16x16x32_bf16 v[32:35], v[192:195], v[104:107], v[32:35]\n\t"
      "ds_read_b64_tr_b16 v[192:193], v236 offset:1024\n\t"
      "v_mfma_f32_16x16x32_bf16 v[88:91], v[168:171], v[120:123], v[88:91]\n\t"
      "v_exp_f32 v73, v73\n\t"
      "ds_read_b128 v[168:171], v230 offset:20480\n\t"
      "ds_read_b64_tr_b16 v[194:195], v236 offset:5120\n\t"
      "v_mfma_f32_16x16x32_bf16 v[40:43], v[196:199], v[104:107], v[40:43]\n\t"
      "ds_read_b64_tr_b16 v[196:197], v237 offset:1024\n\t"
      "v_cvt_pk_bf16_f32 v98, v72, v73\n\t"
      "v_mfma_f32_16x16x32_bf16 v[80:83], v[156:159], v[124:127], v[80:83]\n\t"
      "s_waitcnt lgkmcnt(15)\n\t"
      "v_exp_f32 v74, v74\n\t"
      "ds_read_b128 v[156:159], v231 offset:16384\n\t"
      "ds_read_b64_tr_b16 v[198:199], v237 offset:5120\n\t"
      "v_mfma_f32_16x16x32_bf16 v[48:51], v[200:203], v[104:107], v[48:51]\n\t"
      "ds_read_b64_tr_b16 v[200:201], v236 offset:1536\n\t"
      "v_mfma_f32_16x16x32_bf16 v[88:91], v[172:175], v[124:127], v[88:91]\n\t"
      "v_exp_f32 v75, v75\n\t"
      "ds_read_b128 v[172:175], v231 offset:20480\n\t"
      "ds_read_b64_tr_b16 v[202:203], v236 offset:5632\n\t"
      "v_mfma_f32_16x16x32_bf16 v[56:59], v[204:207], v[104:107], v[56:59]\n\t"
      "ds_read_b64_tr_b16 v[204:205], v237 offset:1536\n\t"
      "v_cvt_pk_bf16_f32 v99, v74, v75\n\t"
      "s_nop 0\n\t"
      "v_mfma_f32_16x16x32_bf16 v[216:219], v[224:227], v[104:107], v[216:219]\n\t"
      "v_mfma_f32_16x16x32_bf16 v[64:67], v[144:147], v[112:115], v[208:211]\n\t"
      "s_waitcnt lgkmcnt(13)\n\t"
      "v_exp_f32 v80, v80\n\t"
      "ds_read_b128 v[144:147], v228 offset:24576\n\t"
      "ds_read_b64_tr_b16 v[206:207], v237 offset:5632\n\t"
      "v_mfma_f32_16x16x32_bf16 v[0:3], v[176:179], v[96:99], v[0:3]\n\t"
      "ds_read_b64_tr_b16 v[176:177], v236 offset:8192\n\t"
      "v_mfma_f32_16x16x32_bf16 v[72:75], v[160:163], v[112:115], v[208:211]\n\t"
      "v_exp_f32 v81, v81\n\t"
      "ds_read_b128 v[160:163], v228 offset:28672\n\t"
      "ds_read_b64_tr_b16 v[178:179], v236 offset:12288\n\t"
      "v_mfma_f32_16x16x32_bf16 v[8:11], v[180:183], v[96:99], v[8:11]\n\t"
      "ds_read_b64_tr_b16 v[180:181], v237 offset:8192\n\t"
      "v_cvt_pk_bf16_f32 v104, v80, v81\n\t"
      "v_mfma_f32_16x16x32_bf16 v[64:67], v[148:151], v[116:119], v[64:67]\n\t"
      "v_exp_f32 v82, v82\n\t"
      "ds_read_b128 v[148:151], v229 offset:24576\n\t"
      "ds_read_b64_tr_b16 v[182:183], v237 offset:12288\n\t"
      "v_mfma_f32_16x16x32_bf16 v[16:19], v[184:187], v[96:99], v[16:19]\n\t"
      "ds_read_b64_tr_b16 v[184:185], v236 offset:8704\n\t"
      "v_mfma_f32_16x16x32_bf16 v[72:75], v[164:167], v[116:119], v[72:75]\n\t"
      "s_waitcnt lgkmcnt(13)\n\t"
      "v_exp_f32 v83, v83\n\t"
      "ds_read_b128 v[164:167], v229 offset:28672\n\t"
      "ds_read_b64_tr_b16 v[186:187], v236 offset:12800\n\t"
      "v_mfma_f32_16x16x32_bf16 v[24:27], v[188:191], v[96:99], v[24:27]\n\t"
      "ds_read_b64_tr_b16 v[188:189], v237 offset:8704\n\t"
      "v_cvt_pk_bf16_f32 v105, v82, v83\n\t"
      "v_mfma_f32_16x16x32_bf16 v[64:67], v[152:155], v[120:123], v[64:67]\n\t"
      "v_exp_f32 v88, v88\n\t"
      "ds_read_b128 v[152:155], v230 offset:24576\n\t"
      "ds_read_b64_tr_b16 v[190:191], v237 offset:12800\n\t"
      "v_mfma_f32_16x16x32_bf16 v[32:35], v[192:195], v[96:99], v[32:35]\n\t"
      "ds_read_b64_tr_b16 v[192:193], v236 offset:9216\n\t"
      "v_mfma_f32_16x16x32_bf16 v[72:75], v[168:171], v[120:123], v[72:75]\n\t"
      "v_exp_f32 v89, v89\n\t"
      "ds_read_b128 v[168:171], v230 offset:28672\n\t"
      "ds_read_b64_tr_b16 v[194:195], v236 offset:13312\n\t"
      "v_mfma_f32_16x16x32_bf16 v[40:43], v[196:199], v[96:99], v[40:43]\n\t"
      "ds_read_b64_tr_b16 v[196:197], v237 offset:9216\n\t"
      "v_cvt_pk_bf16_f32 v106, v88, v89\n\t"
      "v_mfma_f32_16x16x32_bf16 v[64:67], v[156:159], v[124:127], v[64:67]\n\t"
      "s_waitcnt lgkmcnt(15)\n\t"
      "v_exp_f32 v90, v90\n\t"
      "ds_read_b128 v[156:159], v231 offset:24576\n\t"
      "ds_read_b64_tr_b16 v[198:199], v237 offset:13312\n\t"
      "v_mfma_f32_16x16x32_bf16 v[48:51], v[200:203], v[96:99], v[48:51]\n\t"
      "ds_read_b64_tr_b16 v[200:201], v236 offset:9728\n\t"
      "v_mfma_f32_16x16x32_bf16 v[72:75], v[172:175], v[124:127], v[72:75]\n\t"
      "v_exp_f32 v91, v91\n\t"
      "ds_read_b128 v[172:175], v231 offset:28672\n\t"
      "ds_read_b64_tr_b16 v[202:203], v236 offset:13824\n\t"
      "v_mfma_f32_16x16x32_bf16 v[56:59], v[204:207], v[96:99], v[56:59]\n\t"
      "ds_read_b64_tr_b16 v[204:205], v237 offset:9728\n\t"
      "v_cvt_pk_bf16_f32 v107, v90, v91\n\t"
      "s_nop 0\n\t"
      "v_mfma_f32_16x16x32_bf16 v[216:219], v[224:227], v[96:99], v[216:219]\n\t"
      "s_barrier\n\t"
      "v_mfma_f32_16x16x32_bf16 v[80:83], v[144:147], v[112:115], v[208:211]\n\t"
      "s_waitcnt lgkmcnt(13)\n\t"
      "v_exp_f32 v64, v64\n\t"
      "ds_read_b128 v[144:147], v228 offset:32768\n\t"
      "ds_read_b64_tr_b16 v[206:207], v237 offset:13824\n\t"
      "v_mfma_f32_16x16x32_bf16 v[0:3], v[176:179], v[104:107], v[0:3]\n\t"
      "ds_read_b64_tr_b16 v[176:177], v236 offset:16384\n\t"
      "v_mfma_f32_16x16x32_bf16 v[88:91], v[160:163], v[112:115], v[208:211]\n\t"
      "v_exp_f32 v65, v65\n\t"
      "ds_read_b128 v[160:163], v228 offset:36864\n\t"
      "ds_read_b64_tr_b16 v[178:179], v236 offset:20480\n\t"
      "v_mfma_f32_16x16x32_bf16 v[8:11], v[180:183], v[104:107], v[8:11]\n\t"
      "ds_read_b64_tr_b16 v[180:181], v237 offset:16384\n\t"
      "v_cvt_pk_bf16_f32 v96, v64, v65\n\t"
      "v_mfma_f32_16x16x32_bf16 v[80:83], v[148:151], v[116:119], v[80:83]\n\t"
      "v_exp_f32 v66, v66\n\t"
      "ds_read_b128 v[148:151], v229 offset:32768\n\t"
      "ds_read_b64_tr_b16 v[182:183], v237 offset:20480\n\t"
      "v_mfma_f32_16x16x32_bf16 v[16:19], v[184:187], v[104:107], v[16:19]\n\t"
      "ds_read_b64_tr_b16 v[184:185], v236 offset:16896\n\t"
      "v_mfma_f32_16x16x32_bf16 v[88:91], v[164:167], v[116:119], v[88:91]\n\t"
      "s_waitcnt lgkmcnt(13)\n\t"
      "v_exp_f32 v67, v67\n\t"
      "ds_read_b128 v[164:167], v229 offset:36864\n\t"
      "ds_read_b64_tr_b16 v[186:187], v236 offset:20992\n\t"
      "v_mfma_f32_16x16x32_bf16 v[24:27], v[188:191], v[104:107], v[24:27]\n\t"
      "ds_read_b64_tr_b16 v[188:189], v237 offset:16896\n\t"
      "v_cvt_pk_bf16_f32 v97, v66, v67\n\t"
      "v_mfma_f32_16x16x32_bf16 v[80:83], v[152:155], v[120:123], v[80:83]\n\t"
      "v_exp_f32 v72, v72\n\t"
      "ds_read_b128 v[152:155], v230 offset:32768\n\t"
      "ds_read_b64_tr_b16 v[190:191], v237 offset:20992\n\t"
      "v_mfma_f32_16x16x32_bf16 v[32:35], v[192:195], v[104:107], v[32:35]\n\t"
      "ds_read_b64_tr_b16 v[192:193], v236 offset:17408\n\t"
      "v_mfma_f32_16x16x32_bf16 v[88:91], v[168:171], v[120:123], v[88:91]\n\t"
      "v_exp_f32 v73, v73\n\t"
      "ds_read_b128 v[168:171], v230 offset:36864\n\t"
      "ds_read_b64_tr_b16 v[194:195], v236 offset:21504\n\t"
      "v_mfma_f32_16x16x32_bf16 v[40:43], v[196:199], v[104:107], v[40:43]\n\t"
      "ds_read_b64_tr_b16 v[196:197], v237 offset:17408\n\t"
      "v_cvt_pk_bf16_f32 v98, v72, v73\n\t"
      "v_mfma_f32_16x16x32_bf16 v[80:83], v[156:159], v[124:127], v[80:83]\n\t"
      "s_waitcnt lgkmcnt(15)\n\t"
      "v_exp_f32 v74, v74\n\t"
      "ds_read_b128 v[156:159], v231 offset:32768\n\t"
      "ds_read_b64_tr_b16 v[198:199], v237 offset:21504\n\t"
      "v_mfma_f32_16x16x32_bf16 v[48:51], v[200:203], v[104:107], v[48:51]\n\t"
      "ds_read_b64_tr_b16 v[200:201], v236 offset:17920\n\t"
      "v_mfma_f32_16x16x32_bf16 v[88:91], v[172:175], v[124:127], v[88:91]\n\t"
      "v_exp_f32 v75, v75\n\t"
      "ds_read_b128 v[172:175], v231 offset:36864\n\t"
      "ds_read_b64_tr_b16 v[202:203], v236 offset:22016\n\t"
      "v_mfma_f32_16x16x32_bf16 v[56:59], v[204:207], v[104:107], v[56:59]\n\t"
      "ds_read_b64_tr_b16 v[204:205], v237 offset:17920\n\t"
      "v_cvt_pk_bf16_f32 v99, v74, v75\n\t"
      "s_nop 0\n\t"
      "v_mfma_f32_16x16x32_bf16 v[216:219], v[224:227], v[104:107], v[216:219]\n\t"
      "v_mfma_f32_16x16x32_bf16 v[64:67], v[144:147], v[112:115], v[208:211]\n\t"
      "s_waitcnt lgkmcnt(13)\n\t"
      "v_exp_f32 v80, v80\n\t"
      "ds_read_b128 v[144:147], v228 offset:40960\n\t"
      "ds_read_b64_tr_b16 v[206:207], v237 offset:22016\n\t"
      "v_mfma_f32_16x16x32_bf16 v[0:3], v[176:179], v[96:99], v[0:3]\n\t"
      "ds_read_b64_tr_b16 v[176:177], v236 offset:24576\n\t"
      "v_mfma_f32_16x16x32_bf16 v[72:75], v[160:163], v[112:115], v[208:211]\n\t"
      "v_exp_f32 v81, v81\n\t"
      "ds_read_b128 v[160:163], v228 offset:45056\n\t"
      "ds_read_b64_tr_b16 v[178:179], v236 offset:28672\n\t"
      "v_mfma_f32_16x16x32_bf16 v[8:11], v[180:183], v[96:99], v[8:11]\n\t"
      "ds_read_b64_tr_b16 v[180:181], v237 offset:24576\n\t"
      "v_cvt_pk_bf16_f32 v104, v80, v81\n\t"
      "v_mfma_f32_16x16x32_bf16 v[64:67], v[148:151], v[116:119], v[64:67]\n\t"
      "v_exp_f32 v82, v82\n\t"
      "ds_read_b128 v[148:151], v229 offset:40960\n\t"
      "ds_read_b64_tr_b16 v[182:183], v237 offset:28672\n\t"
      "v_mfma_f32_16x16x32_bf16 v[16:19], v[184:187], v[96:99], v[16:19]\n\t"
      "ds_read_b64_tr_b16 v[184:185], v236 offset:25088\n\t"
      "v_mfma_f32_16x16x32_bf16 v[72:75], v[164:167], v[116:119], v[72:75]\n\t"
      "s_waitcnt lgkmcnt(13)\n\t"
      "v_exp_f32 v83, v83\n\t"
      "ds_read_b128 v[164:167], v229 offset:45056\n\t"
      "ds_read_b64_tr_b16 v[186:187], v236 offset:29184\n\t"
      "v_mfma_f32_16x16x32_bf16 v[24:27], v[188:191], v[96:99], v[24:27]\n\t"
      "ds_read_b64_tr_b16 v[188:189], v237 offset:25088\n\t"
      "v_cvt_pk_bf16_f32 v105, v82, v83\n\t"
      "v_mfma_f32_16x16x32_bf16 v[64:67], v[152:155], v[120:123], v[64:67]\n\t"
      "v_exp_f32 v88, v88\n\t"
      "ds_read_b128 v[152:155], v230 offset:40960\n\t"
      "ds_read_b64_tr_b16 v[190:191], v237 offset:29184\n\t"
      "v_mfma_f32_16x16x32_bf16 v[32:35], v[192:195], v[96:99], v[32:35]\n\t"
      "ds_read_b64_tr_b16 v[192:193], v236 offset:25600\n\t"
      "v_mfma_f32_16x16x32_bf16 v[72:75], v[168:171], v[120:123], v[72:75]\n\t"
      "v_exp_f32 v89, v89\n\t"
      "ds_read_b128 v[168:171], v230 offset:45056\n\t"
      "ds_read_b64_tr_b16 v[194:195], v236 offset:29696\n\t"
      "v_mfma_f32_16x16x32_bf16 v[40:43], v[196:199], v[96:99], v[40:43]\n\t"
      "ds_read_b64_tr_b16 v[196:197], v237 offset:25600\n\t"
      "v_cvt_pk_bf16_f32 v106, v88, v89\n\t"
      "v_mfma_f32_16x16x32_bf16 v[64:67], v[156:159], v[124:127], v[64:67]\n\t"
      "s_waitcnt lgkmcnt(15)\n\t"
      "v_exp_f32 v90, v90\n\t"
      "ds_read_b128 v[156:159], v231 offset:40960\n\t"
      "ds_read_b64_tr_b16 v[198:199], v237 offset:29696\n\t"
      "v_mfma_f32_16x16x32_bf16 v[48:51], v[200:203], v[96:99], v[48:51]\n\t"
      "ds_read_b64_tr_b16 v[200:201], v236 offset:26112\n\t"
      "v_mfma_f32_16x16x32_bf16 v[72:75], v[172:175], v[124:127], v[72:75]\n\t"
      "v_exp_f32 v91, v91\n\t"
      "ds_read_b128 v[172:175], v231 offset:45056\n\t"
      "ds_read_b64_tr_b16 v[202:203], v236 offset:30208\n\t"
      "v_mfma_f32_16x16x32_bf16 v[56:59], v[204:207], v[96:99], v[56:59]\n\t"
      "ds_read_b64_tr_b16 v[204:205], v237 offset:26112\n\t"
      "v_cvt_pk_bf16_f32 v107, v90, v91\n\t"
      "s_nop 0\n\t"
      "v_mfma_f32_16x16x32_bf16 v[216:219], v[224:227], v[96:99], v[216:219]\n\t"
      "s_barrier\n\t"
      "v_mfma_f32_16x16x32_bf16 v[80:83], v[144:147], v[112:115], v[208:211]\n\t"
      "s_waitcnt lgkmcnt(13)\n\t"
      "v_exp_f32 v64, v64\n\t"
      "ds_read_b128 v[144:147], v228 offset:49152\n\t"
      "ds_read_b64_tr_b16 v[206:207], v237 offset:30208\n\t"
      "v_mfma_f32_16x16x32_bf16 v[0:3], v[176:179], v[104:107], v[0:3]\n\t"
      "ds_read_b64_tr_b16 v[176:177], v236 offset:32768\n\t"
      "v_mfma_f32_16x16x32_bf16 v[88:91], v[160:163], v[112:115], v[208:211]\n\t"
      "v_exp_f32 v65, v65\n\t"
      "ds_read_b128 v[160:163], v228 offset:53248\n\t"
      "ds_read_b64_tr_b16 v[178:179], v236 offset:36864\n\t"
      "v_mfma_f32_16x16x32_bf16 v[8:11], v[180:183], v[104:107], v[8:11]\n\t"
      "ds_read_b64_tr_b16 v[180:181], v237 offset:32768\n\t"
      "v_cvt_pk_bf16_f32 v96, v64, v65\n\t"
      "v_mfma_f32_16x16x32_bf16 v[80:83], v[148:151], v[116:119], v[80:83]\n\t"
      "v_exp_f32 v66, v66\n\t"
      "ds_read_b128 v[148:151], v229 offset:49152\n\t"
      "ds_read_b64_tr_b16 v[182:183], v237 offset:36864\n\t"
      "v_mfma_f32_16x16x32_bf16 v[16:19], v[184:187], v[104:107], v[16:19]\n\t"
      "ds_read_b64_tr_b16 v[184:185], v236 offset:33280\n\t"
      "v_mfma_f32_16x16x32_bf16 v[88:91], v[164:167], v[116:119], v[88:91]\n\t"
      "s_waitcnt lgkmcnt(13)\n\t"
      "v_exp_f32 v67, v67\n\t"
      "ds_read_b128 v[164:167], v229 offset:53248\n\t"
      "ds_read_b64_tr_b16 v[186:187], v236 offset:37376\n\t"
      "v_mfma_f32_16x16x32_bf16 v[24:27], v[188:191], v[104:107], v[24:27]\n\t"
      "ds_read_b64_tr_b16 v[188:189], v237 offset:33280\n\t"
      "v_cvt_pk_bf16_f32 v97, v66, v67\n\t"
      "v_mfma_f32_16x16x32_bf16 v[80:83], v[152:155], v[120:123], v[80:83]\n\t"
      "v_exp_f32 v72, v72\n\t"
      "ds_read_b128 v[152:155], v230 offset:49152\n\t"
      "ds_read_b64_tr_b16 v[190:191], v237 offset:37376\n\t"
      "v_mfma_f32_16x16x32_bf16 v[32:35], v[192:195], v[104:107], v[32:35]\n\t"
      "ds_read_b64_tr_b16 v[192:193], v236 offset:33792\n\t"
      "v_mfma_f32_16x16x32_bf16 v[88:91], v[168:171], v[120:123], v[88:91]\n\t"
      "v_exp_f32 v73, v73\n\t"
      "ds_read_b128 v[168:171], v230 offset:53248\n\t"
      "ds_read_b64_tr_b16 v[194:195], v236 offset:37888\n\t"
      "v_mfma_f32_16x16x32_bf16 v[40:43], v[196:199], v[104:107], v[40:43]\n\t"
      "ds_read_b64_tr_b16 v[196:197], v237 offset:33792\n\t"
      "v_cvt_pk_bf16_f32 v98, v72, v73\n\t"
      "v_mfma_f32_16x16x32_bf16 v[80:83], v[156:159], v[124:127], v[80:83]\n\t"
      "s_waitcnt lgkmcnt(15)\n\t"
      "v_exp_f32 v74, v74\n\t"
      "ds_read_b128 v[156:159], v231 offset:49152\n\t"
      "ds_read_b64_tr_b16 v[198:199], v237 offset:37888\n\t"
      "v_mfma_f32_16x16x32_bf16 v[48:51], v[200:203], v[104:107], v[48:51]\n\t"
      "ds_read_b64_tr_b16 v[200:201], v236 offset:34304\n\t"
      "v_mfma_f32_16x16x32_bf16 v[88:91], v[172:175], v[124:127], v[88:91]\n\t"
      "v_exp_f32 v75, v75\n\t"
      "ds_read_b128 v[172:175], v231 offset:53248\n\t"
      "ds_read_b64_tr_b16 v[202:203], v236 offset:38400\n\t"
      "v_mfma_f32_16x16x32_bf16 v[56:59], v[204:207], v[104:107], v[56:59]\n\t"
      "ds_read_b64_tr_b16 v[204:205], v237 offset:34304\n\t"
      "v_cvt_pk_bf16_f32 v99, v74, v75\n\t"
      "s_nop 0\n\t"
      "v_mfma_f32_16x16x32_bf16 v[216:219], v[224:227], v[104:107], v[216:219]\n\t"
      "v_mfma_f32_16x16x32_bf16 v[64:67], v[144:147], v[112:115], v[208:211]\n\t"
      "s_waitcnt lgkmcnt(13)\n\t"
      "v_exp_f32 v80, v80\n\t"
      "ds_read_b128 v[144:147], v228 offset:57344\n\t"
      "ds_read_b64_tr_b16 v[206:207], v237 offset:38400\n\t"
      "v_mfma_f32_16x16x32_bf16 v[0:3], v[176:179], v[96:99], v[0:3]\n\t"
      "ds_read_b64_tr_b16 v[176:177], v236 offset:40960\n\t"
      "v_mfma_f32_16x16x32_bf16 v[72:75], v[160:163], v[112:115], v[208:211]\n\t"
      "v_exp_f32 v81, v81\n\t"
      "ds_read_b128 v[160:163], v228 offset:61440\n\t"
      "ds_read_b64_tr_b16 v[178:179], v236 offset:45056\n\t"
      "v_mfma_f32_16x16x32_bf16 v[8:11], v[180:183], v[96:99], v[8:11]\n\t"
      "ds_read_b64_tr_b16 v[180:181], v237 offset:40960\n\t"
      "v_cvt_pk_bf16_f32 v104, v80, v81\n\t"
      "v_mfma_f32_16x16x32_bf16 v[64:67], v[148:151], v[116:119], v[64:67]\n\t"
      "v_exp_f32 v82, v82\n\t"
      "ds_read_b128 v[148:151], v229 offset:57344\n\t"
      "ds_read_b64_tr_b16 v[182:183], v237 offset:45056\n\t"
      "v_mfma_f32_16x16x32_bf16 v[16:19], v[184:187], v[96:99], v[16:19]\n\t"
      "ds_read_b64_tr_b16 v[184:185], v236 offset:41472\n\t"
      "v_mfma_f32_16x16x32_bf16 v[72:75], v[164:167], v[116:119], v[72:75]\n\t"
      "s_waitcnt lgkmcnt(13)\n\t"
      "v_exp_f32 v83, v83\n\t"
      "ds_read_b128 v[164:167], v229 offset:61440\n\t"
      "ds_read_b64_tr_b16 v[186:187], v236 offset:45568\n\t"
      "v_mfma_f32_16x16x32_bf16 v[24:27], v[188:191], v[96:99], v[24:27]\n\t"
      "ds_read_b64_tr_b16 v[188:189], v237 offset:41472\n\t"
      "v_cvt_pk_bf16_f32 v105, v82, v83\n\t"
      "v_mfma_f32_16x16x32_bf16 v[64:67], v[152:155], v[120:123], v[64:67]\n\t"
      "v_exp_f32 v88, v88\n\t"
      "ds_read_b128 v[152:155], v230 offset:57344\n\t"
      "ds_read_b64_tr_b16 v[190:191], v237 offset:45568\n\t"
      "v_mfma_f32_16x16x32_bf16 v[32:35], v[192:195], v[96:99], v[32:35]\n\t"
      "ds_read_b64_tr_b16 v[192:193], v236 offset:41984\n\t"
      "v_mfma_f32_16x16x32_bf16 v[72:75], v[168:171], v[120:123], v[72:75]\n\t"
      "v_exp_f32 v89, v89\n\t"
      "ds_read_b128 v[168:171], v230 offset:61440\n\t"
      "ds_read_b64_tr_b16 v[194:195], v236 offset:46080\n\t"
      "v_mfma_f32_16x16x32_bf16 v[40:43], v[196:199], v[96:99], v[40:43]\n\t"
      "ds_read_b64_tr_b16 v[196:197], v237 offset:41984\n\t"
      "v_cvt_pk_bf16_f32 v106, v88, v89\n\t"
      "v_mfma_f32_16x16x32_bf16 v[64:67], v[156:159], v[124:127], v[64:67]\n\t"
      "s_waitcnt lgkmcnt(15)\n\t"
      "v_exp_f32 v90, v90\n\t"
      "ds_read_b128 v[156:159], v231 offset:57344\n\t"
      "ds_read_b64_tr_b16 v[198:199], v237 offset:46080\n\t"
      "v_mfma_f32_16x16x32_bf16 v[48:51], v[200:203], v[96:99], v[48:51]\n\t"
      "ds_read_b64_tr_b16 v[200:201], v236 offset:42496\n\t"
      "v_mfma_f32_16x16x32_bf16 v[72:75], v[172:175], v[124:127], v[72:75]\n\t"
      "v_exp_f32 v91, v91\n\t"
      "ds_read_b128 v[172:175], v231 offset:61440\n\t"
      "ds_read_b64_tr_b16 v[202:203], v236 offset:46592\n\t"
      "v_mfma_f32_16x16x32_bf16 v[56:59], v[204:207], v[96:99], v[56:59]\n\t"
      "ds_read_b64_tr_b16 v[204:205], v237 offset:42496\n\t"
      "v_cvt_pk_bf16_f32 v107, v90, v91\n\t"
      "s_nop 0\n\t"
      "v_mfma_f32_16x16x32_bf16 v[216:219], v[224:227], v[96:99], v[216:219]\n\t"
      "s_barrier\n\t"
      "v_mfma_f32_16x16x32_bf16 v[80:83], v[144:147], v[112:115], v[208:211]\n\t"
      "s_waitcnt lgkmcnt(13)\n\t"
      "v_exp_f32 v64, v64\n\t"
      "ds_read_b128 v[144:147], v232 offset:0\n\t"
      "ds_read_b64_tr_b16 v[206:207], v237 offset:46592\n\t"
      "v_mfma_f32_16x16x32_bf16 v[0:3], v[176:179], v[104:107], v[0:3]\n\t"
      "ds_read_b64_tr_b16 v[176:177], v236 offset:49152\n\t"
      "v_mfma_f32_16x16x32_bf16 v[88:91], v[160:163], v[112:115], v[208:211]\n\t"
      "v_exp_f32 v65, v65\n\t"
      "ds_read_b128 v[160:163], v232 offset:4096\n\t"
      "ds_read_b64_tr_b16 v[178:179], v236 offset:53248\n\t"
      "v_mfma_f32_16x16x32_bf16 v[8:11], v[180:183], v[104:107], v[8:11]\n\t"
      "ds_read_b64_tr_b16 v[180:181], v237 offset:49152\n\t"
      "v_cvt_pk_bf16_f32 v96, v64, v65\n\t"
      "v_mfma_f32_16x16x32_bf16 v[80:83], v[148:151], v[116:119], v[80:83]\n\t"
      "v_exp_f32 v66, v66\n\t"
      "ds_read_b128 v[148:151], v233 offset:0\n\t"
      "ds_read_b64_tr_b16 v[182:183], v237 offset:53248\n\t"
      "v_mfma_f32_16x16x32_bf16 v[16:19], v[184:187], v[104:107], v[16:19]\n\t"
      "ds_read_b64_tr_b16 v[184:185], v236 offset:49664\n\t"
      "v_mfma_f32_16x16x32_bf16 v[88:91], v[164:167], v[116:119], v[88:91]\n\t"
      "s_waitcnt lgkmcnt(13)\n\t"
      "v_exp_f32 v67, v67\n\t"
      "ds_read_b128 v[164:167], v233 offset:4096\n\t"
      "ds_read_b64_tr_b16 v[186:187], v236 offset:53760\n\t"
      "v_mfma_f32_16x16x32_bf16 v[24:27], v[188:191], v[104:107], v[24:27]\n\t"
      "ds_read_b64_tr_b16 v[188:189], v237 offset:49664\n\t"
      "v_cvt_pk_bf16_f32 v97, v66, v67\n\t"
      "v_mfma_f32_16x16x32_bf16 v[80:83], v[152:155], v[120:123], v[80:83]\n\t"
      "v_exp_f32 v72, v72\n\t"
      "ds_read_b128 v[152:155], v234 offset:0\n\t"
      "ds_read_b64_tr_b16 v[190:191], v237 offset:53760\n\t"
      "v_mfma_f32_16x16x32_bf16 v[32:35], v[192:195], v[104:107], v[32:35]\n\t"
      "ds_read_b64_tr_b16 v[192:193], v236 offset:50176\n\t"
      "v_mfma_f32_16x16x32_bf16 v[88:91], v[168:171], v[120:123], v[88:91]\n\t"
      "v_exp_f32 v73, v73\n\t"
      "ds_read_b128 v[168:171], v234 offset:4096\n\t"
      "ds_read_b64_tr_b16 v[194:195], v236 offset:54272\n\t"
      "v_mfma_f32_16x16x32_bf16 v[40:43], v[196:199], v[104:107], v[40:43]\n\t"
      "ds_read_b64_tr_b16 v[196:197], v237 offset:50176\n\t"
      "v_cvt_pk_bf16_f32 v98, v72, v73\n\t"
      "v_mfma_f32_16x16x32_bf16 v[80:83], v[156:159], v[124:127], v[80:83]\n\t"
      "s_waitcnt lgkmcnt(15)\n\t"
      "v_exp_f32 v74, v74\n\t"
      "ds_read_b128 v[156:159], v235 offset:0\n\t"
      "ds_read_b64_tr_b16 v[198:199], v237 offset:54272\n\t"
      "v_mfma_f32_16x16x32_bf16 v[48:51], v[200:203], v[104:107], v[48:51]\n\t"
      "ds_read_b64_tr_b16 v[200:201], v236 offset:50688\n\t"
      "v_mfma_f32_16x16x32_bf16 v[88:91], v[172:175], v[124:127], v[88:91]\n\t"
      "v_exp_f32 v75, v75\n\t"
      "ds_read_b128 v[172:175], v235 offset:4096\n\t"
      "ds_read_b64_tr_b16 v[202:203], v236 offset:54784\n\t"
      "v_mfma_f32_16x16x32_bf16 v[56:59], v[204:207], v[104:107], v[56:59]\n\t"
      "ds_read_b64_tr_b16 v[204:205], v237 offset:50688\n\t"
      "v_cvt_pk_bf16_f32 v99, v74, v75\n\t"
      "s_nop 0\n\t"
      "v_mfma_f32_16x16x32_bf16 v[216:219], v[224:227], v[104:107], v[216:219]\n\t"
      "v_mfma_f32_16x16x32_bf16 v[64:67], v[144:147], v[112:115], v[208:211]\n\t"
      "s_waitcnt lgkmcnt(13)\n\t"
      "v_exp_f32 v80, v80\n\t"
      "ds_read_b128 v[144:147], v232 offset:8192\n\t"
      "ds_read_b64_tr_b16 v[206:207], v237 offset:54784\n\t"
      "v_mfma_f32_16x16x32_bf16 v[0:3], v[176:179], v[96:99], v[0:3]\n\t"
      "ds_read_b64_tr_b16 v[176:177], v236 offset:57344\n\t"
      "v_mfma_f32_16x16x32_bf16 v[72:75], v[160:163], v[112:115], v[208:211]\n\t"
      "v_exp_f32 v81, v81\n\t"
      "ds_read_b128 v[160:163], v232 offset:12288\n\t"
      "ds_read_b64_tr_b16 v[178:179], v236 offset:61440\n\t"
      "v_mfma_f32_16x16x32_bf16 v[8:11], v[180:183], v[96:99], v[8:11]\n\t"
      "ds_read_b64_tr_b16 v[180:181], v237 offset:57344\n\t"
      "v_cvt_pk_bf16_f32 v104, v80, v81\n\t"
      "v_mfma_f32_16x16x32_bf16 v[64:67], v[148:151], v[116:119], v[64:67]\n\t"
      "v_exp_f32 v82, v82\n\t"
      "ds_read_b128 v[148:151], v233 offset:8192\n\t"
      "ds_read_b64_tr_b16 v[182:183], v237 offset:61440\n\t"
      "v_mfma_f32_16x16x32_bf16 v[16:19], v[184:187], v[96:99], v[16:19]\n\t"
      "ds_read_b64_tr_b16 v[184:185], v236 offset:57856\n\t"
      "v_mfma_f32_16x16x32_bf16 v[72:75], v[164:167], v[116:119], v[72:75]\n\t"
      "s_waitcnt lgkmcnt(13)\n\t"
      "v_exp_f32 v83, v83\n\t"
      "ds_read_b128 v[164:167], v233 offset:12288\n\t"
      "ds_read_b64_tr_b16 v[186:187], v236 offset:61952\n\t"
      "v_mfma_f32_16x16x32_bf16 v[24:27], v[188:191], v[96:99], v[24:27]\n\t"
      "ds_read_b64_tr_b16 v[188:189], v237 offset:57856\n\t"
      "v_cvt_pk_bf16_f32 v105, v82, v83\n\t"
      "v_mfma_f32_16x16x32_bf16 v[64:67], v[152:155], v[120:123], v[64:67]\n\t"
      "v_exp_f32 v88, v88\n\t"
      "ds_read_b128 v[152:155], v234 offset:8192\n\t"
      "ds_read_b64_tr_b16 v[190:191], v237 offset:61952\n\t"
      "v_mfma_f32_16x16x32_bf16 v[32:35], v[192:195], v[96:99], v[32:35]\n\t"
      "ds_read_b64_tr_b16 v[192:193], v236 offset:58368\n\t"
      "v_mfma_f32_16x16x32_bf16 v[72:75], v[168:171], v[120:123], v[72:75]\n\t"
      "v_exp_f32 v89, v89\n\t"
      "ds_read_b128 v[168:171], v234 offset:12288\n\t"
      "ds_read_b64_tr_b16 v[194:195], v236 offset:62464\n\t"
      "v_mfma_f32_16x16x32_bf16 v[40:43], v[196:199], v[96:99], v[40:43]\n\t"
      "ds_read_b64_tr_b16 v[196:197], v237 offset:58368\n\t"
      "v_cvt_pk_bf16_f32 v106, v88, v89\n\t"
      "v_mfma_f32_16x16x32_bf16 v[64:67], v[156:159], v[124:127], v[64:67]\n\t"
      "s_waitcnt lgkmcnt(15)\n\t"
      "v_exp_f32 v90, v90\n\t"
      "ds_read_b128 v[156:159], v235 offset:8192\n\t"
      "ds_read_b64_tr_b16 v[198:199], v237 offset:62464\n\t"
      "v_mfma_f32_16x16x32_bf16 v[48:51], v[200:203], v[96:99], v[48:51]\n\t"
      "ds_read_b64_tr_b16 v[200:201], v236 offset:58880\n\t"
      "v_mfma_f32_16x16x32_bf16 v[72:75], v[172:175], v[124:127], v[72:75]\n\t"
      "v_exp_f32 v91, v91\n\t"
      "ds_read_b128 v[172:175], v235 offset:12288\n\t"
      "ds_read_b64_tr_b16 v[202:203], v236 offset:62976\n\t"
      "v_mfma_f32_16x16x32_bf16 v[56:59], v[204:207], v[96:99], v[56:59]\n\t"
      "ds_read_b64_tr_b16 v[204:205], v237 offset:58880\n\t"
      "v_cvt_pk_bf16_f32 v107, v90, v91\n\t"
      "s_nop 0\n\t"
      "v_mfma_f32_16x16x32_bf16 v[216:219], v[224:227], v[96:99], v[216:219]\n\t"
      "s_barrier\n\t"
      "v_mfma_f32_16x16x32_bf16 v[80:83], v[144:147], v[112:115], v[208:211]\n\t"
      "s_waitcnt lgkmcnt(13)\n\t"
      "v_exp_f32 v64, v64\n\t"
      "ds_read_b128 v[144:147], v228 offset:0\n\t"
      "ds_read_b64_tr_b16 v[206:207], v237 offset:62976\n\t"
      "v_mfma_f32_16x16x32_bf16 v[0:3], v[176:179], v[104:107], v[0:3]\n\t"
      "ds_read_b64_tr_b16 v[176:177], v238 offset:0\n\t"
      "v_mfma_f32_16x16x32_bf16 v[88:91], v[160:163], v[112:115], v[208:211]\n\t"
      "v_exp_f32 v65, v65\n\t"
      "ds_read_b128 v[160:163], v228 offset:4096\n\t"
      "ds_read_b64_tr_b16 v[178:179], v238 offset:4096\n\t"
      "v_mfma_f32_16x16x32_bf16 v[8:11], v[180:183], v[104:107], v[8:11]\n\t"
      "ds_read_b64_tr_b16 v[180:181], v239 offset:0\n\t"
      "v_cvt_pk_bf16_f32 v96, v64, v65\n\t"
      "v_mfma_f32_16x16x32_bf16 v[80:83], v[148:151], v[116:119], v[80:83]\n\t"
      "v_exp_f32 v66, v66\n\t"
      "ds_read_b128 v[148:151], v229 offset:0\n\t"
      "ds_read_b64_tr_b16 v[182:183], v239 offset:4096\n\t"
      "v_mfma_f32_16x16x32_bf16 v[16:19], v[184:187], v[104:107], v[16:19]\n\t"
      "ds_read_b64_tr_b16 v[184:185], v238 offset:512\n\t"
      "v_mfma_f32_16x16x32_bf16 v[88:91], v[164:167], v[116:119], v[88:91]\n\t"
      "s_waitcnt lgkmcnt(13)\n\t"
      "v_exp_f32 v67, v67\n\t"
      "ds_read_b128 v[164:167], v229 offset:4096\n\t"
      "ds_read_b64_tr_b16 v[186:187], v238 offset:4608\n\t"
      "v_mfma_f32_16x16x32_bf16 v[24:27], v[188:191], v[104:107], v[24:27]\n\t"
      "ds_read_b64_tr_b16 v[188:189], v239 offset:512\n\t"
      "v_cvt_pk_bf16_f32 v97, v66, v67\n\t"
      "v_mfma_f32_16x16x32_bf16 v[80:83], v[152:155], v[120:123], v[80:83]\n\t"
      "v_exp_f32 v72, v72\n\t"
      "ds_read_b128 v[152:155], v230 offset:0\n\t"
      "ds_read_b64_tr_b16 v[190:191], v239 offset:4608\n\t"
      "v_mfma_f32_16x16x32_bf16 v[32:35], v[192:195], v[104:107], v[32:35]\n\t"
      "ds_read_b64_tr_b16 v[192:193], v238 offset:1024\n\t"
      "v_mfma_f32_16x16x32_bf16 v[88:91], v[168:171], v[120:123], v[88:91]\n\t"
      "v_exp_f32 v73, v73\n\t"
      "ds_read_b128 v[168:171], v230 offset:4096\n\t"
      "ds_read_b64_tr_b16 v[194:195], v238 offset:5120\n\t"
      "v_mfma_f32_16x16x32_bf16 v[40:43], v[196:199], v[104:107], v[40:43]\n\t"
      "ds_read_b64_tr_b16 v[196:197], v239 offset:1024\n\t"
      "v_cvt_pk_bf16_f32 v98, v72, v73\n\t"
      "v_mfma_f32_16x16x32_bf16 v[80:83], v[156:159], v[124:127], v[80:83]\n\t"
      "s_waitcnt lgkmcnt(15)\n\t"
      "v_exp_f32 v74, v74\n\t"
      "ds_read_b128 v[156:159], v231 offset:0\n\t"
      "ds_read_b64_tr_b16 v[198:199], v239 offset:5120\n\t"
      "v_mfma_f32_16x16x32_bf16 v[48:51], v[200:203], v[104:107], v[48:51]\n\t"
      "ds_read_b64_tr_b16 v[200:201], v238 offset:1536\n\t"
      "v_mfma_f32_16x16x32_bf16 v[88:91], v[172:175], v[124:127], v[88:91]\n\t"
      "v_exp_f32 v75, v75\n\t"
      "ds_read_b128 v[172:175], v231 offset:4096\n\t"
      "ds_read_b64_tr_b16 v[202:203], v238 offset:5632\n\t"
      "v_mfma_f32_16x16x32_bf16 v[56:59], v[204:207], v[104:107], v[56:59]\n\t"
      "ds_read_b64_tr_b16 v[204:205], v239 offset:1536\n\t"
      "v_cvt_pk_bf16_f32 v99, v74, v75\n\t"
      "s_nop 0\n\t"
      "v_mfma_f32_16x16x32_bf16 v[216:219], v[224:227], v[104:107], v[216:219]\n\t"
      "v_mfma_f32_16x16x32_bf16 v[64:67], v[144:147], v[112:115], v[208:211]\n\t"
      "s_waitcnt lgkmcnt(13)\n\t"
      "v_exp_f32 v80, v80\n\t"
      "ds_read_b128 v[144:147], v228 offset:8192\n\t"
      "ds_read_b64_tr_b16 v[206:207], v239 offset:5632\n\t"
      "v_mfma_f32_16x16x32_bf16 v[0:3], v[176:179], v[96:99], v[0:3]\n\t"
      "ds_read_b64_tr_b16 v[176:177], v238 offset:8192\n\t"
      "v_mfma_f32_16x16x32_bf16 v[72:75], v[160:163], v[112:115], v[208:211]\n\t"
      "v_exp_f32 v81, v81\n\t"
      "ds_read_b128 v[160:163], v228 offset:12288\n\t"
      "ds_read_b64_tr_b16 v[178:179], v238 offset:12288\n\t"
      "v_mfma_f32_16x16x32_bf16 v[8:11], v[180:183], v[96:99], v[8:11]\n\t"
      "ds_read_b64_tr_b16 v[180:181], v239 offset:8192\n\t"
      "v_cvt_pk_bf16_f32 v104, v80, v81\n\t"
      "v_mfma_f32_16x16x32_bf16 v[64:67], v[148:151], v[116:119], v[64:67]\n\t"
      "v_exp_f32 v82, v82\n\t"
      "ds_read_b128 v[148:151], v229 offset:8192\n\t"
      "ds_read_b64_tr_b16 v[182:183], v239 offset:12288\n\t"
      "v_mfma_f32_16x16x32_bf16 v[16:19], v[184:187], v[96:99], v[16:19]\n\t"
      "ds_read_b64_tr_b16 v[184:185], v238 offset:8704\n\t"
      "v_mfma_f32_16x16x32_bf16 v[72:75], v[164:167], v[116:119], v[72:75]\n\t"
      "s_waitcnt lgkmcnt(13)\n\t"
      "v_exp_f32 v83, v83\n\t"
      "ds_read_b128 v[164:167], v229 offset:12288\n\t"
      "ds_read_b64_tr_b16 v[186:187], v238 offset:12800\n\t"
      "v_mfma_f32_16x16x32_bf16 v[24:27], v[188:191], v[96:99], v[24:27]\n\t"
      "ds_read_b64_tr_b16 v[188:189], v239 offset:8704\n\t"
      "v_cvt_pk_bf16_f32 v105, v82, v83\n\t"
      "v_mfma_f32_16x16x32_bf16 v[64:67], v[152:155], v[120:123], v[64:67]\n\t"
      "v_exp_f32 v88, v88\n\t"
      "ds_read_b128 v[152:155], v230 offset:8192\n\t"
      "ds_read_b64_tr_b16 v[190:191], v239 offset:12800\n\t"
      "v_mfma_f32_16x16x32_bf16 v[32:35], v[192:195], v[96:99], v[32:35]\n\t"
      "ds_read_b64_tr_b16 v[192:193], v238 offset:9216\n\t"
      "v_mfma_f32_16x16x32_bf16 v[72:75], v[168:171], v[120:123], v[72:75]\n\t"
      "v_exp_f32 v89, v89\n\t"
      "ds_read_b128 v[168:171], v230 offset:12288\n\t"
      "ds_read_b64_tr_b16 v[194:195], v238 offset:13312\n\t"
      "v_mfma_f32_16x16x32_bf16 v[40:43], v[196:199], v[96:99], v[40:43]\n\t"
      "ds_read_b64_tr_b16 v[196:197], v239 offset:9216\n\t"
      "v_cvt_pk_bf16_f32 v106, v88, v89\n\t"
      "v_mfma_f32_16x16x32_bf16 v[64:67], v[156:159], v[124:127], v[64:67]\n\t"
      "s_waitcnt lgkmcnt(15)\n\t"
      "v_exp_f32 v90, v90\n\t"
      "ds_read_b128 v[156:159], v231 offset:8192\n\t"
      "ds_read_b64_tr_b16 v[198:199], v239 offset:13312\n\t"
      "v_mfma_f32_16x16x32_bf16 v[48:51], v[200:203], v[96:99], v[48:51]\n\t"
      "ds_read_b64_tr_b16 v[200:201], v238 offset:9728\n\t"
      "v_mfma_f32_16x16x32_bf16 v[72:75], v[172:175], v[124:127], v[72:75]\n\t"
      "v_exp_f32 v91, v91\n\t"
      "ds_read_b128 v[172:175], v231 offset:12288\n\t"
      "ds_read_b64_tr_b16 v[202:203], v238 offset:13824\n\t"
      "v_mfma_f32_16x16x32_bf16 v[56:59], v[204:207], v[96:99], v[56:59]\n\t"
      "ds_read_b64_tr_b16 v[204:205], v239 offset:9728\n\t"
      "v_cvt_pk_bf16_f32 v107, v90, v91\n\t"
      "s_nop 0\n\t"
      "v_mfma_f32_16x16x32_bf16 v[216:219], v[224:227], v[96:99], v[216:219]\n\t"
      "s_barrier\n\t"
      "v_mfma_f32_16x16x32_bf16 v[80:83], v[144:147], v[112:115], v[208:211]\n\t"
      "s_waitcnt lgkmcnt(13)\n\t"
      "v_exp_f32 v64, v64\n\t"
      "ds_read_b64_tr_b16 v[206:207], v239 offset:13824\n\t"
      "v_mfma_f32_16x16x32_bf16 v[0:3], v[176:179], v[104:107], v[0:3]\n\t"
      "ds_read_b64_tr_b16 v[176:177], v236 offset:0\n\t"
      "v_mfma_f32_16x16x32_bf16 v[88:91], v[160:163], v[112:115], v[208:211]\n\t"
      "v_exp_f32 v65, v65\n\t"
      "ds_read_b64_tr_b16 v[178:179], v236 offset:4096\n\t"
      "v_mfma_f32_16x16x32_bf16 v[8:11], v[180:183], v[104:107], v[8:11]\n\t"
      "ds_read_b64_tr_b16 v[180:181], v237 offset:0\n\t"
      "v_cvt_pk_bf16_f32 v96, v64, v65\n\t"
      "v_mfma_f32_16x16x32_bf16 v[80:83], v[148:151], v[116:119], v[80:83]\n\t"
      "v_exp_f32 v66, v66\n\t"
      "ds_read_b64_tr_b16 v[182:183], v237 offset:4096\n\t"
      "v_mfma_f32_16x16x32_bf16 v[16:19], v[184:187], v[104:107], v[16:19]\n\t"
      "ds_read_b64_tr_b16 v[184:185], v236 offset:512\n\t"
      "v_mfma_f32_16x16x32_bf16 v[88:91], v[164:167], v[116:119], v[88:91]\n\t"
      "s_waitcnt lgkmcnt(10)\n\t"
      "v_exp_f32 v67, v67\n\t"
      "ds_read_b64_tr_b16 v[186:187], v236 offset:4608\n\t"
      "v_mfma_f32_16x16x32_bf16 v[24:27], v[188:191], v[104:107], v[24:27]\n\t"
      "ds_read_b64_tr_b16 v[188:189], v237 offset:512\n\t"
      "v_cvt_pk_bf16_f32 v97, v66, v67\n\t"
      "v_mfma_f32_16x16x32_bf16 v[80:83], v[152:155], v[120:123], v[80:83]\n\t"
      "v_exp_f32 v72, v72\n\t"
      "ds_read_b64_tr_b16 v[190:191], v237 offset:4608\n\t"
      "v_mfma_f32_16x16x32_bf16 v[32:35], v[192:195], v[104:107], v[32:35]\n\t"
      "ds_read_b64_tr_b16 v[192:193], v236 offset:1024\n\t"
      "v_mfma_f32_16x16x32_bf16 v[88:91], v[168:171], v[120:123], v[88:91]\n\t"
      "v_exp_f32 v73, v73\n\t"
      "ds_read_b64_tr_b16 v[194:195], v236 offset:5120\n\t"
      "v_mfma_f32_16x16x32_bf16 v[40:43], v[196:199], v[104:107], v[40:43]\n\t"
      "ds_read_b64_tr_b16 v[196:197], v237 offset:1024\n\t"
      "v_cvt_pk_bf16_f32 v98, v72, v73\n\t"
      "v_mfma_f32_16x16x32_bf16 v[80:83], v[156:159], v[124:127], v[80:83]\n\t"
      "s_waitcnt lgkmcnt(11)\n\t"
      "v_exp_f32 v74, v74\n\t"
      "ds_read_b64_tr_b16 v[198:199], v237 offset:5120\n\t"
      "v_mfma_f32_16x16x32_bf16 v[48:51], v[200:203], v[104:107], v[48:51]\n\t"
      "ds_read_b64_tr_b16 v[200:201], v236 offset:1536\n\t"
      "v_mfma_f32_16x16x32_bf16 v[88:91], v[172:175], v[124:127], v[88:91]\n\t"
      "v_exp_f32 v75, v75\n\t"
      "ds_read_b64_tr_b16 v[202:203], v236 offset:5632\n\t"
      "v_mfma_f32_16x16x32_bf16 v[56:59], v[204:207], v[104:107], v[56:59]\n\t"
      "ds_read_b64_tr_b16 v[204:205], v237 offset:1536\n\t"
      "v_cvt_pk_bf16_f32 v99, v74, v75\n\t"
      "s_nop 0\n\t"
      "v_mfma_f32_16x16x32_bf16 v[216:219], v[224:227], v[104:107], v[216:219]\n\t"
      "s_waitcnt lgkmcnt(9)\n\t"
      "v_exp_f32 v80, v80\n\t"
      "ds_read_b64_tr_b16 v[206:207], v237 offset:5632\n\t"
      "v_mfma_f32_16x16x32_bf16 v[0:3], v[176:179], v[96:99], v[0:3]\n\t"
      "ds_read_b64_tr_b16 v[176:177], v236 offset:8192\n\t"
      "v_exp_f32 v81, v81\n\t"
      "ds_read_b64_tr_b16 v[178:179], v236 offset:12288\n\t"
      "v_mfma_f32_16x16x32_bf16 v[8:11], v[180:183], v[96:99], v[8:11]\n\t"
      "ds_read_b64_tr_b16 v[180:181], v237 offset:8192\n\t"
      "v_cvt_pk_bf16_f32 v104, v80, v81\n\t"
      "v_exp_f32 v82, v82\n\t"
      "ds_read_b64_tr_b16 v[182:183], v237 offset:12288\n\t"
      "v_mfma_f32_16x16x32_bf16 v[16:19], v[184:187], v[96:99], v[16:19]\n\t"
      "ds_read_b64_tr_b16 v[184:185], v236 offset:8704\n\t"
      "s_waitcnt lgkmcnt(9)\n\t"
      "v_exp_f32 v83, v83\n\t"
      "ds_read_b64_tr_b16 v[186:187], v236 offset:12800\n\t"
      "v_mfma_f32_16x16x32_bf16 v[24:27], v[188:191], v[96:99], v[24:27]\n\t"
      "ds_read_b64_tr_b16 v[188:189], v237 offset:8704\n\t"
      "v_cvt_pk_bf16_f32 v105, v82, v83\n\t"
      "v_exp_f32 v88, v88\n\t"
      "ds_read_b64_tr_b16 v[190:191], v237 offset:12800\n\t"
      "v_mfma_f32_16x16x32_bf16 v[32:35], v[192:195], v[96:99], v[32:35]\n\t"
      "ds_read_b64_tr_b16 v[192:193], v236 offset:9216\n\t"
      "v_exp_f32 v89, v89\n\t"
      "ds_read_b64_tr_b16 v[194:195], v236 offset:13312\n\t"
      "v_mfma_f32_16x16x32_bf16 v[40:43], v[196:199], v[96:99], v[40:43]\n\t"
      "ds_read_b64_tr_b16 v[196:197], v237 offset:9216\n\t"
      "v_cvt_pk_bf16_f32 v106, v88, v89\n\t"
      "s_waitcnt lgkmcnt(11)\n\t"
      "v_exp_f32 v90, v90\n\t"
      "ds_read_b64_tr_b16 v[198:199], v237 offset:13312\n\t"
      "v_mfma_f32_16x16x32_bf16 v[48:51], v[200:203], v[96:99], v[48:51]\n\t"
      "ds_read_b64_tr_b16 v[200:201], v236 offset:9728\n\t"
      "v_exp_f32 v91, v91\n\t"
      "ds_read_b64_tr_b16 v[202:203], v236 offset:13824\n\t"
      "v_mfma_f32_16x16x32_bf16 v[56:59], v[204:207], v[96:99], v[56:59]\n\t"
      "ds_read_b64_tr_b16 v[204:205], v237 offset:9728\n\t"
      "v_cvt_pk_bf16_f32 v107, v90, v91\n\t"
      "s_nop 0\n\t"
      "v_mfma_f32_16x16x32_bf16 v[216:219], v[224:227], v[96:99], v[216:219]\n\t"
      "s_barrier\n\t"
      "ds_read_b64_tr_b16 v[206:207], v237 offset:13824\n\t"
      "s_mov_b32 s43, 0x5d800000\n\t"
      "v_mfma_f32_16x16x32_bf16 v[216:219], v[224:227], v[104:107], v[216:219]\n\t"
      "s_waitcnt lgkmcnt(10)\n\t"
      "v_mfma_f32_16x16x32_bf16 v[0:3], v[176:179], v[104:107], v[0:3]\n\t"
      "v_mfma_f32_16x16x32_bf16 v[8:11], v[180:183], v[104:107], v[8:11]\n\t"
      "v_mfma_f32_16x16x32_bf16 v[16:19], v[184:187], v[104:107], v[16:19]\n\t"
      "s_waitcnt lgkmcnt(4)\n\t"
      "v_mfma_f32_16x16x32_bf16 v[24:27], v[188:191], v[104:107], v[24:27]\n\t"
      "v_mfma_f32_16x16x32_bf16 v[32:35], v[192:195], v[104:107], v[32:35]\n\t"
      "v_mfma_f32_16x16x32_bf16 v[40:43], v[196:199], v[104:107], v[40:43]\n\t"
      "s_waitcnt lgkmcnt(0)\n\t"
      "v_mfma_f32_16x16x32_bf16 v[48:51], v[200:203], v[104:107], v[48:51]\n\t"
      "v_mfma_f32_16x16x32_bf16 v[56:59], v[204:207], v[104:107], v[56:59]\n\t"
      "v_mov_b32 v240, v216\n\t"
      "s_nop 1\n\t"
      "v_permlane32_swap_b32 v216, v240\n\t"
      "s_nop 0\n\t"
      "v_mov_b32 v240, v216\n\t"
      "s_nop 1\n\t"
      "v_permlane16_swap_b32 v216, v240\n\t"
      "s_nop 0\n\t"
      "v_cmp_nge_f32 vcc, s43, v216\n\t"
      "v_rcp_f32 v216, v216\n\t"
      "s_nop 0\n\t"
      "s_nop 4\n\t"
      "v_mul_f32 v0, v0, v216\n\t"
      "v_mul_f32 v1, v1, v216\n\t"
      "v_mul_f32 v2, v2, v216\n\t"
      "v_mul_f32 v3, v3, v216\n\t"
      "v_mul_f32 v8, v8, v216\n\t"
      "v_mul_f32 v9, v9, v216\n\t"
      "v_mul_f32 v10, v10, v216\n\t"
      "v_mul_f32 v11, v11, v216\n\t"
      "v_cvt_pk_bf16_f32 v0, v0, v1\n\t"
      "v_cvt_pk_bf16_f32 v1, v2, v3\n\t"
      "v_mul_f32 v16, v16, v216\n\t"
      "v_mul_f32 v17, v17, v216\n\t"
      "v_mul_f32 v18, v18, v216\n\t"
      "v_mul_f32 v19, v19, v216\n\t"
      "v_cvt_pk_bf16_f32 v8, v8, v9\n\t"
      "v_cvt_pk_bf16_f32 v9, v10, v11\n\t"
      "ds_write_b64 v248, v[0:1] offset:0\n\t"
      "v_mul_f32 v24, v24, v216\n\t"
      "v_mul_f32 v25, v25, v216\n\t"
      "v_mul_f32 v26, v26, v216\n\t"
      "v_mul_f32 v27, v27, v216\n\t"
      "v_cvt_pk_bf16_f32 v16, v16, v17\n\t"
      "v_cvt_pk_bf16_f32 v17, v18, v19\n\t"
      "ds_write_b64 v248, v[8:9] offset:544\n\t"
      "v_mul_f32 v32, v32, v216\n\t"
      "v_mul_f32 v33, v33, v216\n\t"
      "v_mul_f32 v34, v34, v216\n\t"
      "v_mul_f32 v35, v35, v216\n\t"
      "v_cvt_pk_bf16_f32 v24, v24, v25\n\t"
      "v_cvt_pk_bf16_f32 v25, v26, v27\n\t"
      "ds_write_b64 v248, v[16:17] offset:1088\n\t"
      "v_mul_f32 v40, v40, v216\n\t"
      "v_mul_f32 v41, v41, v216\n\t"
      "v_mul_f32 v42, v42, v216\n\t"
      "v_mul_f32 v43, v43, v216\n\t"
      "v_cvt_pk_bf16_f32 v32, v32, v33\n\t"
      "v_cvt_pk_bf16_f32 v33, v34, v35\n\t"
      "ds_write_b64 v248, v[24:25] offset:1632\n\t"
      "v_mul_f32 v48, v48, v216\n\t"
      "v_mul_f32 v49, v49, v216\n\t"
      "v_mul_f32 v50, v50, v216\n\t"
      "v_mul_f32 v51, v51, v216\n\t"
      "v_cvt_pk_bf16_f32 v40, v40, v41\n\t"
      "v_cvt_pk_bf16_f32 v41, v42, v43\n\t"
      "ds_write_b64 v248, v[32:33] offset:2176\n\t"
      "v_mul_f32 v56, v56, v216\n\t"
      "v_mul_f32 v57, v57, v216\n\t"
      "v_mul_f32 v58, v58, v216\n\t"
      "v_mul_f32 v59, v59, v216\n\t"
      "v_cvt_pk_bf16_f32 v48, v48, v49\n\t"
      "v_cvt_pk_bf16_f32 v49, v50, v51\n\t"
      "ds_write_b64 v248, v[40:41] offset:2720\n\t"
      "v_cvt_pk_bf16_f32 v56, v56, v57\n\t"
      "v_cvt_pk_bf16_f32 v57, v58, v59\n\t"
      "ds_write_b64 v248, v[48:49] offset:3264\n\t"
      "ds_write_b64 v248, v[56:57] offset:3808\n\t"
      "s_waitcnt lgkmcnt(0)\n\t"
      "ds_read_b128 v[176:179], v249 offset:0\n\t"
      "ds_read_b128 v[180:183], v250 offset:128\n\t"
      "ds_read_b128 v[184:187], v249 offset:256\n\t"
      "ds_read_b128 v[188:191], v250 offset:384\n\t"
      "s_waitcnt lgkmcnt(3)\n\t"
      "s_add_u32 s42, s40, 0\n\t"
      "buffer_store_dwordx4 v[176:179], v251, %[so], s42 offen sc1 nt\n\t"
      "s_waitcnt lgkmcnt(2)\n\t"
      "s_add_u32 s42, s40, 1024\n\t"
      "buffer_store_dwordx4 v[180:183], v251, %[so], s42 offen sc1 nt\n\t"
      "s_waitcnt lgkmcnt(1)\n\t"
      "s_add_u32 s42, s40, 2048\n\t"
      "buffer_store_dwordx4 v[184:187], v251, %[so], s42 offen sc1 nt\n\t"
      "s_waitcnt lgkmcnt(0)\n\t"
      "s_add_u32 s42, s40, 3072\n\t"
      "buffer_store_dwordx4 v[188:191], v251, %[so], s42 offen sc1 nt\n\t"
      "s_or_b32 %[flag], vcc_lo, vcc_hi\n\t"
      "Lend%=:\n\t"
      : [flag] "=&s"(flag)
      : [w] "s"(w), [lds] "s"(ldsb), [sq] "s"(sq), [sk] "s"(sk), [sv] "s"(sv), [so] "s"(so), [idx] "s"(idx)
      : "memory", "vcc", "scc", "s40", "s41", "s42", "s43", "s44", "s45", "s46", "s47", "s48", "s49", "s50", "s51", "s52", "s53", "s54", "s55", "s56", "s57", "s58", "s59", "v0", "v1", "v2", "v3", "v4", "v5", "v6", "v7", "v8", "v9", "v10", "v11", "v12", "v13", "v14", "v15", "v16", "v17", "v18", "v19", "v20", "v21", "v22", "v23", "v24", "v25", "v26", "v27", "v28", "v29", "v30", "v31", "v32", "v33", "v34", "v35", "v36", "v37", "v38", "v39", "v40", "v41", "v42", "v43", "v44", "v45", "v46", "v47", "v48", "v49", "v50", "v51", "v52", "v53", "v54", "v55", "v56", "v57", "v58", "v59", "v60", "v61", "v62", "v63", "v64", "v65", "v66", "v67", "v68", "v69", "v70", "v71", "v72", "v73", "v74", "v75", "v76", "v77", "v78", "v79", "v80", "v81", "v82", "v83", "v84", "v85", "v86", "v87", "v88", "v89", "v90", "v91", "v92", "v93", "v94", "v95", "v96", "v97", "v98", "v99", "v100", "v101", "v102", "v103", "v104", "v105", "v106", "v107", "v108", "v109", "v110", "v111", "v112", "v113", "v114", "v115", "v116", "v117", "v118", "v119", "v120", "v121", "v122", "v123", "v124", "v125", "v126", "v127", "v128", "v129", "v130", "v131", "v132", "v133", "v134", "v135", "v136", "v137", "v138", "v139", "v140", "v141", "v142", "v143", "v144", "v145", "v146", "v147", "v148", "v149", "v150", "v151", "v152", "v153", "v154", "v155", "v156", "v157", "v158", "v159", "v160", "v161", "v162", "v163", "v164", "v165", "v166", "v167", "v168", "v169", "v170", "v171", "v172", "v173", "v174", "v175", "v176", "v177", "v178", "v179", "v180", "v181", "v182", "v183", "v184", "v185", "v186", "v187", "v188", "v189", "v190", "v191", "v192", "v193", "v194", "v195", "v196", "v197", "v198", "v199", "v200", "v201", "v202", "v203", "v204", "v205", "v206", "v207", "v208", "v209", "v210", "v211", "v212", "v213", "v214", "v215", "v216", "v217", "v218", "v219", "v220", "v221", "v222", "v223", "v224", "v225", "v226", "v227", "v228", "v229", "v230", "v231", "v232", "v233", "v234", "v235", "v236", "v237", "v238", "v239", "v240", "v241", "v242", "v243", "v244", "v245", "v246", "v247", "v248", "v249", "v250", "v251");
  if (flag) {
    asm volatile("s_waitcnt vmcnt(0)" ::: "memory");
    const int lane = __builtin_amdgcn_mbcnt_hi(~0u, __builtin_amdgcn_mbcnt_lo(~0u, 0u));
    const int nb = w == 3 ? 1 : 2;
    for (int qb = 0; qb < nb; ++qb) {
      const int r0 = 32 * (int)w + 16 * qb;
      exact_rows16(Qh + r0 * D, Kh, Vh, Oh + r0 * D, lane);
    }
  }
}

extern "C" void kernel_launch(void* const* d_in, const int* in_sizes, int n_in, void* d_out, int out_size, void* d_ws, size_t ws_size, hipStream_t stream) {
  gqa_attn_fwd<<<256, 512, 0, stream>>>((const unsigned short*)d_in[0], (const unsigned short*)d_in[1], (const unsigned short*)d_in[2], (unsigned short*)d_out);
}
```
